```python
import torch

def _softmax(x, axis=-1):
    return torch.softmax(x, dim=axis)
def _gelu(x):
    return torch.nn.functional.gelu(x, approximate="tanh")
def _silu(x):
    return torch.nn.functional.silu(x)
def _sigmoid(x):
    return torch.sigmoid(x)
def _layernorm(x, axis=-1, eps=1e-5):
    m = x.mean(dim=axis, keepdim=True)
    v = x.var(dim=axis, keepdim=True, unbiased=False)
    return (x - m) / torch.sqrt(v + eps)
def _scatter_add(tgt, idx, src, axis=0):
    return tgt.clone().index_add_(axis, idx, src)
def _segment_sum(src, seg, n):
    out = torch.zeros((n,) + src.shape[1:], dtype=src.dtype)
    return out.index_add_(0, seg, src)
def _pad(x, pw):
    flat = tuple(v for pair in reversed(pw) for v in pair)
    return torch.nn.functional.pad(x, flat)

kernel_name = 'sequential_preLN__linear_attn_block__glu_gated_glu__prompt'

def setup_inputs():

    _w = (lambda a: (lambda t: t.to(torch.bfloat16) if t.is_floating_point() else t)(torch.as_tensor(a)))
    def _A_setup():
        B, S, D = 1, 2048, 576
        return {
            "x": _w((lambda *s: torch.randn(*s, dtype=torch.bfloat16) * 0.05)(B, S, D)),
            "wqkv": _w((lambda *s: torch.randn(*s, dtype=torch.bfloat16) * 0.05)(D, 3 * D)),
            "wo": _w((lambda *s: torch.randn(*s, dtype=torch.bfloat16) * 0.05)(D, D)),
        }
    _A_in = _A_setup()

    def _B_setup():
        B, S, D, F = 1, 2048, 576, 4608
        return {
            "x": _w((lambda *s: torch.randn(*s, dtype=torch.bfloat16) * 0.05)(B, S, D)),
            "w_gate": _w((lambda *s: torch.randn(*s, dtype=torch.bfloat16) * 0.05)(D, F)),
            "w_up": _w((lambda *s: torch.randn(*s, dtype=torch.bfloat16) * 0.05)(D, F)),
            "w_down": _w((lambda *s: torch.randn(*s, dtype=torch.bfloat16) * 0.05)(F, D)),
        }
    _B_in = _B_setup()

    return {
        "A_x": _A_in["x"],
        "A_wqkv": _A_in["wqkv"],
        "A_wo": _A_in["wo"],
        "B_x": _B_in["x"],
        "B_w_gate": _B_in["w_gate"],
        "B_w_up": _B_in["w_up"],
        "B_w_down": _B_in["w_down"],
    }

def reference(A_x, A_wqkv, A_wo, B_x, B_w_gate, B_w_up, B_w_down):
    def _A_ref(x, wqkv, wo):
        B, S, D = x.shape
        H = 32; Dh = D // H
        qkv = (lambda x, *d: x.permute(*d))((x @ wqkv).reshape(B, S, 3, H, Dh), 2, 0, 3, 1, 4)
        q, k, v = qkv[0], qkv[1], qkv[2]
        phi = lambda t: torch.where(t > 0, t + 1, torch.exp(t)) + 1e-6
        pq, pk = phi(q), phi(k)
        kv = torch.einsum("bhsd,bhse->bhde", pk, v)
        z = torch.einsum("bhsd->bhd", pk)
        num = torch.einsum("bhqd,bhde->bhqe", pq, kv)
        den = torch.einsum("bhqd,bhd->bhq", pq, z)[..., None] + 1e-6
        o = num / den
        return (lambda x, *d: x.permute(*d))(o, 0, 2, 1, 3).reshape(B, S, D) @ wo

    def _B_ref(x, w_gate, w_up, w_down):
        g = torch.einsum("bsd,df->bsf", x, w_gate)
        u = torch.einsum("bsd,df->bsf", x, w_up)
        h = _sigmoid(g) * u
        return torch.einsum("bsf,fd->bsd", h, w_down)

    x = A_x
    h = x + _A_ref(_layernorm(x, axis=-1), A_wqkv, A_wo)
    h2 = h + _B_ref(_layernorm(h, axis=-1), B_w_gate, B_w_up, B_w_down)
    return h2
```

```cpp
#include <hip/hip_runtime.h>
#include <hip/hip_bf16.h>
typedef __hip_bfloat16 bf16;
constexpr int S=2048, D=576, F=4608, H=32, DH=18;
__device__ __forceinline__ float b2f(bf16 x){return __bfloat162float(x);}
__global__ void ln_k(const bf16* x, float* y){
  int r=blockIdx.x; int t=threadIdx.x; float s=0,ss=0; float v[9];
  for(int i=0;i<9;i++){v[i]=b2f(x[r*D+t+64*i]); s+=v[i];}
  for(int o=32;o;o>>=1) s+=__shfl_xor(s,o);
  float m=s/D; for(int i=0;i<9;i++){float d=v[i]-m; ss+=d*d;}
  for(int o=32;o;o>>=1) ss+=__shfl_xor(ss,o);
  float rs=rsqrtf(ss/D+1e-5f);
  for(int i=0;i<9;i++) y[r*D+t+64*i]=(v[i]-m)*rs;
}
__global__ void ln_kf(const float* x, float* y){
  int r=blockIdx.x; int t=threadIdx.x; float s=0,ss=0; float v[9];
  for(int i=0;i<9;i++){v[i]=x[r*D+t+64*i]; s+=v[i];}
  for(int o=32;o;o>>=1) s+=__shfl_xor(s,o);
  float m=s/D; for(int i=0;i<9;i++){float d=v[i]-m; ss+=d*d;}
  for(int o=32;o;o>>=1) ss+=__shfl_xor(ss,o);
  float rs=rsqrtf(ss/D+1e-5f);
  for(int i=0;i<9;i++) y[r*D+t+64*i]=(v[i]-m)*rs;
}
__global__ void gemm_k(const float* A,const bf16* B,float* C,int M,int N,int K){
  __shared__ float As[16][17]; __shared__ float Bs[16][17];
  int tx=threadIdx.x, ty=threadIdx.y; int row=blockIdx.y*16+ty, col=blockIdx.x*16+tx; float acc=0;
  for(int k0=0;k0<K;k0+=16){ As[ty][tx]=A[row*K+k0+tx]; Bs[ty][tx]=b2f(B[(k0+ty)*N+col]); __syncthreads();
    for(int k=0;k<16;k++) acc+=As[ty][k]*Bs[k][tx]; __syncthreads(); }
  C[row*N+col]=acc;
}
__device__ __forceinline__ float phi(float t){return (t>0? t+1.f: __expf(t))+1e-6f;}
__global__ void kv_k(const float* qkv,float* kv){
  int h=blockIdx.x; int t=threadIdx.x; if(t>=18*19) return; int d=t/19,e=t%19; float acc=0;
  for(int s=0;s<S;s++){ float pk=phi(qkv[s*3*D+D+h*DH+d]); float v= e<18? qkv[s*3*D+2*D+h*DH+e]:1.f; acc+=pk*v; }
  kv[h*342+t]=acc;
}
__global__ void o_k(const float* qkv,const float* kv,float* o){
  int s=blockIdx.x; int c=threadIdx.x; if(c>=D) return; int h=c/DH,e=c%DH; float num=0,den=0;
  for(int d=0;d<DH;d++){ float pq=phi(qkv[s*3*D+h*DH+d]); num+=pq*kv[h*342+d*19+e]; den+=pq*kv[h*342+d*19+18]; }
  o[s*D+c]=num/(den+1e-6f);
}
__global__ void addx_k(const bf16* x,const float* a,float* h){int i=blockIdx.x*256+threadIdx.x; h[i]=b2f(x[i])+a[i];}
__global__ void glu_k(const float* g,const float* u,float* hh){int i=blockIdx.x*256+threadIdx.x; hh[i]=u[i]/(1.f+__expf(-g[i]));}
__global__ void fin_k(const float* h,const float* a,bf16* out){int i=blockIdx.x*256+threadIdx.x; out[i]=__float2bfloat16(h[i]+a[i]);}
extern "C" void kernel_launch(void* const* d_in, const int* in_sizes, int n_in, void* d_out, int out_size, void* d_ws, size_t ws_size, hipStream_t stream){
  const bf16* x=(const bf16*)d_in[0]; const bf16* wqkv=(const bf16*)d_in[1]; const bf16* wo=(const bf16*)d_in[2];
  const bf16* wg=(const bf16*)d_in[4]; const bf16* wu=(const bf16*)d_in[5]; const bf16* wd=(const bf16*)d_in[6];
  float* ws=(float*)d_ws; float* xn=ws; float* qkv=xn+S*D; float* kv=qkv+S*3*D; float* o=kv+16384; float* a=o+S*D; float* h=a+S*D; float* hn=h+S*D;
  float* g=hn+S*D; float* u=g+S*F; float* hh=u+S*F; float* f=hh+S*F;
  ln_k<<<S,64,0,stream>>>(x,xn);
  gemm_k<<<dim3(3*D/16,S/16),dim3(16,16),0,stream>>>(xn,wqkv,qkv,S,3*D,D);
  kv_k<<<H,384,0,stream>>>(qkv,kv);
  o_k<<<S,576,0,stream>>>(qkv,kv,o);
  gemm_k<<<dim3(D/16,S/16),dim3(16,16),0,stream>>>(o,wo,a,S,D,D);
  addx_k<<<S*D/256,256,0,stream>>>(x,a,h);
  ln_kf<<<S,64,0,stream>>>(h,hn);
  gemm_k<<<dim3(F/16,S/16),dim3(16,16),0,stream>>>(hn,wg,g,S,F,D);
  gemm_k<<<dim3(F/16,S/16),dim3(16,16),0,stream>>>(hn,wu,u,S,F,D);
  glu_k<<<S*F/256,256,0,stream>>>(g,u,hh);
  gemm_k<<<dim3(D/16,S/16),dim3(16,16),0,stream>>>(hh,wd,f,S,D,F);
  fin_k<<<S*D/256,256,0,stream>>>(h,f,(bf16*)d_out);
}
```

```cpp
#include <hip/hip_runtime.h>
#include <hip/hip_bf16.h>
#include <stdint.h>
#include <type_traits>

typedef __attribute__((ext_vector_type(8))) short bf16x8;
typedef __attribute__((ext_vector_type(4))) short s16x4;
typedef __attribute__((ext_vector_type(16))) float f32x16;
typedef __attribute__((ext_vector_type(4))) float f32x4;
typedef __attribute__((ext_vector_type(2))) float f32x2;
typedef __attribute__((ext_vector_type(2))) __bf16 bf16x2;
typedef __attribute__((ext_vector_type(4))) unsigned u32x4;
typedef __attribute__((ext_vector_type(2))) unsigned u32x2;
typedef __attribute__((address_space(3))) char lds_char;
typedef __attribute__((address_space(3))) s16x4 lds_s16x4;
typedef __attribute__((address_space(3))) u32x4 lds_u32x4;
typedef __attribute__((address_space(3))) u32x2 lds_u32x2;
typedef __attribute__((address_space(3))) f32x4 lds_f32x4;
typedef __attribute__((address_space(3))) float lds_float;

#define DI __device__ __forceinline__
#define MFMA32(a, b, c) __builtin_amdgcn_mfma_f32_32x32x16_bf16((a), (b), (c), 0, 0, 0)
#define MFMA16(a, b, c) __builtin_amdgcn_mfma_f32_16x16x32_bf16((a), (b), (c), 0, 0, 0)
#define WAITV(n) asm volatile("s_waitcnt vmcnt(" #n ")" ::: "memory")
#define WAITL0() asm volatile("s_waitcnt lgkmcnt(0)" ::: "memory")
#define BAR() asm volatile("s_barrier" ::: "memory")
#define SB0() __builtin_amdgcn_sched_barrier(0)

constexpr int S_ = 2048, D_ = 576, F_ = 4608;
constexpr float LN_EPS = 1e-5f;

constexpr size_t FLB = (size_t)S_ * D_ * 2;
constexpr size_t WQT_HP = 36 * 3456;
constexpr size_t WOT_CB = 36 * 4672;
constexpr size_t WF_CH = 972 * 1024;
constexpr size_t OFF_XN = 0;
constexpr size_t OFF_PQ = OFF_XN + FLB;
constexpr size_t OFF_HC = OFF_PQ + FLB;
constexpr size_t OFF_MEANX = OFF_HC + FLB;
constexpr size_t OFF_MEANH = OFF_MEANX + 8192;
constexpr size_t OFF_SSQ = OFF_MEANH + 8192;
constexpr size_t OFF_WBAR = OFF_SSQ + 20 * 8192;
constexpr size_t OFF_KV = OFF_WBAR + 4096;
constexpr size_t OFF_WQT = OFF_KV + 53248;
constexpr size_t OFF_WOT = OFF_WQT + 16 * WQT_HP + 16384;
constexpr size_t OFF_WF = OFF_WOT + 4 * WOT_CB + 4096;
constexpr size_t OFF_PART = OFF_WF + 16 * WF_CH + 65536;
constexpr size_t WS_END = OFF_PART + 16 * FLB;

#define ST_DECL
#define ST()
#define ST_OUT(k, slot)
DI unsigned pk2(float a, float b) {
  f32x2 v = {a, b};
  bf16x2 r = __builtin_convertvector(v, bf16x2);
  return __builtin_bit_cast(unsigned, r);
}
DI float bflo(unsigned u) { return __uint_as_float(u << 16); }
DI float bfhi(unsigned u) { return __uint_as_float(u & 0xffff0000u); }
DI float phi(float t) { return (fmaxf(t, 0.f) + 1e-6f) + __builtin_amdgcn_exp2f(fminf(t * 1.4426950408889634f, 0.f)); }
DI bf16x8 as_frag(u32x4 v) { return __builtin_bit_cast(bf16x8, v); }

DI void dma16(const void* gsrc, unsigned lds_dst) {
  unsigned keep;
  asm volatile("s_mov_b32 %0, m0\n\ts_mov_b32 m0, %2\n\ts_nop 0\n\tglobal_load_lds_dwordx4 %1, off\n\ts_mov_b32 m0, %0"
               : "=&s"(keep) : "v"(gsrc), "s"(lds_dst) : "memory");
}
DI void dma16x3(const void* sbase, unsigned voff, unsigned lds_dst) {
  unsigned keep;
  asm volatile("s_mov_b32 %0, m0\n\ts_mov_b32 m0, %3\n\ts_nop 0\n\t"
               "global_load_lds_dwordx4 %1, %2\n\t"
               "global_load_lds_dwordx4 %1, %2 offset:1024\n\t"
               "global_load_lds_dwordx4 %1, %2 offset:2048\n\t"
               "s_mov_b32 m0, %0"
               : "=&s"(keep) : "v"(voff), "s"(sbase), "s"(lds_dst) : "memory");
}
DI void dma16x1(const void* sbase, unsigned voff, unsigned lds_dst) {
  unsigned keep;
  asm volatile("s_mov_b32 %0, m0\n\ts_mov_b32 m0, %3\n\ts_nop 0\n\t"
               "global_load_lds_dwordx4 %1, %2\n\t"
               "s_mov_b32 m0, %0"
               : "=&s"(keep) : "v"(voff), "s"(sbase), "s"(lds_dst) : "memory");
}
DI u32x4 tr_frag(lds_char* p0, lds_char* p1) {
  s16x4 lo = __builtin_amdgcn_ds_read_tr16_b64_v4i16((lds_s16x4*)p0);
  s16x4 hi = __builtin_amdgcn_ds_read_tr16_b64_v4i16((lds_s16x4*)p1);
  u32x2 a = __builtin_bit_cast(u32x2, lo), b = __builtin_bit_cast(u32x2, hi);
  u32x4 r = {a[0], a[1], b[0], b[1]};
  return r;
}

#define ST_AUX 16
#define WF_AUX 16
#define WS_RSRC(P) __builtin_amdgcn_make_buffer_rsrc((P).ws, 0, (int)WS_END, 0x00020000)
#define WS_ST16(P, off, v, aux) __builtin_amdgcn_raw_buffer_store_b128((v), WS_RSRC(P), (int)(off), 0, (aux))
#define WS_ST8(P, off, v, aux) __builtin_amdgcn_raw_buffer_store_b64((v), WS_RSRC(P), (int)(off), 0, (aux))
#define WS_ST4(P, off, v, aux) __builtin_amdgcn_raw_buffer_store_b32((v), WS_RSRC(P), (int)(off), 0, (aux))
struct Ptrs {
  const char* x; const char* wqkv; const char* wo; const char* wg; const char* wu; const char* wd;
  char* ws; char* out;
};

constexpr int ITEM_LDS = 9216;
constexpr int SUB_PER_XCD = 486;

DI void sub_load(const Ptrs& P, int x, int j, int lane, unsigned lds_base) {
  const int rl = lane >> 3, cp = lane & 7;
  if (j < 324) {
    const int mat = j / 162, r = j % 162, S = r / 9, cb = r % 9;
    const char* base = (mat ? P.wu : P.wg) + (size_t)(32 * S) * (F_ * 2) + (576 * x + 64 * cb) * 2;
#pragma unroll
    for (int it = 0; it < 4; ++it) {
      const int X = ((rl >> 1) & 1) | ((it & 1) << 1);
      dma16x1(base + (size_t)(8 * it) * (F_ * 2), rl * (F_ * 2) + (((cp >> 1) ^ X) * 32) + (cp & 1) * 16, lds_base + it * 1024);
    }
  } else {
    const int jj = j - 324, Ug = jj / 9, db = jj % 9;
    const char* base = P.wd + (size_t)(576 * x + 32 * Ug) * (D_ * 2) + db * 128;
#pragma unroll
    for (int it = 0; it < 4; ++it)
      dma16x1(base + (8 * it) * (D_ * 2), rl * (D_ * 2) + ((cp ^ (((rl >> 1) & 1) << 2)) * 16), lds_base + it * 1024);
  }
}
DI void sub_store(const Ptrs& P, int x, int j, int lane, lds_char* img) {
  const int g = lane >> 4, q = (lane >> 2) & 3, p = lane & 3;
  const int wf = (int)OFF_WF + lane * 16;
  if (j < 324) {
    const int mat = j / 162, r = j % 162, S = r / 9, cb = r % 9;
    const int X = ((q >> 1) & 1) | ((g & 1) << 1);
#pragma unroll
    for (int u4 = 0; u4 < 4; ++u4) {
      lds_char* a = img + (8 * g + q) * 128 + ((u4 ^ X) * 32) + 8 * p;
      u32x4 v = tr_frag(a, a + 512);
      const int ug = 4 * cb + u4, c = 2 * x + (ug >= 18 ? 1 : 0), u = ug >= 18 ? ug - 18 : ug;
      const int n = (u >> 1) * 72 + (2 * S + mat) * 2 + (u & 1);
      WS_ST16(P, wf + c * (int)WF_CH + n * 1024, v, WF_AUX);
    }
  } else {
    const int jj = j - 324, Ug = jj / 9, db = jj % 9;
    const int c = 2 * x + (Ug >= 9 ? 1 : 0), U = Ug >= 9 ? Ug - 9 : Ug;
#pragma unroll
    for (int k = 0; k < 4; ++k) {
      const int Q2 = k >> 1, t01 = k & 1;
      lds_char* a = img + (4 * g + q) * 128 + (((4 * Q2 + p) ^ (((q >> 1) & 1) << 2)) * 16) + 8 * t01;
      u32x4 v = tr_frag(a, a + 2048);
      const int n = 648 + db * 36 + (2 * U + t01) * 2 + Q2;
      WS_ST16(P, wf + c * (int)WF_CH + n * 1024, v, WF_AUX);
    }
  }
}

constexpr int K0_ITEMS = 696;

__global__ __launch_bounds__(256) void k0_prep(Ptrs P) {
  extern __shared__ __attribute__((aligned(16))) char smem[];
  const int lane = threadIdx.x & 63, wid = __builtin_amdgcn_readfirstlane(threadIdx.x >> 6);
  const int id = blockIdx.x * 4 + wid;
  lds_char* img = (lds_char*)smem + wid * ITEM_LDS;
  const unsigned img_a = (unsigned)(uintptr_t)img;
  if (id < 256 || (id >= 616 && id < 688)) {
    const bool is_x = id < 256;
    const int r0 = (is_x ? id : id - 616) * 8, row = r0 + (lane >> 3), i = lane & 7;
    const char* src = (is_x ? P.x : P.wo) + (size_t)row * 1152 + i * 16;
    u32x4 v[9];
#pragma unroll
    for (int j = 0; j < 9; ++j) v[j] = *(const u32x4*)(src + j * 128);
    float sum = 0.f;
#pragma unroll
    for (int j = 0; j < 9; ++j)
#pragma unroll
      for (int k = 0; k < 4; ++k) sum += bflo(v[j][k]) + bfhi(v[j][k]);
    sum += __shfl_xor(sum, 1); sum += __shfl_xor(sum, 2); sum += __shfl_xor(sum, 4);
    const float mean = sum * (1.f / D_);
    if (!is_x) {
      const int hd = row / 18, e = row % 18;
      const int ks = e < 16 ? hd : 32 + (hd >> 3), jj = e < 16 ? (e & 3) + 4 * (e >> 3) : (hd & 7), hh = e < 16 ? (e >> 2) & 1 : e - 16;
      const unsigned hi = pk2(mean, 0.f) & 0xffffu;
      const unsigned lo = pk2(mean - bflo(hi), 0.f) & 0xffffu;
      *(unsigned short*)(P.ws + OFF_WOT + (size_t)(i >> 1) * WOT_CB + ks * 4672 + (hh * 146 + 144 + (i & 1)) * 16 + jj * 2) =
          (unsigned short)((i & 1) ? lo : hi);
      return;
    }
    float ss = 0.f;
#pragma unroll
    for (int j = 0; j < 9; ++j)
#pragma unroll
      for (int k = 0; k < 4; ++k) { const float a = bflo(v[j][k]) - mean, b = bfhi(v[j][k]) - mean; ss += a * a + b * b; }
    ss += __shfl_xor(ss, 1); ss += __shfl_xor(ss, 2); ss += __shfl_xor(ss, 4);
    const float rstd = rsqrtf(ss * (1.f / D_) + LN_EPS);
    if (i == 0) WS_ST4(P, OFF_MEANX + row * 4, __float_as_uint(mean), ST_AUX);
    const int dst = (int)OFF_XN + ((row >> 5) * 36 * 64 + (row & 31) + 32 * (i & 1) + (i >> 1) * 64) * 16;
#pragma unroll
    for (int j = 0; j < 9; ++j) {
      u32x4 o;
#pragma unroll
      for (int k = 0; k < 4; ++k) o[k] = pk2((bflo(v[j][k]) - mean) * rstd, (bfhi(v[j][k]) - mean) * rstd);
      WS_ST16(P, dst + j * 4096, o, ST_AUX);
    }
  } else if (id < 544) {
    const int j = id - 256, s = j >> 3, hp0 = (j & 7) * 2;
#pragma unroll
    for (int it = 0; it < 7; ++it) {
      int e = it * 64 + lane; e = e < 432 ? e : 431;
      const int row = e / 27, r = e % 27, seg = r / 9, ch = r % 9;
      dma16(P.wqkv + (size_t)(16 * s + row) * 3456 + seg * 1152 + hp0 * 72 + ch * 16, img_a + it * 1024);
    }
    WAITV(0);
    const int g = lane >> 4, q = (lane >> 2) & 3, p = lane & 3;
#pragma unroll
    for (int it = 0; it < 7; ++it) {
      int Q = 16 * it + 4 * g + p; Q = Q < 108 ? Q : 107;
      const int hl = Q / 54, r = Q % 54, h = r / 27, sg = r % 27;
      int seg, col;
      if (sg < 4) { seg = 0; col = 4 * sg; } else if (sg < 8) { seg = 0; col = 4 * sg + 4; } else if (sg == 8) { seg = 0; col = 16; }
      else if (sg < 18) { seg = 1; col = 4 * (sg - 9); } else { seg = 2; col = 4 * (sg - 18); }
      lds_char* a = img + (8 * h + q) * 432 + seg * 144 + (36 * hl + col) * 2;
      u32x4 v = tr_frag(a, a + 4 * 432);
      const int o = it * 64 + lane;
      if (o < 432) WS_ST16(P, OFF_WQT + (hp0 + o / 216) * (int)WQT_HP + s * 3456 + (o % 216) * 16, v, ST_AUX);
    }
  } else if (id < 616) {
    const int j = id - 544, ks = j >> 1, hf = j & 1;
#pragma unroll
    for (int it = 0; it < 9; ++it) {
      const int e = it * 64 + lane, kk = e / 36, ch = e % 36, h = kk >> 3, jj = kk & 7;
      const int row = ks < 32 ? 18 * ks + (jj & 3) + 8 * (jj >> 2) + 4 * h : 18 * (8 * (ks - 32) + jj) + 16 + h;
      dma16(P.wo + (size_t)row * 1152 + hf * 576 + ch * 16, img_a + it * 1024);
    }
    WAITV(0);
    const int g = lane >> 4, q = (lane >> 2) & 3, p = lane & 3;
#pragma unroll
    for (int it = 0; it < 9; ++it) {
      const int G16 = 4 * it + g;
      const int cl = G16 / 18, r = G16 % 18, h = r / 9, sg = r % 9;
      lds_char* a = img + (8 * h + q) * 576 + (144 * cl + 16 * sg + 4 * p) * 2;
      u32x4 v = tr_frag(a, a + 4 * 576);
      WS_ST16(P, OFF_WOT + (2 * hf + cl) * (int)WOT_CB + ks * 4672 + (h * 146 + 16 * sg + (lane & 15)) * 16, v, ST_AUX);
    }
  } else if (id < 696) {
    const u32x4 z = {0u, 0u, 0u, 0u};
    for (int k = id - 688; k < 50; k += 8) WS_ST16(P, OFF_KV + k * 1024 + lane * 16, z, ST_AUX);
  }
}

constexpr int K1_W = 0;
constexpr int K1_ITEM = 124 * 1024;
constexpr int K1_LDS = K1_ITEM + 4 * ITEM_LDS;
constexpr int KVP = 192;

__global__ __launch_bounds__(512, 2) void k1_qkv(Ptrs P) {
  extern __shared__ __attribute__((aligned(16))) char smem[];
  lds_char* lds = (lds_char*)smem;
  const unsigned lds_a = (unsigned)(uintptr_t)lds;
  const int lane = threadIdx.x & 63, wid = __builtin_amdgcn_readfirstlane(threadIdx.x >> 6);
  const int b = blockIdx.x, hp = ((b & 7) >> 1) * 4 + ((b >> 3) & 3), mb = (b & 1) * 8 + (b >> 5);
  const int r = lane & 31, h = lane >> 5;
  ST_DECL; ST();
  if (wid >= 4) {
    const int w = wid - 4;
    const char* wsrc = P.ws + OFF_WQT + (size_t)hp * WQT_HP + w * 1024;
    const unsigned wdst = lds_a + K1_W + w * 1024;
    const int x = b & 7, j0 = ((b >> 3) * 4 + w) * 2;
#pragma unroll
    for (int i = 0; i < 12; ++i) dma16x1(wsrc + i * 4096, lane * 16, wdst + i * 4096);
    WAITV(6); BAR();
    ST();
#pragma unroll
    for (int i = 12; i < 18; ++i) dma16x1(wsrc + i * 4096, lane * 16, wdst + i * 4096);
    WAITV(6); BAR();
#pragma unroll
    for (int i = 18; i < 24; ++i) dma16x1(wsrc + i * 4096, lane * 16, wdst + i * 4096);
    WAITV(6); BAR();
#pragma unroll
    for (int i = 24; i < 31; ++i) dma16x1(wsrc + i * 4096, lane * 16, wdst + i * 4096);
    WAITV(7); BAR();
    sub_load(P, x, j0, lane, lds_a + K1_ITEM + w * ITEM_LDS);
    sub_load(P, x, j0 + 1, lane, lds_a + K1_ITEM + w * ITEM_LDS + 4096);
    WAITV(8); BAR();
    ST();
    BAR();
    ST();
    WAITV(0);
    ST();
    sub_store(P, x, j0, lane, lds + K1_ITEM + w * ITEM_LDS);
    sub_store(P, x, j0 + 1, lane, lds + K1_ITEM + w * ITEM_LDS + 4096);
    return;
  }
  const int mt = mb * 4 + wid;
  u32x4 xf[36];
  const char* xsrc = P.ws + OFF_XN + (size_t)mt * 36864 + lane * 16;
#pragma unroll
  for (int s = 0; s < 14; ++s) xf[s] = *(const u32x4*)(xsrc + s * 1024);
  f32x16 acc[4];
#pragma unroll
  for (int t = 0; t < 4; ++t)
#pragma unroll
    for (int i = 0; i < 16; ++i) acc[t][i] = 0.f;
  lds_char* wa = lds + K1_W + (h * 108 + r) * 16;
  lds_char* wa3 = lds + K1_W + (h * 108 + 96 + (r < 12 ? r : 11)) * 16;
#pragma unroll
  for (int s = 0; s < 36; ++s) {
    if (s == 0 || s == 7 || s == 14 || s == 21 || s == 28) {
      BAR();
      if (s < 21) {
#pragma unroll
        for (int k = s + 14; k < (s == 14 ? 36 : s + 21); ++k) xf[k] = *(const u32x4*)(xsrc + k * 1024);
      }
    }
    u32x4 a0 = *(lds_u32x4*)(wa + s * 3456), a1 = *(lds_u32x4*)(wa + s * 3456 + 512), a2 = *(lds_u32x4*)(wa + s * 3456 + 1024);
    u32x4 a3 = *(lds_u32x4*)(wa3 + s * 3456);
    acc[0] = MFMA32(as_frag(a0), as_frag(xf[s]), acc[0]);
    acc[1] = MFMA32(as_frag(a1), as_frag(xf[s]), acc[1]);
    acc[2] = MFMA32(as_frag(a2), as_frag(xf[s]), acc[2]);
    acc[3] = MFMA32(as_frag(a3), as_frag(xf[s]), acc[3]);
  }
  ST();
  const int pq = (int)OFF_PQ + mt * 36864;
  lds_char* kvrow = lds + (wid * 32 + r) * KVP;
#pragma unroll
  for (int t = 1; t < 4; ++t)
#pragma unroll
    for (int g = 0; g < 4; ++g) {
      const int qd0 = 8 * t + 2 * g;
      if (qd0 >= 28) continue;
      u32x2 o;
      if (qd0 < 18) {
        o[0] = pk2(phi(acc[t][4 * g]), phi(acc[t][4 * g + 1])); o[1] = pk2(phi(acc[t][4 * g + 2]), phi(acc[t][4 * g + 3]));
      } else {
        o[0] = pk2(acc[t][4 * g], acc[t][4 * g + 1]); o[1] = pk2(acc[t][4 * g + 2], acc[t][4 * g + 3]);
      }
      if (qd0 == 26 && h == 1) { o[0] = 0x3f803f80u; o[1] = 0x3f803f80u; }
      *(lds_u32x2*)(kvrow + (qd0 + h - 8) * 8) = o;
      if (qd0 == 8 && h == 0)
        WS_ST8(P, pq + (32 + (hp >> 2)) * 1024 + (r + 32 * ((hp >> 1) & 1)) * 16 + 8 * (hp & 1), o, ST_AUX);
    }
  WAITL0();
  BAR();
  ST();
  if (wid < 2) {
    const int g = lane >> 4, q = (lane >> 2) & 3, p = lane & 3, op = 4 * (g & 1) + p;
    int vq = (wid ? 22 : 18) + op; vq = (op == 5 || vq > 27) ? 27 : vq;
    if (wid == 0 && op > 5) vq = 27;
    const int kq = (wid ? 13 : 9) + op;
    lds_char* va = lds + (8 * (g >> 1) + q) * KVP + (vq - 8) * 8;
    lds_char* ka = lds + (8 * (g >> 1) + q) * KVP + (kq - 8) * 8;
    f32x16 kv;
#pragma unroll
    for (int i = 0; i < 16; ++i) kv[i] = 0.f;
#pragma unroll
    for (int ks = 0; ks < 8; ++ks) {
      u32x4 a = tr_frag(va + ks * 16 * KVP, va + ks * 16 * KVP + 4 * KVP);
      u32x4 bb = tr_frag(ka + ks * 16 * KVP, ka + ks * 16 * KVP + 4 * KVP);
      kv = MFMA32(as_frag(a), as_frag(bb), kv);
    }
    const int d = r - 2 * wid;
    const int dd = wid ? (d >= 2 ? d - 2 : 16 + d) : d;
    float* dst = (float*)(P.ws + OFF_KV) + (2 * hp + wid) * 400 + dd;
    const bool dok = d >= 0 && d < 18;
#pragma unroll
    for (int i = 0; i < 12; ++i) {
      const int row = (i & 3) + 8 * (i >> 2) + 4 * h;
      const int e = row == 20 ? 18 : row - 2 * wid;
      if (dok && (row == 20 || (e >= 0 && e < 18 && row < 20))) atomicAdd(dst + e * 20, kv[i]);
    }
  }
  ST();
#pragma unroll
  for (int e = 0; e < 2; ++e) {
    u32x4 o;
#pragma unroll
    for (int k = 0; k < 4; ++k) o[k] = pk2(phi(acc[0][8 * e + 2 * k]), phi(acc[0][8 * e + 2 * k + 1]));
    WS_ST16(P, pq + (2 * hp + e) * 1024 + lane * 16, o, ST_AUX);
  }
}

constexpr int K2_O = 0;
constexpr int K2_RED = 36864;
constexpr int K2_MEAN = K2_RED + 32768;
constexpr int K2_ITEM = K2_MEAN + 2048;
constexpr int K2_LDS = K2_ITEM + 4 * ITEM_LDS;

__global__ __launch_bounds__(768, 3) void k2_attn(Ptrs P) {
  extern __shared__ __attribute__((aligned(16))) char smem[];
  lds_char* lds = (lds_char*)smem;
  const unsigned lds_a = (unsigned)(uintptr_t)lds;
  const int lane = threadIdx.x & 63, wid = __builtin_amdgcn_readfirstlane(threadIdx.x >> 6);
  const int b = blockIdx.x, cb = (b >> 3) & 3, mt = (b & 7) * 8 + (b >> 5);
  const int r = lane & 31, h = lane >> 5;
  ST_DECL; ST();
  if (wid >= 8) {
    const int w = wid - 8;
    const int x = b & 7, j0 = 256 + ((b >> 3) * 4 + w) * 2;
    const bool on = j0 < SUB_PER_XCD;
    BAR();
    ST();
    if (on) {
      sub_load(P, x, j0, lane, lds_a + K2_ITEM + w * ITEM_LDS);
      sub_load(P, x, j0 + 1, lane, lds_a + K2_ITEM + w * ITEM_LDS + 4096);
    }
    ST();
    BAR();
    ST();
    WAITV(0);
    ST();
    if (on) {
      sub_store(P, x, j0, lane, lds + K2_ITEM + w * ITEM_LDS);
      sub_store(P, x, j0 + 1, lane, lds + K2_ITEM + w * ITEM_LDS + 4096);
    }
    return;
  }
  const int t = wid & 3, kh = wid >> 2;
  const float* kvb = (const float*)(P.ws + OFF_KV) + (4 * wid) * 400;
  const int e = r < 16 ? r : (r == 16 ? 16 : (r == 20 ? 17 : 18));
  const bool rok = r <= 17 || r == 20 || r == 21;
  f32x4 k0[4], k1[4]; f32x2 k2[4];
#pragma unroll
  for (int i = 0; i < 4; ++i) {
    const float* kp = kvb + i * 400 + e * 20;
    k0[i] = *(const f32x4*)(kp + 4 * h); k1[i] = *(const f32x4*)(kp + 8 + 4 * h); k2[i] = *(const f32x2*)(kp + 16);
  }
  const char* pq = P.ws + OFF_PQ + (size_t)mt * 36864 + lane * 16;
  u32x4 qf[4];
#pragma unroll
  for (int i = 0; i < 4; ++i) qf[i] = *(const u32x4*)(pq + (4 * wid + i) * 1024);
  const u32x4 qr = *(const u32x4*)(pq + (32 + (wid >> 1)) * 1024);
  const char* wo = P.ws + OFF_WOT + (size_t)cb * WOT_CB + h * 2336;
  u32x4 af[18], a4[5];
#pragma unroll
  for (int i = 0; i < 13; ++i) af[i] = *(const u32x4*)(wo + (18 * kh + i) * 4672 + (32 * t + r) * 16);
  const int k4 = wid < 4 ? 5 * wid : 20 + 4 * (wid - 4), n4 = wid < 4 ? 5 : 4;
  float orem[4];
#pragma unroll
  for (int i = 0; i < 4; ++i) {
    u32x4 a1 = {pk2(k0[i][0], k0[i][1]), pk2(k0[i][2], k0[i][3]), pk2(k1[i][0], k1[i][1]), pk2(k1[i][2], k1[i][3])};
    u32x4 a2 = {0u, 0u, 0u, 0u};
    a2[i] = (h == (wid & 1)) ? pk2(k2[i][0], k2[i][1]) : 0u;
    if (!rok) { a1 = (u32x4){0u, 0u, 0u, 0u}; a2[i] = 0u; }
    f32x16 c;
#pragma unroll
    for (int j = 0; j < 16; ++j) c[j] = 0.f;
    c = MFMA32(as_frag(a1), as_frag(qf[i]), c);
    c = MFMA32(as_frag(a2), as_frag(qr), c);
    const float inv = __builtin_amdgcn_rcpf(c[9] + 1e-6f);
    float o[9];
#pragma unroll
    for (int j = 0; j < 9; ++j) o[j] = c[j] * inv;
    u32x4 of = {pk2(o[0], o[1]), pk2(o[2], o[3]), pk2(o[4], o[5]), pk2(o[6], o[7])};
    *(lds_u32x4*)(lds + K2_O + (4 * wid + i) * 1024 + lane * 16) = of;
    orem[i] = o[8];
    if (i == 1) SB0();
  }
  {
    u32x2 o2 = {pk2(orem[0], orem[1]), pk2(orem[2], orem[3])};
    *(lds_u32x2*)(lds + K2_O + (32 + (wid >> 1)) * 1024 + lane * 16 + 8 * (wid & 1)) = o2;
  }
  SB0();
  ST();
#pragma unroll
  for (int i = 13; i < 18; ++i) af[i] = *(const u32x4*)(wo + (18 * kh + i) * 4672 + (32 * t + r) * 16);
#pragma unroll
  for (int i = 0; i < 5; ++i) a4[i] = *(const u32x4*)(wo + (k4 + (i < n4 ? i : 0)) * 4672 + (128 + (r < 17 ? r : 17)) * 16);
  const int row = mt * 32 + r;
  const int s0 = cb * 9 + 2 * (wid < 4 ? wid : 4);
  const u32x4 xv0 = *(const u32x4*)(P.x + (size_t)row * 1152 + s0 * 32 + h * 16);
  const u32x4 xv1 = *(const u32x4*)(P.x + (size_t)row * 1152 + (wid < 4 ? s0 + 1 : s0) * 32 + h * 16);
  float mean = ((const float*)(P.ws + OFF_MEANX))[row];
  WAITL0();
  BAR();
  ST();
  f32x16 acc, acc4;
#pragma unroll
  for (int j = 0; j < 16; ++j) { acc[j] = 0.f; acc4[j] = 0.f; }
  lds_char* ob = lds + K2_O + lane * 16;
#pragma unroll
  for (int i = 0; i < 18; ++i) {
    acc = MFMA32(as_frag(af[i]), as_frag(*(lds_u32x4*)(ob + (18 * kh + i) * 1024)), acc);
    if (i % 6 == 5) SB0();
  }
  if (n4 == 4) a4[4] = (u32x4){0u, 0u, 0u, 0u};
#pragma unroll
  for (int i = 0; i < 5; ++i) acc4 = MFMA32(as_frag(a4[i]), as_frag(*(lds_u32x4*)(ob + (k4 + (i < n4 ? i : 0)) * 1024)), acc4);
  {
    *(lds_float*)(lds + K2_MEAN + (wid * 64 + lane) * 4) = acc4[8] + acc4[9];
    lds_char* p4 = lds + K2_RED + 16384 + wid * 2048 + lane * 16;
    *(lds_f32x4*)(p4) = (f32x4){acc4[0], acc4[1], acc4[2], acc4[3]};
    *(lds_f32x4*)(p4 + 1024) = (f32x4){acc4[4], acc4[5], acc4[6], acc4[7]};
    if (kh == 1) {
      lds_char* pr = lds + K2_RED + t * 4096 + lane * 16;
#pragma unroll
      for (int g = 0; g < 4; ++g) *(lds_f32x4*)(pr + g * 1024) = (f32x4){acc[4 * g], acc[4 * g + 1], acc[4 * g + 2], acc[4 * g + 3]};
    }
  }
  WAITL0();
  BAR();
  if (wid > 4) return;
  ST();
#pragma unroll
  for (int w = 0; w < 8; ++w) mean += *(lds_float*)(lds + K2_MEAN + (w * 64 + r) * 4);
  if (wid < 4) {
    lds_char* pr = lds + K2_RED + t * 4096 + lane * 16;
#pragma unroll
    for (int g = 0; g < 4; ++g) {
      f32x4 v = *(lds_f32x4*)(pr + g * 1024);
#pragma unroll
      for (int j = 0; j < 4; ++j) acc[4 * g + j] += v[j];
    }
  } else {
#pragma unroll
    for (int j = 0; j < 16; ++j) acc[j] = 0.f;
#pragma unroll
    for (int w = 0; w < 8; ++w) {
      lds_char* p4 = lds + K2_RED + 16384 + w * 2048 + lane * 16;
      f32x4 v0 = *(lds_f32x4*)(p4), v1 = *(lds_f32x4*)(p4 + 1024);
#pragma unroll
      for (int j = 0; j < 4; ++j) { acc[j] += v0[j]; acc[4 + j] += v1[j]; }
    }
  }
  float ssq = 0.f;
#pragma unroll
  for (int i = 0; i < 2; ++i) {
    if (wid == 4 && i == 1) break;
    float v[8];
#pragma unroll
    for (int j = 0; j < 4; ++j) {
      auto sw = __builtin_amdgcn_permlane32_swap(__float_as_uint(acc[8 * i + j]), __float_as_uint(acc[8 * i + 4 + j]), false, false);
      v[j] = __uint_as_float(sw[0]); v[4 + j] = __uint_as_float(sw[1]);
    }
    const u32x4 xv = i ? xv1 : xv0;
    u32x4 o;
#pragma unroll
    for (int k = 0; k < 4; ++k) {
      const float a = v[2 * k] + bflo(xv[k]) - mean, c = v[2 * k + 1] + bfhi(xv[k]) - mean;
      ssq += a * a + c * c;
      o[k] = pk2(a, c);
    }
    const int d8 = 2 * (s0 + i) + h;
    WS_ST16(P, OFF_HC + (((row >> 4) * 18 + (d8 >> 2)) * 64 + (row & 15) + 16 * (d8 & 3)) * 16, o, ST_AUX);
  }
  ssq += __shfl_xor(ssq, 32);
  if (h == 0) {
    WS_ST4(P, OFF_SSQ + ((mt * 20 + cb * 5 + wid) * 32 + r) * 4, __float_as_uint(ssq), ST_AUX);
    if (cb == 0 && wid == 0) WS_ST4(P, OFF_MEANH + row * 4, __float_as_uint(mean), ST_AUX);
  }
}

constexpr int K3_HH = 0;
constexpr int K3_RING = 73728;
constexpr int K3_SSQ = K3_RING + 3 * 24576;
constexpr int K3_LDS = K3_SSQ + 4 * 3072;
#define K3_MAP 1
#define K3_ROT 0
#define PART_AUX 16

__global__ __launch_bounds__(512, 2) void k3_ffn(Ptrs P) {
  extern __shared__ __attribute__((aligned(16))) char smem[];
  lds_char* lds = (lds_char*)smem;
  const unsigned lds_a = (unsigned)(uintptr_t)lds;
  const int lane = threadIdx.x & 63, wid = __builtin_amdgcn_readfirstlane(threadIdx.x >> 6);
  const int b = blockIdx.x;
  const int c = ((b & 7) >> 1) * 4 + ((b >> 3) & 3), mb = (b & 1) * 8 + (b >> 5);
  const int mi = wid & 3, fj = wid >> 2;
  const int mt = mb * 4 + mi;
  ST_DECL; ST();
  const int rot = K3_ROT ? (mb >= 9 ? mb - 9 : mb) : 0;
  const unsigned voff = lane * 16;
  const char* wf = P.ws + OFF_WF + (size_t)c * WF_CH;
  const unsigned ring_w = lds_a + K3_RING + wid * 3072;
  auto slot_src = [&](int p) -> const char* {
    if (p < 27) { int u = p / 3 + rot; u = u >= 9 ? u - 9 : u; return wf + u * 73728 + (p % 3) * 24576 + wid * 3072; }
    const int hs = 2 * (p - 27) + fj; int d = hs / 3 + rot; d = d >= 9 ? d - 9 : d; d = d >= 9 ? d - 9 : d;
    return wf + 663552 + d * 36864 + (hs % 3) * 12288 + mi * 3072;
  };
  const char* hsrc = P.ws + OFF_HC + (size_t)mt * 36864;
  const char* hmine = hsrc + fj * 18432;
  constexpr int ST0 = K3_RING + 2 * 24576, ST1 = K3_HH, ST2 = K3_HH + 24576;
  if (fj == 1) dma16x3(P.ws + OFF_SSQ + mt * 2560, voff, lds_a + K3_SSQ + mi * 3072);
  dma16x3(hmine, voff, lds_a + ST0 + (mi * 2 + fj) * 3072);
  dma16x3(hmine + 3072, voff, lds_a + ST1 + (mi * 2 + fj) * 3072);
  dma16x3(slot_src(0), voff, ring_w);
  dma16x3(hmine + 6144, voff, lds_a + ST2 + (mi * 2 + fj) * 6144);
  dma16x3(hmine + 9216, voff, lds_a + ST2 + (mi * 2 + fj) * 6144 + 3072);
  dma16x3(slot_src(1), voff, ring_w + 24576);
  u32x4 hf[2][18];
  asm volatile("global_load_dwordx4 %0, %6, %7\n\tglobal_load_dwordx4 %1, %6, %7 offset:1024\n\tglobal_load_dwordx4 %2, %6, %7 offset:2048\n\t"
               "global_load_dwordx4 %3, %6, %8\n\tglobal_load_dwordx4 %4, %6, %8 offset:1024\n\tglobal_load_dwordx4 %5, %6, %8 offset:2048"
               : "=&v"(hf[0][15]), "=&v"(hf[0][16]), "=&v"(hf[0][17]), "=&v"(hf[1][15]), "=&v"(hf[1][16]), "=&v"(hf[1][17])
               : "v"(voff), "s"(hsrc + 15 * 1024), "s"(hsrc + 33 * 1024) : "memory");
  WAITV(15);
  BAR();
  lds_char* ring_rd = lds + K3_RING + fj * 1024 + lane * 16;
  lds_char* hh_rd = lds + K3_HH + mi * 18432 + lane * 16;
  u32x4 a[6];
#pragma unroll
  for (int i = 0; i < 6; ++i) hf[i & 1][i >> 1] = *(lds_u32x4*)(lds + ST0 + (mi * 2 + (i & 1)) * 3072 + (i >> 1) * 1024 + lane * 16);
#pragma unroll
  for (int j = 0; j < 3; ++j) a[j] = *(lds_u32x4*)(ring_rd + j * 2048);
#pragma unroll
  for (int i = 0; i < 6; ++i) hf[i & 1][3 + (i >> 1)] = *(lds_u32x4*)(lds + ST1 + (mi * 2 + (i & 1)) * 3072 + (i >> 1) * 1024 + lane * 16);
  WAITL0();
  BAR();
  dma16x3(slot_src(2), voff, ring_w + 2 * 24576);
  dma16x3(hmine + 12288, voff, lds_a + ST1 + (mi * 2 + fj) * 3072);
  ST();
  float sigma[2], gsc[2];
  const char* dsrc = wf;
  struct Acc { f32x4 v[2][2]; };

#define K3_SLOT_HEAD(WAITS, RD_OK)                                                                                       \
      const int nr = sl + 3;                                                                                             \
      const bool bar = (sl % 12) == 9;                                                                                   \
      if (bar) { WAITS; WAITL0(); BAR(); }                                                                               \
      else if (RD_OK) a[nr % 6] = *(lds_u32x4*)(ring_rd + ((nr / 12) % 3) * 24576 + (nr % 12) * 2048);                   \
      SB0();
#define K3_MFMA2(acc, B0, B1, ZERO)                                                                                      \
      if (ZERO) {                                                                                                        \
        const f32x4 z = {0.f, 0.f, 0.f, 0.f};                                                                            \
        acc.v[sl & 1][0] = MFMA16(as_frag(a[sl % 6]), as_frag(B0), z);                                                   \
        acc.v[sl & 1][1] = MFMA16(as_frag(a[sl % 6]), as_frag(B1), z);                                                   \
      } else {                                                                                                           \
        acc.v[sl & 1][0] = MFMA16(as_frag(a[sl % 6]), as_frag(B0), acc.v[sl & 1][0]);                                    \
        acc.v[sl & 1][1] = MFMA16(as_frag(a[sl % 6]), as_frag(B1), acc.v[sl & 1][1]);                                    \
      }                                                                                                                  \
      SB0();
#define K3_DMA(ON)                                                                                                       \
      if ((ON) && (sl & 1) == 0) {                                                                                       \
        const int ph = (sl + 2) % 12;                                                                                    \
        if (fj == ((ph >> 1) & 1))                                                                                       \
          dma16x1(dsrc + (ph >> 2) * 1024, voff, ring_w + (((sl + 2) / 12 + 2) % 3) * 24576 + (ph >> 2) * 1024);         \
        SB0();                                                                                                           \
      }

  auto unit = [&](Acc& acc, Acc& accp, const int up, auto first_c) {
    constexpr bool FIRST = decltype(first_c)::value;
#pragma unroll
    for (int sl = 0; sl < 36; ++sl) {
      K3_SLOT_HEAD({
        if (FIRST && sl == 9) WAITV(12); else WAITV(3);
        if (FIRST && sl == 21)
          asm volatile("" : "+v"(hf[0][15]), "+v"(hf[0][16]), "+v"(hf[0][17]), "+v"(hf[1][15]), "+v"(hf[1][16]), "+v"(hf[1][17]));
      }, true)
      K3_MFMA2(acc, hf[0][sl >> 1], hf[1][sl >> 1], sl < 2)
      if (bar) {
        a[nr % 6] = *(lds_u32x4*)(ring_rd + ((nr / 12) % 3) * 24576 + (nr % 12) * 2048);
        if (FIRST && sl == 9) {
#pragma unroll
          for (int i = 0; i < 12; ++i) hf[i & 1][6 + (i >> 1)] = *(lds_u32x4*)(lds + ST2 + (mi * 2 + (i & 1)) * 6144 + (i >> 1) * 1024 + lane * 16);
        }
        if (FIRST && sl == 21) {
#pragma unroll
          for (int i = 0; i < 6; ++i) hf[i & 1][12 + (i >> 1)] = *(lds_u32x4*)(lds + ST1 + (mi * 2 + (i & 1)) * 3072 + (i >> 1) * 1024 + lane * 16);
        }
        dsrc = slot_src(3 * up + nr / 12 + 2);
        SB0();
      }
      K3_DMA(!FIRST || sl > 9)
      if (FIRST && sl == 27) {
#pragma unroll
        for (int mh = 0; mh < 2; ++mh) {
          float ssq = 0.f;
#pragma unroll
          for (int i = 0; i < 20; ++i) ssq += *(lds_float*)(lds + K3_SSQ + mi * 3072 + (i * 32 + mh * 16 + (lane & 15)) * 4);
          sigma[mh] = sqrtf(ssq * (1.f / D_) + LN_EPS);
          gsc[mh] = -1.4426950408889634f / sigma[mh];
          asm volatile("" : "+v"(sigma[mh]), "+v"(gsc[mh]));
        }
        SB0();
      }
      if (!FIRST) {
        if (sl >= 2 && sl < 34) {
          const int e = (sl - 2) >> 2, ph = (sl - 2) & 3, mh = e >> 2;
          float tv = accp.v[0][mh][e & 3];
          if (ph == 0) tv = __builtin_amdgcn_exp2f(tv * gsc[mh]);
          if (ph == 1) tv = __builtin_amdgcn_rcpf(__builtin_fmaf(tv, sigma[mh], sigma[mh]));
          if (ph == 2) tv = tv * accp.v[1][mh][e & 3];
          if (ph < 3) { asm volatile("" : "+v"(tv)); accp.v[0][mh][e & 3] = tv; }
        }
        if (sl == 19 || sl == 35) {
          const int mh = sl == 35;
          u32x2 hv = {pk2(accp.v[0][mh][0], accp.v[0][mh][1]), pk2(accp.v[0][mh][2], accp.v[0][mh][3])};
          int u = up - 1 + rot; u = u >= 9 ? u - 9 : u;
          *(lds_u32x2*)(hh_rd + (mh * 9 + u) * 1024 + 8 * fj) = hv;
        }
        SB0();
      }
    }
  };
  Acc accA, accB;
  unit(accA, accB, 0, std::true_type{});
  ST();
#pragma unroll 1
  for (int up = 1; up < 9; up += 2) {
    unit(accB, accA, up, std::false_type{});
    unit(accA, accB, up + 1, std::false_type{});
  }
  ST();
#pragma unroll
  for (int mh = 0; mh < 2; ++mh) {
    float t[4];
#pragma unroll
    for (int e = 0; e < 4; ++e) {
      const float ex = __builtin_amdgcn_exp2f(accA.v[0][mh][e] * gsc[mh]);
      t[e] = accA.v[1][mh][e] * __builtin_amdgcn_rcpf(__builtin_fmaf(ex, sigma[mh], sigma[mh]));
    }
    u32x2 hv = {pk2(t[0], t[1]), pk2(t[2], t[3])};
    int u = 8 + rot; u = u >= 9 ? u - 9 : u;
    *(lds_u32x2*)(hh_rd + (mh * 9 + u) * 1024 + 8 * fj) = hv;
  }
  WAITL0();
  BAR();
  u32x4 hh[2][9];
#pragma unroll
  for (int i = 0; i < 18; ++i) hh[i & 1][i >> 1] = *(lds_u32x4*)(hh_rd + ((i & 1) * 9 + (i >> 1)) * 1024);
  ST();

  const auto prs = __builtin_amdgcn_make_buffer_rsrc(P.ws + OFF_PART, 0, (int)(16 * FLB), 0x00020000);
  const int pbase = ((mt * 36 + fj) * 16 + c) * 1024 + lane * 16;
  auto store_half = [&](Acc& ac, const int dq, const int mh) {
    u32x4 o = {pk2(ac.v[0][mh][0], ac.v[0][mh][1]), pk2(ac.v[0][mh][2], ac.v[0][mh][3]),
               pk2(ac.v[1][mh][0], ac.v[1][mh][1]), pk2(ac.v[1][mh][2], ac.v[1][mh][3])};
    int d = dq + rot; d = d >= 9 ? d - 9 : d;
    __builtin_amdgcn_raw_buffer_store_b128(o, prs, pbase, (2 * d + 18 * mh) * 16384, PART_AUX);
  };
  auto dblock = [&](const int k, auto mode_c) {
    constexpr int MODE = decltype(mode_c)::value;
#pragma unroll
    for (int sl = 0; sl < (MODE == 3 ? 18 : 36); ++sl) {
      K3_SLOT_HEAD({
        if (MODE == 3) WAITV(2);
        else if (sl == 21 || (MODE == 0 && sl == 9)) WAITV(3);
        else WAITV(5);
      }, MODE != 3 || nr < 18)
      Acc& acc = sl < 18 ? accA : accB;
      K3_MFMA2(acc, hh[0][(sl % 18) >> 1], hh[1][(sl % 18) >> 1], (sl % 18) < 2)
      if (bar) {
        a[nr % 6] = *(lds_u32x4*)(ring_rd + ((nr / 12) % 3) * 24576 + (nr % 12) * 2048);
        if (MODE < 2 || (MODE == 2 && sl < 33)) dsrc = slot_src(27 + 3 * k + nr / 12 + 2);
        SB0();
      }
      K3_DMA(MODE < 2 || (MODE == 2 && sl < 33))
      if (MODE > 0 && (sl == 3 || sl == 5)) { store_half(accB, 2 * k - 1, (sl - 3) >> 1); SB0(); }
      if (sl == 23 || sl == 25) { store_half(accA, 2 * k, (sl - 23) >> 1); SB0(); }
    }
  };
  dblock(0, std::integral_constant<int, 0>{});
#pragma unroll 1
  for (int k = 1; k < 3; ++k) dblock(k, std::integral_constant<int, 1>{});
  dblock(3, std::integral_constant<int, 2>{});
  dblock(4, std::integral_constant<int, 3>{});
  ST();
  store_half(accA, 8, 0);
  store_half(accA, 8, 1);
}

__global__ __launch_bounds__(256) void k4_reduce(Ptrs P) {
  const int lane = threadIdx.x & 63, fr = blockIdx.x * 4 + (threadIdx.x >> 6);
  const int t16 = fr / 18, Q = fr % 18, row = t16 * 16 + (lane & 15);
  const char* pp = P.ws + OFF_PART + (size_t)fr * 16384 + lane * 16;
  u32x4 v[16];
#pragma unroll
  for (int c = 0; c < 16; ++c) v[c] = *(const u32x4*)(pp + c * 1024);
  const u32x4 hv = *(const u32x4*)(P.ws + OFF_HC + (size_t)fr * 1024 + lane * 16);
  const float mean = ((const float*)(P.ws + OFF_MEANH))[row];
  float a[8];
#pragma unroll
  for (int k = 0; k < 4; ++k) { a[2 * k] = bflo(hv[k]) + mean; a[2 * k + 1] = bfhi(hv[k]) + mean; }
#pragma unroll
  for (int c = 0; c < 16; ++c)
#pragma unroll
    for (int k = 0; k < 4; ++k) { a[2 * k] += bflo(v[c][k]); a[2 * k + 1] += bfhi(v[c][k]); }
  u32x4 o = {pk2(a[0], a[1]), pk2(a[2], a[3]), pk2(a[4], a[5]), pk2(a[6], a[7])};
  *(u32x4*)(P.out + (size_t)row * 1152 + Q * 64 + (lane >> 4) * 16) = o;
}

extern "C" void kernel_launch(void* const* d_in, const int* in_sizes, int n_in, void* d_out, int out_size, void* d_ws,
                              size_t ws_size, hipStream_t stream) {
  Ptrs P;
  P.x = (const char*)d_in[0]; P.wqkv = (const char*)d_in[1]; P.wo = (const char*)d_in[2];
  P.wg = (const char*)d_in[4]; P.wu = (const char*)d_in[5]; P.wd = (const char*)d_in[6];
  P.ws = (char*)d_ws; P.out = (char*)d_out;
  (void)hipFuncSetAttribute((const void*)k1_qkv, hipFuncAttributeMaxDynamicSharedMemorySize, K1_LDS);
  (void)hipFuncSetAttribute((const void*)k2_attn, hipFuncAttributeMaxDynamicSharedMemorySize, K2_LDS);
  (void)hipFuncSetAttribute((const void*)k3_ffn, hipFuncAttributeMaxDynamicSharedMemorySize, K3_LDS);
  k0_prep<<<(K0_ITEMS + 3) / 4, 256, 4 * ITEM_LDS, stream>>>(P);
  k1_qkv<<<256, 512, K1_LDS, stream>>>(P);
  k2_attn<<<256, 768, K2_LDS, stream>>>(P);
  k3_ffn<<<256, 512, K3_LDS, stream>>>(P);
  k4_reduce<<<576, 256, 0, stream>>>(P);
}
```

```cpp
#include <hip/hip_runtime.h>
#include <hip/hip_bf16.h>
#include <stdint.h>
#include <type_traits>

typedef __attribute__((ext_vector_type(8))) short bf16x8;
typedef __attribute__((ext_vector_type(4))) short s16x4;
typedef __attribute__((ext_vector_type(16))) float f32x16;
typedef __attribute__((ext_vector_type(4))) float f32x4;
typedef __attribute__((ext_vector_type(2))) float f32x2;
typedef __attribute__((ext_vector_type(2))) __bf16 bf16x2;
typedef __attribute__((ext_vector_type(4))) unsigned u32x4;
typedef __attribute__((ext_vector_type(2))) unsigned u32x2;
typedef __attribute__((address_space(3))) char lds_char;
typedef __attribute__((address_space(3))) s16x4 lds_s16x4;
typedef __attribute__((address_space(3))) u32x4 lds_u32x4;
typedef __attribute__((address_space(3))) u32x2 lds_u32x2;
typedef __attribute__((address_space(3))) f32x4 lds_f32x4;
typedef __attribute__((address_space(3))) float lds_float;
typedef __attribute__((address_space(3))) unsigned lds_uint;

#define DI __device__ __forceinline__
#define MFMA32(a, b, c) __builtin_amdgcn_mfma_f32_32x32x16_bf16((a), (b), (c), 0, 0, 0)
#define MFMA16(a, b, c) __builtin_amdgcn_mfma_f32_16x16x32_bf16((a), (b), (c), 0, 0, 0)
#define WAITV(n) asm volatile("s_waitcnt vmcnt(" #n ")" ::: "memory")
#define WAITL0() asm volatile("s_waitcnt lgkmcnt(0)" ::: "memory")
#define BAR() asm volatile("s_barrier" ::: "memory")
#define SB0() __builtin_amdgcn_sched_barrier(0)

constexpr int S_ = 2048, D_ = 576, F_ = 4608;
constexpr float LN_EPS = 1e-5f;

constexpr size_t FLB = (size_t)S_ * D_ * 2;
constexpr size_t WQT_HP = 36 * 3456;
constexpr int WQT_Q = 36 * 2432;
constexpr size_t WOT_CB = 36 * 4672;
constexpr size_t WF_CH = 972 * 1024;
constexpr size_t OFF_XN = 0;
constexpr size_t OFF_PQ = OFF_XN + FLB;
constexpr size_t OFF_HC = OFF_PQ + FLB;
constexpr size_t OFF_MEANX = OFF_HC + FLB;
constexpr size_t OFF_MEANH = OFF_MEANX + 8192;
constexpr size_t OFF_SSQ = OFF_MEANH + 8192;
constexpr size_t OFF_WBAR = OFF_SSQ + 20 * 8192;
constexpr size_t OFF_KV = OFF_WBAR + 4096;
constexpr size_t OFF_WQT = OFF_KV + 53248;
constexpr size_t OFF_WOT = OFF_WQT + 16 * WQT_HP + 16384;
constexpr size_t OFF_WF = OFF_WOT + 4 * WOT_CB + 4096;
constexpr size_t OFF_PART = OFF_WF + 16 * WF_CH + 65536;
constexpr size_t WS_END = OFF_PART + 16 * FLB;

#define ST_DECL
#define ST()
#define ST_OUT(k, slot)
DI unsigned pk2(float a, float b) {
  f32x2 v = {a, b};
  bf16x2 r = __builtin_convertvector(v, bf16x2);
  return __builtin_bit_cast(unsigned, r);
}
DI float bflo(unsigned u) { return __uint_as_float(u << 16); }
DI float bfhi(unsigned u) { return __uint_as_float(u & 0xffff0000u); }
DI float phi(float t) { return (fmaxf(t, 0.f) + 1e-6f) + __builtin_amdgcn_exp2f(fminf(t * 1.4426950408889634f, 0.f)); }
DI bf16x8 as_frag(u32x4 v) { return __builtin_bit_cast(bf16x8, v); }

DI void dma16(const void* gsrc, unsigned lds_dst) {
  unsigned keep;
  asm volatile("s_mov_b32 %0, m0\n\ts_mov_b32 m0, %2\n\ts_nop 0\n\tglobal_load_lds_dwordx4 %1, off\n\ts_mov_b32 m0, %0"
               : "=&s"(keep) : "v"(gsrc), "s"(lds_dst) : "memory");
}
DI void dma16x3(const void* sbase, unsigned voff, unsigned lds_dst) {
  unsigned keep;
  asm volatile("s_mov_b32 %0, m0\n\ts_mov_b32 m0, %3\n\ts_nop 0\n\t"
               "global_load_lds_dwordx4 %1, %2\n\t"
               "global_load_lds_dwordx4 %1, %2 offset:1024\n\t"
               "global_load_lds_dwordx4 %1, %2 offset:2048\n\t"
               "s_mov_b32 m0, %0"
               : "=&s"(keep) : "v"(voff), "s"(sbase), "s"(lds_dst) : "memory");
}
DI void dma16x1(const void* sbase, unsigned voff, unsigned lds_dst) {
  unsigned keep;
  asm volatile("s_mov_b32 %0, m0\n\ts_mov_b32 m0, %3\n\ts_nop 0\n\t"
               "global_load_lds_dwordx4 %1, %2\n\t"
               "s_mov_b32 m0, %0"
               : "=&s"(keep) : "v"(voff), "s"(sbase), "s"(lds_dst) : "memory");
}
DI u32x4 tr_frag(lds_char* p0, lds_char* p1) {
  s16x4 lo = __builtin_amdgcn_ds_read_tr16_b64_v4i16((lds_s16x4*)p0);
  s16x4 hi = __builtin_amdgcn_ds_read_tr16_b64_v4i16((lds_s16x4*)p1);
  u32x2 a = __builtin_bit_cast(u32x2, lo), b = __builtin_bit_cast(u32x2, hi);
  u32x4 r = {a[0], a[1], b[0], b[1]};
  return r;
}

#define ST_AUX 16
#define WF_AUX 16
#define WS_RSRC(P) __builtin_amdgcn_make_buffer_rsrc((P).ws, 0, (int)WS_END, 0x00020000)
#define WS_ST16(P, off, v, aux) __builtin_amdgcn_raw_buffer_store_b128((v), WS_RSRC(P), (int)(off), 0, (aux))
#define WS_ST8(P, off, v, aux) __builtin_amdgcn_raw_buffer_store_b64((v), WS_RSRC(P), (int)(off), 0, (aux))
#define WS_ST4(P, off, v, aux) __builtin_amdgcn_raw_buffer_store_b32((v), WS_RSRC(P), (int)(off), 0, (aux))
struct Ptrs {
  const char* x; const char* wqkv; const char* wo; const char* wg; const char* wu; const char* wd;
  char* ws; char* out;
};

constexpr int ITEM_LDS = 9216;
constexpr int SUB_PER_XCD = 486;

DI void sub_load(const Ptrs& P, int x, int j, int lane, unsigned lds_base) {
  const int rl = lane >> 3, cp = lane & 7;
  if (j < 324) {
    const int mat = j / 162, r = j % 162, S = r / 9, cb = r % 9;
    const char* base = (mat ? P.wu : P.wg) + (size_t)(32 * S) * (F_ * 2) + (576 * x + 64 * cb) * 2;
#pragma unroll
    for (int it = 0; it < 4; ++it) {
      const int X = ((rl >> 1) & 1) | ((it & 1) << 1);
      dma16x1(base + (size_t)(8 * it) * (F_ * 2), rl * (F_ * 2) + (((cp >> 1) ^ X) * 32) + (cp & 1) * 16, lds_base + it * 1024);
    }
  } else {
    const int jj = j - 324, Ug = jj / 9, db = jj % 9;
    const char* base = P.wd + (size_t)(576 * x + 32 * Ug) * (D_ * 2) + db * 128;
#pragma unroll
    for (int it = 0; it < 4; ++it)
      dma16x1(base + (8 * it) * (D_ * 2), rl * (D_ * 2) + ((cp ^ (((rl >> 1) & 1) << 2)) * 16), lds_base + it * 1024);
  }
}
DI void sub_store(const Ptrs& P, int x, int j, int lane, lds_char* img) {
  const int g = lane >> 4, q = (lane >> 2) & 3, p = lane & 3;
  const int wf = (int)OFF_WF + lane * 16;
  if (j < 324) {
    const int mat = j / 162, r = j % 162, S = r / 9, cb = r % 9;
    const int X = ((q >> 1) & 1) | ((g & 1) << 1);
#pragma unroll
    for (int u4 = 0; u4 < 4; ++u4) {
      lds_char* a = img + (8 * g + q) * 128 + ((u4 ^ X) * 32) + 8 * p;
      u32x4 v = tr_frag(a, a + 512);
      const int ug = 4 * cb + u4, c = 2 * x + (ug >= 18 ? 1 : 0), u = ug >= 18 ? ug - 18 : ug;
      const int n = (u >> 1) * 72 + (2 * S + mat) * 2 + (u & 1);
      WS_ST16(P, wf + c * (int)WF_CH + n * 1024, v, WF_AUX);
    }
  } else {
    const int jj = j - 324, Ug = jj / 9, db = jj % 9;
    const int c = 2 * x + (Ug >= 9 ? 1 : 0), U = Ug >= 9 ? Ug - 9 : Ug;
#pragma unroll
    for (int k = 0; k < 4; ++k) {
      const int Q2 = k >> 1, t01 = k & 1;
      lds_char* a = img + (4 * g + q) * 128 + (((4 * Q2 + p) ^ (((q >> 1) & 1) << 2)) * 16) + 8 * t01;
      u32x4 v = tr_frag(a, a + 2048);
      const int n = 648 + db * 36 + (2 * U + t01) * 2 + Q2;
      WS_ST16(P, wf + c * (int)WF_CH + n * 1024, v, WF_AUX);
    }
  }
}

#define CODE_PF 1
#define N0_SUB 0
#define N1_SUB 256
constexpr int K0_ITEMS = 704 + 8 * (N0_SUB / 2);

constexpr int K1_W = 0;
constexpr int K1_ITEM = 124 * 1024;
constexpr int K1_CNT = K1_ITEM + 32768;
constexpr int K1_LDS = K1_CNT + 64;
constexpr int KVP = 192;

__global__ __launch_bounds__(512, 2) void k1_qkv(Ptrs P) {
  extern __shared__ __attribute__((aligned(16))) char smem[];
  lds_char* lds = (lds_char*)smem;
  const unsigned lds_a = (unsigned)(uintptr_t)lds;
  const int lane = threadIdx.x & 63, wid = __builtin_amdgcn_readfirstlane(threadIdx.x >> 6);
  const int b = blockIdx.x, hp = ((b & 7) >> 1) * 4 + ((b >> 3) & 3), mb = (b & 1) * 8 + (b >> 5);
  const int r = lane & 31, h = lane >> 5;
  volatile lds_uint* cnt = (volatile lds_uint*)(lds + K1_CNT);
  ST_DECL; ST();
  if (wid >= 4) {
    const int w = wid - 4;
    const char* wsrc = P.ws + OFF_WQT + (size_t)hp * WQT_HP + w * 1024;
    const unsigned wdst = lds_a + K1_W + w * 1024;
    const int x = b & 7, jl = ((b >> 3) * 2 + (w & 1)) * 4, j0 = N0_SUB + jl;
    const bool on = jl < N1_SUB;
    if (w == 0 && lane == 0) *cnt = 0u;
#pragma unroll
    for (int i = 0; i < 14; ++i) dma16x1(wsrc + i * 4096, lane * 16, wdst + i * 4096);
    WAITV(8); WAITL0(); BAR();
    ST();
#pragma unroll
    for (int i = 14; i < 20; ++i) dma16x1(wsrc + i * 4096, lane * 16, wdst + i * 4096);
    WAITV(9); BAR();
#pragma unroll
    for (int i = 20; i < 26; ++i) dma16x1(wsrc + i * 4096, lane * 16, wdst + i * 4096);
    WAITV(9); BAR();
#pragma unroll
    for (int i = 26; i < 31; ++i) dma16x1(wsrc + i * 4096, lane * 16, wdst + i * 4096);
    WAITV(9); BAR();
    if (w >= 2) {
#pragma unroll
      for (int i = 0; i < 4; ++i) if (on) sub_load(P, x, j0 + i, lane, lds_a + K1_ITEM + (w & 1) * 16384 + i * 4096);
      if (on) WAITV(16); else WAITV(0);
      BAR();
      ST();
      WAITV(0);
      ST();
#pragma unroll
      for (int i = 0; i < 4; ++i) if (on) sub_store(P, x, j0 + i, lane, lds + K1_ITEM + (w & 1) * 16384 + i * 4096);
      return;
    }
    WAITV(0); BAR();
    ST();
    const int g = lane >> 4, q = (lane >> 2) & 3, p = lane & 3, op = 4 * (g & 1) + p;
    const int vq = op >= 5 ? 19 : (w ? 14 : 10) + op;
    const int kq = (w ? 5 : 1) + op;
    lds_char* va = lds + (8 * (g >> 1) + q) * KVP + vq * 8;
    lds_char* ka = lds + (8 * (g >> 1) + q) * KVP + kq * 8;
    while (__builtin_amdgcn_readfirstlane(*cnt) < 4u) __builtin_amdgcn_s_sleep(1);
    asm volatile("" ::: "memory");
    ST();
    f32x16 kv;
#pragma unroll
    for (int i = 0; i < 16; ++i) kv[i] = 0.f;
#pragma unroll
    for (int ks = 0; ks < 8; ++ks) {
      u32x4 a = tr_frag(va + ks * 16 * KVP, va + ks * 16 * KVP + 4 * KVP);
      u32x4 bb = tr_frag(ka + ks * 16 * KVP, ka + ks * 16 * KVP + 4 * KVP);
      kv = MFMA32(as_frag(a), as_frag(bb), kv);
    }
    const int d = r - 2 * w;
    const int dd = w ? (d >= 2 ? d - 2 : 16 + d) : d;
    float* dst = (float*)(P.ws + OFF_KV) + (2 * hp + w) * 400 + dd;
    const bool dok = d >= 0 && d < 18;
#pragma unroll
    for (int i = 0; i < 12; ++i) {
      const int row = (i & 3) + 8 * (i >> 2) + 4 * h;
      const int e = row == 20 ? 18 : row - 2 * w;
      if (dok && (row == 20 || (e >= 0 && e < 18 && row < 20))) atomicAdd(dst + e * 20, kv[i]);
    }
    return;
  }
  const int mt = mb * 4 + wid;
  u32x4 xf[36];
  const char* xsrc = P.ws + OFF_XN + (size_t)mt * 36864 + lane * 16;
#pragma unroll
  for (int s = 0; s < 18; ++s) xf[s] = *(const u32x4*)(xsrc + s * 1024);
  f32x16 acc[3], accq;
#pragma unroll
  for (int i = 0; i < 16; ++i) { acc[0][i] = 0.f; acc[1][i] = 0.f; acc[2][i] = 0.f; accq[i] = 0.f; }
  lds_char* wa = lds + K1_W + (h * 76 + r) * 16;
  lds_char* wa2 = lds + K1_W + (h * 76 + 64 + (r < 12 ? r : 11)) * 16;
  u32x4 af[2][3];
#pragma unroll
  for (int s = 0; s < 36; ++s) {
    if (s % 9 == 0) {
      BAR();
      if (s < 18) {
#pragma unroll
        for (int k = s + 18; k < s + 27; ++k) xf[k] = *(const u32x4*)(xsrc + k * 1024);
      }
      af[s & 1][0] = *(lds_u32x4*)(wa + s * 2432); af[s & 1][1] = *(lds_u32x4*)(wa + s * 2432 + 512); af[s & 1][2] = *(lds_u32x4*)(wa2 + s * 2432);
    }
    if (s % 9 != 8) {
      af[(s + 1) & 1][0] = *(lds_u32x4*)(wa + (s + 1) * 2432); af[(s + 1) & 1][1] = *(lds_u32x4*)(wa + (s + 1) * 2432 + 512);
      af[(s + 1) & 1][2] = *(lds_u32x4*)(wa2 + (s + 1) * 2432);
    }
    SB0();
#pragma unroll
    for (int t = 0; t < 3; ++t) acc[t] = MFMA32(as_frag(af[s & 1][t]), as_frag(xf[s]), acc[t]);
    SB0();
  }
  ST();
  BAR();
  const int pq = (int)OFF_PQ + mt * 36864;
  lds_char* kvrow = lds + (wid * 32 + r) * KVP;
  lds_char* wq = lds + K1_W + WQT_Q + lane * 16;
  u32x4 aq[2][4];
#pragma unroll
  for (int i = 0; i < 4; ++i) aq[0][i] = *(lds_u32x4*)(wq + i * 1024);
#pragma unroll
  for (int gi = 0; gi < 10; ++gi) {
    if (gi < 8) {
#pragma unroll
      for (int i = 0; i < 4; ++i) aq[(gi + 1) & 1][i] = *(lds_u32x4*)(wq + (4 * gi + 4 + i) * 1024);
    }
    if (gi < 9) {
#pragma unroll
      for (int i = 0; i < 4; ++i) accq = MFMA32(as_frag(aq[gi & 1][i]), as_frag(xf[4 * gi + i]), accq);
    }
    const int t = gi >> 2, g = gi & 3, qd0 = 2 * gi;
    u32x2 o;
    if (qd0 < 10) {
      o[0] = pk2(phi(acc[t][4 * g]), phi(acc[t][4 * g + 1])); o[1] = pk2(phi(acc[t][4 * g + 2]), phi(acc[t][4 * g + 3]));
    } else {
      o[0] = pk2(acc[t][4 * g], acc[t][4 * g + 1]); o[1] = pk2(acc[t][4 * g + 2], acc[t][4 * g + 3]);
    }
    if (qd0 == 18 && h == 1) { o[0] = 0x3f803f80u; o[1] = 0x3f803f80u; }
    *(lds_u32x2*)(kvrow + (qd0 + h) * 8) = o;
    if (qd0 == 0 && h == 0)
      WS_ST8(P, pq + (32 + (hp >> 2)) * 1024 + (r + 32 * ((hp >> 1) & 1)) * 16 + 8 * (hp & 1), o, ST_AUX);
    if (gi < 5) {
#pragma unroll
      for (int i = 0; i < 4; ++i) {
        __builtin_amdgcn_sched_group_barrier(0x100, 1, 0);
        __builtin_amdgcn_sched_group_barrier(0x008, 1, 0);
        __builtin_amdgcn_sched_group_barrier(0x002, 7, 0);
      }
    }
    SB0();
    if (gi == 4) {
#pragma unroll
      for (int g2 = 5; g2 < 10; ++g2) {
        const int t2 = g2 >> 2, gg = g2 & 3;
        u32x2 o2 = {pk2(acc[t2][4 * gg], acc[t2][4 * gg + 1]), pk2(acc[t2][4 * gg + 2], acc[t2][4 * gg + 3])};
        if (g2 == 9 && h == 1) { o2[0] = 0x3f803f80u; o2[1] = 0x3f803f80u; }
        *(lds_u32x2*)(kvrow + (2 * g2 + h) * 8) = o2;
      }
      WAITL0();
      if (lane == 0) __hip_atomic_fetch_add((lds_uint*)cnt, 1u, __ATOMIC_RELAXED, __HIP_MEMORY_SCOPE_WORKGROUP);
      ST();
#pragma unroll
      for (int g3 = 5; g3 < 9; ++g3) {
        if (g3 < 8) {
#pragma unroll
          for (int i = 0; i < 4; ++i) aq[(g3 + 1) & 1][i] = *(lds_u32x4*)(wq + (4 * g3 + 4 + i) * 1024);
        }
        SB0();
#pragma unroll
        for (int i = 0; i < 4; ++i) accq = MFMA32(as_frag(aq[g3 & 1][i]), as_frag(xf[4 * g3 + i]), accq);
        SB0();
      }
      break;
    }
  }
  ST();
#pragma unroll
  for (int e = 0; e < 2; ++e) {
    u32x4 o;
#pragma unroll
    for (int k = 0; k < 4; ++k) o[k] = pk2(phi(accq[8 * e + 2 * k]), phi(accq[8 * e + 2 * k + 1]));
    WS_ST16(P, pq + (2 * hp + e) * 1024 + lane * 16, o, ST_AUX);
  }
}

constexpr int K2_O = 0;
constexpr int K2_RED = 36864;
constexpr int K2_MEAN = K2_RED + 32768;
constexpr int K2_ITEM = K2_MEAN + 2048;
constexpr int K2_LDS = K2_ITEM + 4 * ITEM_LDS;

__global__ __launch_bounds__(768, 3) void k2_attn(Ptrs P) {
  extern __shared__ __attribute__((aligned(16))) char smem[];
  lds_char* lds = (lds_char*)smem;
  const unsigned lds_a = (unsigned)(uintptr_t)lds;
  const int lane = threadIdx.x & 63, wid = __builtin_amdgcn_readfirstlane(threadIdx.x >> 6);
  const int b = blockIdx.x, cb = (b >> 3) & 3, mt = (b & 7) * 8 + (b >> 5);
  const int r = lane & 31, h = lane >> 5;
  ST_DECL; ST();
  if (wid >= 8) {
    const int w = wid - 8;
    const int x = b & 7, j0 = N0_SUB + N1_SUB + ((b >> 3) * 4 + w) * 2;
    const bool on = j0 < SUB_PER_XCD;
    if (on) {
      sub_load(P, x, j0, lane, lds_a + K2_ITEM + w * ITEM_LDS);
      sub_load(P, x, j0 + 1, lane, lds_a + K2_ITEM + w * ITEM_LDS + 4096);
    }
    ST();
    BAR();
    ST();
    WAITV(0);
    ST();
    if (on) {
      sub_store(P, x, j0, lane, lds + K2_ITEM + w * ITEM_LDS);
      sub_store(P, x, j0 + 1, lane, lds + K2_ITEM + w * ITEM_LDS + 4096);
    }
    ST();
    BAR();
    return;
  }
  const int t = wid & 3, kh = wid >> 2;
  const float* kvb = (const float*)(P.ws + OFF_KV) + (4 * wid) * 400;
  const int e = r < 16 ? r : (r == 16 ? 16 : (r == 20 ? 17 : 18));
  const bool rok = r <= 17 || r == 20 || r == 21;
  f32x4 k0[4], k1[4]; f32x2 k2[4];
#pragma unroll
  for (int i = 0; i < 4; ++i) {
    const float* kp = kvb + i * 400 + e * 20;
    k0[i] = *(const f32x4*)(kp + 4 * h); k1[i] = *(const f32x4*)(kp + 8 + 4 * h); k2[i] = *(const f32x2*)(kp + 16);
  }
  const char* pq = P.ws + OFF_PQ + (size_t)mt * 36864 + lane * 16;
  u32x4 qf[4];
#pragma unroll
  for (int i = 0; i < 4; ++i) qf[i] = *(const u32x4*)(pq + (4 * wid + i) * 1024);
  const u32x4 qr = *(const u32x4*)(pq + (32 + (wid >> 1)) * 1024);
  const char* wo = P.ws + OFF_WOT + (size_t)cb * WOT_CB + h * 2336;
  u32x4 af[18], a4[5];
#pragma unroll
  for (int i = 0; i < 13; ++i) af[i] = *(const u32x4*)(wo + (18 * kh + i) * 4672 + (32 * t + r) * 16);
  const int k4 = wid < 4 ? 5 * wid : 20 + 4 * (wid - 4), n4 = wid < 4 ? 5 : 4;
  float orem[4];
#pragma unroll
  for (int i = 0; i < 4; ++i) {
    u32x4 a1 = {pk2(k0[i][0], k0[i][1]), pk2(k0[i][2], k0[i][3]), pk2(k1[i][0], k1[i][1]), pk2(k1[i][2], k1[i][3])};
    u32x4 a2 = {0u, 0u, 0u, 0u};
    a2[i] = (h == (wid & 1)) ? pk2(k2[i][0], k2[i][1]) : 0u;
    if (!rok) { a1 = (u32x4){0u, 0u, 0u, 0u}; a2[i] = 0u; }
    f32x16 c;
#pragma unroll
    for (int j = 0; j < 16; ++j) c[j] = 0.f;
    c = MFMA32(as_frag(a1), as_frag(qf[i]), c);
    c = MFMA32(as_frag(a2), as_frag(qr), c);
    const float inv = __builtin_amdgcn_rcpf(c[9] + 1e-6f);
    float o[9];
#pragma unroll
    for (int j = 0; j < 9; ++j) o[j] = c[j] * inv;
    u32x4 of = {pk2(o[0], o[1]), pk2(o[2], o[3]), pk2(o[4], o[5]), pk2(o[6], o[7])};
    *(lds_u32x4*)(lds + K2_O + (4 * wid + i) * 1024 + lane * 16) = of;
    orem[i] = o[8];
    if (i == 1) SB0();
  }
  {
    u32x2 o2 = {pk2(orem[0], orem[1]), pk2(orem[2], orem[3])};
    *(lds_u32x2*)(lds + K2_O + (32 + (wid >> 1)) * 1024 + lane * 16 + 8 * (wid & 1)) = o2;
  }
  SB0();
  ST();
  WAITL0();
  BAR();
#pragma unroll
  for (int i = 13; i < 18; ++i) af[i] = *(const u32x4*)(wo + (18 * kh + i) * 4672 + (32 * t + r) * 16);
#pragma unroll
  for (int i = 0; i < 5; ++i) a4[i] = *(const u32x4*)(wo + (k4 + (i < n4 ? i : 0)) * 4672 + (128 + (r < 17 ? r : 17)) * 16);
  const int row = mt * 32 + r;
  const int s0 = cb * 9 + 2 * (wid < 4 ? wid : 4);
  const u32x4 xv0 = *(const u32x4*)(P.x + (size_t)row * 1152 + s0 * 32 + h * 16);
  const u32x4 xv1 = *(const u32x4*)(P.x + (size_t)row * 1152 + (wid < 4 ? s0 + 1 : s0) * 32 + h * 16);
  float mean = ((const float*)(P.ws + OFF_MEANX))[row];
  SB0();
  ST();
  f32x16 acc, acc4;
#pragma unroll
  for (int j = 0; j < 16; ++j) { acc[j] = 0.f; acc4[j] = 0.f; }
  lds_char* ob = lds + K2_O + lane * 16;
#pragma unroll
  for (int i = 0; i < 18; ++i) {
    acc = MFMA32(as_frag(af[i]), as_frag(*(lds_u32x4*)(ob + (18 * kh + i) * 1024)), acc);
    if (i % 6 == 5) SB0();
  }
  if (n4 == 4) a4[4] = (u32x4){0u, 0u, 0u, 0u};
#pragma unroll
  for (int i = 0; i < 5; ++i) acc4 = MFMA32(as_frag(a4[i]), as_frag(*(lds_u32x4*)(ob + (k4 + (i < n4 ? i : 0)) * 1024)), acc4);
  {
    *(lds_float*)(lds + K2_MEAN + (wid * 64 + lane) * 4) = acc4[8] + acc4[9];
    lds_char* p4 = lds + K2_RED + 16384 + wid * 2048 + lane * 16;
    *(lds_f32x4*)(p4) = (f32x4){acc4[0], acc4[1], acc4[2], acc4[3]};
    *(lds_f32x4*)(p4 + 1024) = (f32x4){acc4[4], acc4[5], acc4[6], acc4[7]};
    if (kh == 1) {
      lds_char* pr = lds + K2_RED + t * 4096 + lane * 16;
#pragma unroll
      for (int g = 0; g < 4; ++g) *(lds_f32x4*)(pr + g * 1024) = (f32x4){acc[4 * g], acc[4 * g + 1], acc[4 * g + 2], acc[4 * g + 3]};
    }
  }
  WAITL0();
  BAR();
  if (wid > 4) return;
  ST();
#pragma unroll
  for (int w = 0; w < 8; ++w) mean += *(lds_float*)(lds + K2_MEAN + (w * 64 + r) * 4);
  if (wid < 4) {
    lds_char* pr = lds + K2_RED + t * 4096 + lane * 16;
#pragma unroll
    for (int g = 0; g < 4; ++g) {
      f32x4 v = *(lds_f32x4*)(pr + g * 1024);
#pragma unroll
      for (int j = 0; j < 4; ++j) acc[4 * g + j] += v[j];
    }
  } else {
#pragma unroll
    for (int j = 0; j < 16; ++j) acc[j] = 0.f;
#pragma unroll
    for (int w = 0; w < 8; ++w) {
      lds_char* p4 = lds + K2_RED + 16384 + w * 2048 + lane * 16;
      f32x4 v0 = *(lds_f32x4*)(p4), v1 = *(lds_f32x4*)(p4 + 1024);
#pragma unroll
      for (int j = 0; j < 4; ++j) { acc[j] += v0[j]; acc[4 + j] += v1[j]; }
    }
  }
  float ssq = 0.f;
#pragma unroll
  for (int i = 0; i < 2; ++i) {
    if (wid == 4 && i == 1) break;
    float v[8];
#pragma unroll
    for (int j = 0; j < 4; ++j) {
      auto sw = __builtin_amdgcn_permlane32_swap(__float_as_uint(acc[8 * i + j]), __float_as_uint(acc[8 * i + 4 + j]), false, false);
      v[j] = __uint_as_float(sw[0]); v[4 + j] = __uint_as_float(sw[1]);
    }
    const u32x4 xv = i ? xv1 : xv0;
    u32x4 o;
#pragma unroll
    for (int k = 0; k < 4; ++k) {
      const float a = v[2 * k] + bflo(xv[k]) - mean, c = v[2 * k + 1] + bfhi(xv[k]) - mean;
      ssq += a * a + c * c;
      o[k] = pk2(a, c);
    }
    const int d8 = 2 * (s0 + i) + h;
    WS_ST16(P, OFF_HC + (((row >> 4) * 18 + (d8 >> 2)) * 64 + (row & 15) + 16 * (d8 & 3)) * 16, o, ST_AUX);
  }
  ssq += __shfl_xor(ssq, 32);
  if (h == 0) {
    WS_ST4(P, OFF_SSQ + ((mt * 20 + cb * 5 + wid) * 32 + r) * 4, __float_as_uint(ssq), ST_AUX);
    if (cb == 0 && wid == 0) WS_ST4(P, OFF_MEANH + row * 4, __float_as_uint(mean), ST_AUX);
  }
}

constexpr int K3_HH = 0;
constexpr int K3_RING = 73728;
constexpr int K3_SSQ = K3_RING + 3 * 24576;
constexpr int K3_LDS = K3_SSQ + 4 * 3072;
#define K3_MAP 1
#define K3_ROT 0
#define PART_AUX 16

DI void dma1_nosave(const void* sbase, unsigned voff, unsigned lds_dst) {
  asm volatile("s_mov_b32 m0, %2\n\ts_nop 0\n\tglobal_load_lds_dwordx4 %0, %1" :: "v"(voff), "s"(sbase), "s"(lds_dst) : "memory");
}

__global__ __launch_bounds__(512, 2) void k3_ffn(Ptrs P) {
  extern __shared__ __attribute__((aligned(16))) char smem[];
  lds_char* lds = (lds_char*)smem;
  const unsigned lds_a = (unsigned)(uintptr_t)lds;
  const int lane = threadIdx.x & 63, wid = __builtin_amdgcn_readfirstlane(threadIdx.x >> 6);
  const int b = blockIdx.x;
  const int c = ((b & 7) >> 1) * 4 + ((b >> 3) & 3), mb = (b & 1) * 8 + (b >> 5);
  const int mi = wid & 3, fj = wid >> 2;
  const int mt = mb * 4 + mi;
  ST_DECL; ST();
  const unsigned voff = lane * 16;
  const char* dsrc = P.ws + OFF_WF + (size_t)c * WF_CH + wid * 3072;
  const unsigned ring_w = lds_a + K3_RING + wid * 3072;
  const char* hsrc = P.ws + OFF_HC + (size_t)mt * 36864;
  const char* hmine = hsrc + fj * 18432;
  constexpr int ST0 = K3_RING + 2 * 24576, ST1 = K3_HH, ST2 = K3_HH + 24576;
  if (fj == 1) dma16x3(P.ws + OFF_SSQ + mt * 2560, voff, lds_a + K3_SSQ + mi * 3072);
  dma16x3(hmine, voff, lds_a + ST0 + (mi * 2 + fj) * 3072);
  dma16x3(hmine + 3072, voff, lds_a + ST1 + (mi * 2 + fj) * 3072);
  dma16x3(dsrc, voff, ring_w);
  dma16x3(hmine + 6144, voff, lds_a + ST2 + (mi * 2 + fj) * 6144);
  dma16x3(hmine + 9216, voff, lds_a + ST2 + (mi * 2 + fj) * 6144 + 3072);
  dma16x3(dsrc + 24576, voff, ring_w + 24576);
  u32x4 hf[2][18];
  asm volatile("global_load_dwordx4 %0, %6, %7\n\tglobal_load_dwordx4 %1, %6, %7 offset:1024\n\tglobal_load_dwordx4 %2, %6, %7 offset:2048\n\t"
               "global_load_dwordx4 %3, %6, %8\n\tglobal_load_dwordx4 %4, %6, %8 offset:1024\n\tglobal_load_dwordx4 %5, %6, %8 offset:2048"
               : "=&v"(hf[0][15]), "=&v"(hf[0][16]), "=&v"(hf[0][17]), "=&v"(hf[1][15]), "=&v"(hf[1][16]), "=&v"(hf[1][17])
               : "v"(voff), "s"(hsrc + 15 * 1024), "s"(hsrc + 33 * 1024) : "memory");
  WAITV(15);
  BAR();
  lds_char* ring_rd = lds + K3_RING + fj * 1024 + lane * 16;
  lds_char* hh_rd = lds + K3_HH + mi * 18432 + lane * 16;
  u32x4 a[6];
#pragma unroll
  for (int i = 0; i < 6; ++i) hf[i & 1][i >> 1] = *(lds_u32x4*)(lds + ST0 + (mi * 2 + (i & 1)) * 3072 + (i >> 1) * 1024 + lane * 16);
#pragma unroll
  for (int j = 0; j < 3; ++j) a[j] = *(lds_u32x4*)(ring_rd + j * 2048);
#pragma unroll
  for (int i = 0; i < 6; ++i) hf[i & 1][3 + (i >> 1)] = *(lds_u32x4*)(lds + ST1 + (mi * 2 + (i & 1)) * 3072 + (i >> 1) * 1024 + lane * 16);
  WAITL0();
  BAR();
  dsrc += 2 * 24576;
  dma16x3(dsrc, voff, ring_w + 2 * 24576);
  dma16x3(hmine + 12288, voff, lds_a + ST1 + (mi * 2 + fj) * 3072);
  ST();
  float sigma[2], gsc[2];
  struct Acc { f32x4 v[2][2]; };

#define K3_SLOT_HEAD(WAITS, RD_OK)                                                                                       \
      const int nr = sl + 3;                                                                                             \
      const bool bar = (sl % 12) == 9;                                                                                   \
      if (bar) { WAITS; WAITL0(); BAR(); __builtin_amdgcn_s_setprio(3); }                                                \
      else if (RD_OK) a[nr % 6] = *(lds_u32x4*)(ring_rd + ((nr / 12) % 3) * 24576 + (nr % 12) * 2048);                   \
      if (nr % 12 == 6) __builtin_amdgcn_s_setprio(2);                                                                   \
      if (nr % 12 == 9) __builtin_amdgcn_s_setprio(1);                                                                   \
      if (nr % 12 == 11) __builtin_amdgcn_s_setprio(0);                                                                  \
      SB0();
#define K3_MFMA(acc, mh, B, ZERO)                                                                                        \
      if (ZERO) asm volatile("v_mfma_f32_16x16x32_bf16 %0, %1, %2, 0" : "=&v"(acc.v[sl & 1][mh]) : "v"(a[sl % 6]), "v"(B)); \
      else asm volatile("v_mfma_f32_16x16x32_bf16 %0, %1, %2, %0" : "+v"(acc.v[sl & 1][mh]) : "v"(a[sl % 6]), "v"(B));   \
      SB0();
#define K3_NEXT_SLOT()                                                                                                   \
      dsrc += 24576;                                                                                                     \
      asm volatile("s_mov_b32 m0, %0" :: "s"(ring_w + ((nr / 12 + 2) % 3) * 24576) : "memory");                          \
      SB0();
#define K3_DMA(ON)                                                                                                       \
      if ((ON) && (sl & 3) == 2) {                                                                                       \
        const int pc = ((sl + 2) % 12) >> 2;                                                                             \
        if (pc == 0) asm volatile("global_load_lds_dwordx4 %0, %1" :: "v"(voff), "s"(dsrc) : "memory");                  \
        if (pc == 1) asm volatile("global_load_lds_dwordx4 %0, %1 offset:1024" :: "v"(voff), "s"(dsrc) : "memory");      \
        if (pc == 2) asm volatile("global_load_lds_dwordx4 %0, %1 offset:2048" :: "v"(voff), "s"(dsrc) : "memory");      \
        SB0();                                                                                                           \
      }
#define MFMA_DRAIN() do { asm volatile("s_nop 7\n\ts_nop 3" ::: "memory"); SB0(); } while (0)

  lds_char* hh_wr = hh_rd + 8 * fj;
  unsigned hpk[2];
  auto epi = [&](Acc& ap, const int g) {
    if (g < 0 || g >= 48) return;
    const int mh = g / 24, q = g % 24;
    if (q < 20) {
      const int e = (q / 10) * 2 + (q & 1), ph = (q % 10) >> 1;
      float tv = ap.v[0][mh][e];
      if (ph == 0) tv = tv * gsc[mh];
      if (ph == 1) tv = __builtin_amdgcn_exp2f(tv);
      if (ph == 2) tv = __builtin_fmaf(tv, sigma[mh], sigma[mh]);
      if (ph == 3) tv = __builtin_amdgcn_rcpf(tv);
      if (ph == 4) tv = tv * ap.v[1][mh][e];
      asm volatile("" : "+v"(tv));
      ap.v[0][mh][e] = tv;
    } else if (q < 22) {
      unsigned t = pk2(ap.v[0][mh][2 * (q - 20)], ap.v[0][mh][2 * (q - 20) + 1]);
      asm volatile("" : "+v"(t));
      hpk[q - 20] = t;
    } else if (q == 22) {
      u32x2 hv = {hpk[0], hpk[1]};
      *(lds_u32x2*)(hh_wr + mh * 9216) = hv;
    }
    SB0();
  };
  auto unit = [&](Acc& acc, Acc& accp, auto first_c) {
    constexpr bool FIRST = decltype(first_c)::value;
#pragma unroll
    for (int sl = 0; sl < 36; ++sl) {
      K3_SLOT_HEAD({
        if (FIRST && sl == 9) WAITV(12); else WAITV(3);
        if (FIRST && sl == 21)
          asm volatile("" : "+v"(hf[0][15]), "+v"(hf[0][16]), "+v"(hf[0][17]), "+v"(hf[1][15]), "+v"(hf[1][16]), "+v"(hf[1][17]));
      }, true)
      K3_MFMA(acc, 0, hf[0][sl >> 1], sl < 2)
      if (!FIRST) epi(accp, (sl & 1) ? 3 * (sl >> 1) - 2 : 3 * (sl >> 1) - 3);
      K3_MFMA(acc, 1, hf[1][sl >> 1], sl < 2)
      if (bar) {
        a[nr % 6] = *(lds_u32x4*)(ring_rd + ((nr / 12) % 3) * 24576 + (nr % 12) * 2048);
        if (FIRST && sl == 9) {
#pragma unroll
          for (int i = 0; i < 12; ++i) hf[i & 1][6 + (i >> 1)] = *(lds_u32x4*)(lds + ST2 + (mi * 2 + (i & 1)) * 6144 + (i >> 1) * 1024 + lane * 16);
        }
        if (FIRST && sl == 21) {
#pragma unroll
          for (int i = 0; i < 6; ++i) hf[i & 1][12 + (i >> 1)] = *(lds_u32x4*)(lds + ST1 + (mi * 2 + (i & 1)) * 3072 + (i >> 1) * 1024 + lane * 16);
        }
        K3_NEXT_SLOT()
      }
      K3_DMA(!FIRST || sl > 9)
      if (!FIRST && (sl & 1)) epi(accp, 3 * (sl >> 1) - 1);
      if (FIRST && sl == 27) {
#pragma unroll
        for (int mh = 0; mh < 2; ++mh) {
          float ssq = 0.f;
#pragma unroll
          for (int i = 0; i < 20; ++i) ssq += *(lds_float*)(lds + K3_SSQ + mi * 3072 + (i * 32 + mh * 16 + (lane & 15)) * 4);
          sigma[mh] = sqrtf(ssq * (1.f / D_) + LN_EPS);
          gsc[mh] = -1.4426950408889634f / sigma[mh];
          asm volatile("" : "+v"(sigma[mh]), "+v"(gsc[mh]));
        }
        SB0();
      }
    }
    if (!FIRST) hh_wr += 1024;
  };
  Acc accA, accB;
  unit(accA, accB, std::true_type{});
  ST();
#pragma unroll 1
  for (int up = 1; up < 9; up += 2) {
    unit(accB, accA, std::false_type{});
    unit(accA, accB, std::false_type{});
  }
  ST();
  MFMA_DRAIN();
#pragma unroll
  for (int mh = 0; mh < 2; ++mh) {
    float t[4];
#pragma unroll
    for (int e = 0; e < 4; ++e) {
      const float ex = __builtin_amdgcn_exp2f(accA.v[0][mh][e] * gsc[mh]);
      t[e] = accA.v[1][mh][e] * __builtin_amdgcn_rcpf(__builtin_fmaf(ex, sigma[mh], sigma[mh]));
    }
    u32x2 hv = {pk2(t[0], t[1]), pk2(t[2], t[3])};
    *(lds_u32x2*)(hh_wr + mh * 9216) = hv;
  }
  WAITL0();
  BAR();
  u32x4 hh[2][9];
#pragma unroll
  for (int i = 0; i < 18; ++i) hh[i & 1][i >> 1] = *(lds_u32x4*)(hh_rd + ((i & 1) * 9 + (i >> 1)) * 1024);
  ST();

  const auto prs = __builtin_amdgcn_make_buffer_rsrc(P.ws + OFF_PART, 0, (int)(16 * FLB), 0x00020000);
  const int pbase = ((mt * 36 + fj) * 16 + c) * 1024 + lane * 16;
  u32x4 ov;
  auto outp = [&](Acc& ac, const int dq, const int g) {
    if (g < 0 || g >= 10) return;
    const int mh = g / 5, q = g % 5;
    if (q < 4) {
      unsigned t = pk2(ac.v[q >> 1][mh][2 * (q & 1)], ac.v[q >> 1][mh][2 * (q & 1) + 1]);
      asm volatile("" : "+v"(t));
      ov[q] = t;
    } else {
      __builtin_amdgcn_raw_buffer_store_b128(ov, prs, pbase, (2 * dq + 18 * mh) * 16384, PART_AUX);
    }
    SB0();
  };
  auto dblock = [&](const int k, auto mode_c) {
    constexpr int MODE = decltype(mode_c)::value;
#pragma unroll
    for (int sl = 0; sl < (MODE == 3 ? 18 : 36); ++sl) {
      K3_SLOT_HEAD({
        if (MODE == 3) WAITV(2);
        else if (sl == 21 || (MODE == 0 && sl == 9)) WAITV(3);
        else WAITV(5);
      }, MODE != 3 || nr < 18)
      Acc& acc = sl < 18 ? accA : accB;
      K3_MFMA(acc, 0, hh[0][(sl % 18) >> 1], (sl % 18) < 2)
      if (MODE > 0 && sl < 8) outp(accB, 2 * k - 1, 2 * sl - 2);
      if (sl >= 22) outp(accA, 2 * k, 2 * (sl - 22));
      K3_MFMA(acc, 1, hh[1][(sl % 18) >> 1], (sl % 18) < 2)
      if (MODE > 0 && sl < 8) outp(accB, 2 * k - 1, 2 * sl - 1);
      if (sl >= 22) outp(accA, 2 * k, 2 * (sl - 22) + 1);
      if (bar) {
        a[nr % 6] = *(lds_u32x4*)(ring_rd + ((nr / 12) % 3) * 24576 + (nr % 12) * 2048);
        K3_NEXT_SLOT()
      }
      K3_DMA(MODE < 2 || (MODE == 2 && sl < 33))
    }
  };
  dblock(0, std::integral_constant<int, 0>{});
#pragma unroll 1
  for (int k = 1; k < 3; ++k) dblock(k, std::integral_constant<int, 1>{});
  dblock(3, std::integral_constant<int, 2>{});
  dblock(4, std::integral_constant<int, 3>{});
  ST();
  MFMA_DRAIN();
#pragma unroll
  for (int g = 0; g < 10; ++g) outp(accA, 8, g);
}

__global__ __launch_bounds__(256) void k4_reduce(Ptrs P) {
  const int lane = threadIdx.x & 63, fr = blockIdx.x * 4 + (threadIdx.x >> 6);
  const int t16 = fr / 18, Q = fr % 18, row = t16 * 16 + (lane & 15);
  const char* pp = P.ws + OFF_PART + (size_t)fr * 16384 + lane * 16;
  u32x4 v[16];
#pragma unroll
  for (int c = 0; c < 16; ++c) v[c] = *(const u32x4*)(pp + c * 1024);
  const u32x4 hv = *(const u32x4*)(P.ws + OFF_HC + (size_t)fr * 1024 + lane * 16);
  const float mean = ((const float*)(P.ws + OFF_MEANH))[row];
  float a[8];
#pragma unroll
  for (int k = 0; k < 4; ++k) { a[2 * k] = bflo(hv[k]) + mean; a[2 * k + 1] = bfhi(hv[k]) + mean; }
#pragma unroll
  for (int c = 0; c < 16; ++c)
#pragma unroll
    for (int k = 0; k < 4; ++k) { a[2 * k] += bflo(v[c][k]); a[2 * k + 1] += bfhi(v[c][k]); }
  u32x4 o = {pk2(a[0], a[1]), pk2(a[2], a[3]), pk2(a[4], a[5]), pk2(a[6], a[7])};
  *(u32x4*)(P.out + (size_t)row * 1152 + Q * 64 + (lane >> 4) * 16) = o;
}

__global__ __launch_bounds__(256) void k0_prep(Ptrs P) {
  extern __shared__ __attribute__((aligned(16))) char smem[];
  const int lane = threadIdx.x & 63, wid = __builtin_amdgcn_readfirstlane(threadIdx.x >> 6);
  const int id = blockIdx.x * 4 + wid;
  lds_char* img = (lds_char*)smem + wid * ITEM_LDS;
  const unsigned img_a = (unsigned)(uintptr_t)img;
  if (blockIdx.x < 64 && wid == 3) {
    unsigned long long pc, k1a;
    asm volatile("s_getpc_b64 %0" : "=s"(pc));
    asm volatile("s_getpc_b64 s[40:41]\n\ts_add_u32 s40, s40, _Z6k1_qkv4Ptrs@rel32@lo+4\n\ts_addc_u32 s41, s41, _Z6k1_qkv4Ptrs@rel32@hi+12\n\t"
                 "s_mov_b64 %0, s[40:41]" : "=s"(k1a) :: "s40", "s41", "scc");
    const char* c0 = (const char*)k1a;
    const char* c1 = (const char*)pc;
    const char* p = c0 + ((blockIdx.x >> 3) * 64 + lane) * 128;
    if (p < c1) { unsigned v = *(const volatile unsigned*)p; asm volatile("" :: "v"(v)); }
  }
  if (id < 256 || (id >= 616 && id < 688)) {
    const bool is_x = id < 256;
    const int r0 = (is_x ? id : id - 616) * 8, row = r0 + (lane >> 3), i = lane & 7;
    const char* src = (is_x ? P.x : P.wo) + (size_t)row * 1152 + i * 16;
    u32x4 v[9];
#pragma unroll
    for (int j = 0; j < 9; ++j) v[j] = *(const u32x4*)(src + j * 128);
    float sum = 0.f;
#pragma unroll
    for (int j = 0; j < 9; ++j)
#pragma unroll
      for (int k = 0; k < 4; ++k) sum += bflo(v[j][k]) + bfhi(v[j][k]);
    sum += __shfl_xor(sum, 1); sum += __shfl_xor(sum, 2); sum += __shfl_xor(sum, 4);
    const float mean = sum * (1.f / D_);
    if (!is_x) {
      const int hd = row / 18, e = row % 18;
      const int ks = e < 16 ? hd : 32 + (hd >> 3), jj = e < 16 ? (e & 3) + 4 * (e >> 3) : (hd & 7), hh = e < 16 ? (e >> 2) & 1 : e - 16;
      const unsigned hi = pk2(mean, 0.f) & 0xffffu;
      const unsigned lo = pk2(mean - bflo(hi), 0.f) & 0xffffu;
      *(unsigned short*)(P.ws + OFF_WOT + (size_t)(i >> 1) * WOT_CB + ks * 4672 + (hh * 146 + 144 + (i & 1)) * 16 + jj * 2) =
          (unsigned short)((i & 1) ? lo : hi);
      return;
    }
    float ss = 0.f;
#pragma unroll
    for (int j = 0; j < 9; ++j)
#pragma unroll
      for (int k = 0; k < 4; ++k) { const float a = bflo(v[j][k]) - mean, b = bfhi(v[j][k]) - mean; ss += a * a + b * b; }
    ss += __shfl_xor(ss, 1); ss += __shfl_xor(ss, 2); ss += __shfl_xor(ss, 4);
    const float rstd = rsqrtf(ss * (1.f / D_) + LN_EPS);
    if (i == 0) WS_ST4(P, OFF_MEANX + row * 4, __float_as_uint(mean), ST_AUX);
    const int dst = (int)OFF_XN + ((row >> 5) * 36 * 64 + (row & 31) + 32 * (i & 1) + (i >> 1) * 64) * 16;
#pragma unroll
    for (int j = 0; j < 9; ++j) {
      u32x4 o;
#pragma unroll
      for (int k = 0; k < 4; ++k) o[k] = pk2((bflo(v[j][k]) - mean) * rstd, (bfhi(v[j][k]) - mean) * rstd);
      WS_ST16(P, dst + j * 4096, o, ST_AUX);
    }
  } else if (id < 544) {
    const int j = id - 256, s = j >> 3, hp0 = (j & 7) * 2;
#pragma unroll
    for (int it = 0; it < 7; ++it) {
      int e = it * 64 + lane; e = e < 432 ? e : 431;
      const int row = e / 27, r = e % 27, seg = r / 9, ch = r % 9;
      dma16(P.wqkv + (size_t)(16 * s + row) * 3456 + seg * 1152 + hp0 * 72 + ch * 16, img_a + it * 1024);
    }
    WAITV(0);
    const int g = lane >> 4, q = (lane >> 2) & 3, p = lane & 3;
#pragma unroll
    for (int it = 0; it < 7; ++it) {
      int Q = 16 * it + 4 * g + p; Q = Q < 108 ? Q : 107;
      const int hl = Q / 54, r = Q % 54, h = r / 27, sg = r % 27;
      int seg, col;
      if (sg < 4) { seg = 0; col = 4 * sg; } else if (sg < 8) { seg = 0; col = 4 * sg + 4; } else if (sg == 8) { seg = 0; col = 16; }
      else if (sg < 18) { seg = 1; col = 4 * (sg - 9); } else { seg = 2; col = 4 * (sg - 18); }
      lds_char* a = img + (8 * h + q) * 432 + seg * 144 + (36 * hl + col) * 2;
      u32x4 v = tr_frag(a, a + 4 * 432);
      const int o = it * 64 + lane, ol = o % 216, oh = ol / 108, slot = ol % 108;
      const int off = slot < 32 ? WQT_Q + s * 1024 + (oh * 32 + slot) * 16 : s * 2432 + (oh * 76 + slot - 32) * 16;
      if (o < 432) WS_ST16(P, OFF_WQT + (hp0 + o / 216) * (int)WQT_HP + off, v, ST_AUX);
    }
  } else if (id < 616) {
    const int j = id - 544, ks = j >> 1, hf = j & 1;
#pragma unroll
    for (int it = 0; it < 9; ++it) {
      const int e = it * 64 + lane, kk = e / 36, ch = e % 36, h = kk >> 3, jj = kk & 7;
      const int row = ks < 32 ? 18 * ks + (jj & 3) + 8 * (jj >> 2) + 4 * h : 18 * (8 * (ks - 32) + jj) + 16 + h;
      dma16(P.wo + (size_t)row * 1152 + hf * 576 + ch * 16, img_a + it * 1024);
    }
    WAITV(0);
    const int g = lane >> 4, q = (lane >> 2) & 3, p = lane & 3;
#pragma unroll
    for (int it = 0; it < 9; ++it) {
      const int G16 = 4 * it + g;
      const int cl = G16 / 18, r = G16 % 18, h = r / 9, sg = r % 9;
      lds_char* a = img + (8 * h + q) * 576 + (144 * cl + 16 * sg + 4 * p) * 2;
      u32x4 v = tr_frag(a, a + 4 * 576);
      WS_ST16(P, OFF_WOT + (2 * hf + cl) * (int)WOT_CB + ks * 4672 + (h * 146 + 16 * sg + (lane & 15)) * 16, v, ST_AUX);
    }
  } else if (id < 696) {
    const u32x4 z = {0u, 0u, 0u, 0u};
    for (int k = id - 688; k < 50; k += 8) WS_ST16(P, OFF_KV + k * 1024 + lane * 16, z, ST_AUX);
  } else if (id < 704) {
  } else if (id >= 704) {
    const int bb = blockIdx.x - 176, x = bb & 7, j0 = ((bb >> 3) * 4 + wid) * 2;
    if (j0 < N0_SUB) {
      sub_load(P, x, j0, lane, img_a);
      sub_load(P, x, j0 + 1, lane, img_a + 4096);
      WAITV(0);
      sub_store(P, x, j0, lane, img);
      sub_store(P, x, j0 + 1, lane, img + 4096);
    }
  }
}

extern "C" void kernel_launch(void* const* d_in, const int* in_sizes, int n_in, void* d_out, int out_size, void* d_ws,
                              size_t ws_size, hipStream_t stream) {
  Ptrs P;
  P.x = (const char*)d_in[0]; P.wqkv = (const char*)d_in[1]; P.wo = (const char*)d_in[2];
  P.wg = (const char*)d_in[4]; P.wu = (const char*)d_in[5]; P.wd = (const char*)d_in[6];
  P.ws = (char*)d_ws; P.out = (char*)d_out;
  (void)hipFuncSetAttribute((const void*)k1_qkv, hipFuncAttributeMaxDynamicSharedMemorySize, K1_LDS);
  (void)hipFuncSetAttribute((const void*)k2_attn, hipFuncAttributeMaxDynamicSharedMemorySize, K2_LDS);
  (void)hipFuncSetAttribute((const void*)k3_ffn, hipFuncAttributeMaxDynamicSharedMemorySize, K3_LDS);
  k0_prep<<<176 + 8 * ((N0_SUB + 7) / 8), 256, 4 * ITEM_LDS, stream>>>(P);
  k1_qkv<<<256, 512, K1_LDS, stream>>>(P);
  k2_attn<<<256, 768, K2_LDS, stream>>>(P);
  k3_ffn<<<256, 512, K3_LDS, stream>>>(P);
  k4_reduce<<<576, 256, 0, stream>>>(P);
}
```

```cpp
#include <hip/hip_runtime.h>
#include <hip/hip_bf16.h>
#include <stdint.h>
#include <type_traits>

typedef __attribute__((ext_vector_type(8))) short bf16x8;
typedef __attribute__((ext_vector_type(4))) short s16x4;
typedef __attribute__((ext_vector_type(16))) float f32x16;
typedef __attribute__((ext_vector_type(4))) float f32x4;
typedef __attribute__((ext_vector_type(2))) float f32x2;
typedef __attribute__((ext_vector_type(2))) __bf16 bf16x2;
typedef __attribute__((ext_vector_type(4))) unsigned u32x4;
typedef __attribute__((ext_vector_type(2))) unsigned u32x2;
typedef __attribute__((address_space(3))) char lds_char;
typedef __attribute__((address_space(3))) s16x4 lds_s16x4;
typedef __attribute__((address_space(3))) u32x4 lds_u32x4;
typedef __attribute__((address_space(3))) u32x2 lds_u32x2;
typedef __attribute__((address_space(3))) f32x4 lds_f32x4;
typedef __attribute__((address_space(3))) float lds_float;
typedef __attribute__((address_space(3))) f32x2 lds_f32x2;
typedef __attribute__((address_space(3))) unsigned lds_uint;

#define DI __device__ __forceinline__
#define MFMA32(a, b, c) __builtin_amdgcn_mfma_f32_32x32x16_bf16((a), (b), (c), 0, 0, 0)
#define MFMA16(a, b, c) __builtin_amdgcn_mfma_f32_16x16x32_bf16((a), (b), (c), 0, 0, 0)
#define WAITV(n) asm volatile("s_waitcnt vmcnt(" #n ")" ::: "memory")
#define WAITL0() asm volatile("s_waitcnt lgkmcnt(0)" ::: "memory")
#define BAR() asm volatile("s_barrier" ::: "memory")
#define SB0() __builtin_amdgcn_sched_barrier(0)

constexpr int S_ = 2048, D_ = 576, F_ = 4608;
constexpr float LN_EPS = 1e-5f;

constexpr size_t FLB = (size_t)S_ * D_ * 2;
constexpr size_t WQT_HP = 36 * 3456;
constexpr int WQT_Q = 36 * 2432;
constexpr size_t WOT_CB = 36 * 4672;
constexpr size_t WF_CH = 972 * 1024;
constexpr size_t OFF_XN = 0;
constexpr size_t OFF_PQ = OFF_XN + FLB;
constexpr size_t OFF_HC = OFF_PQ + FLB;
constexpr size_t OFF_MEANX = OFF_HC + FLB;
constexpr size_t OFF_MEANH = OFF_MEANX + 8192;
constexpr size_t OFF_SSQ = OFF_MEANH + 8192;
constexpr size_t OFF_WBAR = OFF_SSQ + 20 * 8192;
constexpr size_t OFF_KV = OFF_WBAR + 4096;
constexpr size_t OFF_WQT = OFF_KV + 53248;
constexpr size_t OFF_WOT = OFF_WQT + 16 * WQT_HP + 16384;
constexpr size_t OFF_WF = OFF_WOT + 4 * WOT_CB + 4096;
constexpr size_t OFF_PART = OFF_WF + 16 * WF_CH + 65536;
constexpr size_t OFF_XH = OFF_PART + 8 * FLB;
constexpr size_t OFF_FLAG = OFF_XH + 256 * 73728;
constexpr size_t OFF_CNT = OFF_FLAG + 256 * 128;
constexpr size_t WS_END = OFF_CNT + 32 * 128;

#define ST_DECL
#define ST()
#define ST_OUT(k, slot)
DI unsigned pk2(float a, float b) {
  f32x2 v = {a, b};
  bf16x2 r = __builtin_convertvector(v, bf16x2);
  return __builtin_bit_cast(unsigned, r);
}
DI float bflo(unsigned u) { return __uint_as_float(u << 16); }
DI float bfhi(unsigned u) { return __uint_as_float(u & 0xffff0000u); }
DI float phi(float t) {
  const float e = __builtin_amdgcn_fmed3f(__builtin_amdgcn_exp2f(t * 1.4426950408889634f), 0.f, 1.f);
  return (__builtin_amdgcn_fmed3f(t, 0.f, __builtin_inff()) + 1e-6f) + e;
}
DI bf16x8 as_frag(u32x4 v) { return __builtin_bit_cast(bf16x8, v); }

DI void dma16(const void* gsrc, unsigned lds_dst) {
  unsigned keep;
  asm volatile("s_mov_b32 %0, m0\n\ts_mov_b32 m0, %2\n\ts_nop 0\n\tglobal_load_lds_dwordx4 %1, off\n\ts_mov_b32 m0, %0"
               : "=&s"(keep) : "v"(gsrc), "s"(lds_dst) : "memory");
}
DI void dma16x3(const void* sbase, unsigned voff, unsigned lds_dst) {
  unsigned keep;
  asm volatile("s_mov_b32 %0, m0\n\ts_mov_b32 m0, %3\n\ts_nop 0\n\t"
               "global_load_lds_dwordx4 %1, %2\n\t"
               "global_load_lds_dwordx4 %1, %2 offset:1024\n\t"
               "global_load_lds_dwordx4 %1, %2 offset:2048\n\t"
               "s_mov_b32 m0, %0"
               : "=&s"(keep) : "v"(voff), "s"(sbase), "s"(lds_dst) : "memory");
}
DI void dma16x1(const void* sbase, unsigned voff, unsigned lds_dst) {
  unsigned keep;
  asm volatile("s_mov_b32 %0, m0\n\ts_mov_b32 m0, %3\n\ts_nop 0\n\t"
               "global_load_lds_dwordx4 %1, %2\n\t"
               "s_mov_b32 m0, %0"
               : "=&s"(keep) : "v"(voff), "s"(sbase), "s"(lds_dst) : "memory");
}
DI u32x4 tr_frag(lds_char* p0, lds_char* p1) {
  s16x4 lo = __builtin_amdgcn_ds_read_tr16_b64_v4i16((lds_s16x4*)p0);
  s16x4 hi = __builtin_amdgcn_ds_read_tr16_b64_v4i16((lds_s16x4*)p1);
  u32x2 a = __builtin_bit_cast(u32x2, lo), b = __builtin_bit_cast(u32x2, hi);
  u32x4 r = {a[0], a[1], b[0], b[1]};
  return r;
}

#define ST_AUX 16
#define WF_AUX 16
#define WS_RSRC(P) __builtin_amdgcn_make_buffer_rsrc((P).ws, 0, (int)WS_END, 0x00020000)
#define WS_ST16(P, off, v, aux) __builtin_amdgcn_raw_buffer_store_b128((v), WS_RSRC(P), (int)(off), 0, (aux))
#define WS_ST8(P, off, v, aux) __builtin_amdgcn_raw_buffer_store_b64((v), WS_RSRC(P), (int)(off), 0, (aux))
#define WS_ST4(P, off, v, aux) __builtin_amdgcn_raw_buffer_store_b32((v), WS_RSRC(P), (int)(off), 0, (aux))
struct Ptrs {
  const char* x; const char* wqkv; const char* wo; const char* wg; const char* wu; const char* wd;
  char* ws; char* out;
};

constexpr int ITEM_LDS = 9216;
constexpr int SUB_PER_XCD = 486;

DI void sub_load(const Ptrs& P, int x, int j, int lane, unsigned lds_base) {
  const int rl = lane >> 3, cp = lane & 7;
  if (j < 324) {
    const int mat = j / 162, r = j % 162, S = r / 9, cb = r % 9;
    const char* base = (mat ? P.wu : P.wg) + (size_t)(32 * S) * (F_ * 2) + (576 * x + 64 * cb) * 2;
#pragma unroll
    for (int it = 0; it < 4; ++it) {
      const int X = ((rl >> 1) & 1) | ((it & 1) << 1);
      dma16x1(base + (size_t)(8 * it) * (F_ * 2), rl * (F_ * 2) + (((cp >> 1) ^ X) * 32) + (cp & 1) * 16, lds_base + it * 1024);
    }
  } else {
    const int jj = j - 324, Ug = jj / 9, db = jj % 9;
    const char* base = P.wd + (size_t)(576 * x + 32 * Ug) * (D_ * 2) + db * 128;
#pragma unroll
    for (int it = 0; it < 4; ++it)
      dma16x1(base + (8 * it) * (D_ * 2), rl * (D_ * 2) + ((cp ^ (((rl >> 1) & 1) << 2)) * 16), lds_base + it * 1024);
  }
}
DI void sub_store(const Ptrs& P, int x, int j, int lane, lds_char* img) {
  const int g = lane >> 4, q = (lane >> 2) & 3, p = lane & 3;
  const int wf = (int)OFF_WF + lane * 16;
  if (j < 324) {
    const int mat = j / 162, r = j % 162, S = r / 9, cb = r % 9;
    const int X = ((q >> 1) & 1) | ((g & 1) << 1);
#pragma unroll
    for (int u4 = 0; u4 < 4; ++u4) {
      lds_char* a = img + (8 * g + q) * 128 + ((u4 ^ X) * 32) + 8 * p;
      u32x4 v = tr_frag(a, a + 512);
      const int ug = 4 * cb + u4, c = 2 * x + (ug >= 18 ? 1 : 0), u = ug >= 18 ? ug - 18 : ug;
      const int n = (u >> 1) * 72 + (2 * S + mat) * 2 + (u & 1);
      WS_ST16(P, wf + c * (int)WF_CH + n * 1024, v, WF_AUX);
    }
  } else {
    const int jj = j - 324, Ug = jj / 9, db = jj % 9;
    const int cs = Ug >= 9 ? 1 : 0, U = Ug >= 9 ? Ug - 9 : Ug;
#pragma unroll
    for (int k = 0; k < 4; ++k) {
      const int Q2 = k >> 1, t01 = k & 1;
      lds_char* a = img + (4 * g + q) * 128 + (((4 * Q2 + p) ^ (((q >> 1) & 1) << 2)) * 16) + 8 * t01;
      u32x4 v = tr_frag(a, a + 2048);
      const int Q = 2 * db + Q2, pd = Q >= 9 ? 1 : 0, Qr = Q - 9 * pd;
      const int fj = Qr < 4 ? 0 : (Qr == 4 ? t01 : 1), i = Qr < 4 ? 2 * Qr + t01 : (Qr == 4 ? 8 : 2 * (Qr - 5) + t01);
      const int ip = i == 8 ? 0 : i + 1;
      const int ib = (i == 8 || i < 4) ? (U < 6 ? ip * 6 + U : 24 + ip * 3 + U) : 9 + i * 9 + U;
      const int n = pd == cs ? 648 + (U * 9 + i) * 2 + fj : 810 + ib * 2 + fj;
      WS_ST16(P, wf + (2 * x + pd) * (int)WF_CH + n * 1024, v, WF_AUX);
    }
  }
}

#define CODE_PF 1
#define N0_SUB 0
#define N1_SUB 256
constexpr int K0_ITEMS = 704 + 8 * (N0_SUB / 2);

constexpr int K1_W = 0;
constexpr int K1_ITEM = 124 * 1024;
constexpr int K1_CNT = K1_ITEM + 32768;
constexpr int K1_LDS = K1_CNT + 64;
constexpr int KVP = 192;

__global__ __launch_bounds__(512, 2) void k1_qkv(Ptrs P) {
  extern __shared__ __attribute__((aligned(16))) char smem[];
  lds_char* lds = (lds_char*)smem;
  const unsigned lds_a = (unsigned)(uintptr_t)lds;
  const int lane = threadIdx.x & 63, wid = __builtin_amdgcn_readfirstlane(threadIdx.x >> 6);
  const int b = blockIdx.x, hp = ((b & 7) >> 1) * 4 + ((b >> 3) & 3), mb = (b & 1) * 8 + (b >> 5);
  const int r = lane & 31, h = lane >> 5;
  volatile lds_uint* cnt = (volatile lds_uint*)(lds + K1_CNT);
  ST_DECL; ST();
  if (wid >= 4) {
    const int w = wid - 4;
    const char* wsrc = P.ws + OFF_WQT + (size_t)hp * WQT_HP + w * 1024;
    const unsigned wdst = lds_a + K1_W + w * 1024;
    const int x = b & 7, jl = ((b >> 3) * 2 + (w & 1)) * 4, j0 = N0_SUB + jl;
    const bool on = jl < N1_SUB;
    if (w == 0 && lane == 0) *cnt = 0u;
#pragma unroll
    for (int i = 0; i < 14; ++i) dma16x1(wsrc + i * 4096, lane * 16, wdst + i * 4096);
    WAITV(8); WAITL0(); BAR();
    ST();
#pragma unroll
    for (int i = 14; i < 20; ++i) dma16x1(wsrc + i * 4096, lane * 16, wdst + i * 4096);
    WAITV(9); BAR();
#pragma unroll
    for (int i = 20; i < 26; ++i) dma16x1(wsrc + i * 4096, lane * 16, wdst + i * 4096);
    WAITV(9); BAR();
#pragma unroll
    for (int i = 26; i < 31; ++i) dma16x1(wsrc + i * 4096, lane * 16, wdst + i * 4096);
    WAITV(9); BAR();
    if (w >= 2) {
#pragma unroll
      for (int i = 0; i < 4; ++i) if (on) sub_load(P, x, j0 + i, lane, lds_a + K1_ITEM + (w & 1) * 16384 + i * 4096);
      if (on) WAITV(16); else WAITV(0);
      BAR();
      ST();
      WAITV(0);
      ST();
#pragma unroll
      for (int i = 0; i < 4; ++i) if (on) sub_store(P, x, j0 + i, lane, lds + K1_ITEM + (w & 1) * 16384 + i * 4096);
      return;
    }
    WAITV(0); BAR();
    ST();
    const int g = lane >> 4, q = (lane >> 2) & 3, p = lane & 3, op = 4 * (g & 1) + p;
    const int vq = op >= 5 ? 19 : (w ? 14 : 10) + op;
    const int kq = (w ? 5 : 1) + op;
    lds_char* va = lds + (8 * (g >> 1) + q) * KVP + vq * 8;
    lds_char* ka = lds + (8 * (g >> 1) + q) * KVP + kq * 8;
    while (__builtin_amdgcn_readfirstlane(*cnt) < 4u) __builtin_amdgcn_s_sleep(1);
    asm volatile("" ::: "memory");
    ST();
    f32x16 kv;
#pragma unroll
    for (int i = 0; i < 16; ++i) kv[i] = 0.f;
#pragma unroll
    for (int ks = 0; ks < 8; ++ks) {
      u32x4 a = tr_frag(va + ks * 16 * KVP, va + ks * 16 * KVP + 4 * KVP);
      u32x4 bb = tr_frag(ka + ks * 16 * KVP, ka + ks * 16 * KVP + 4 * KVP);
      kv = MFMA32(as_frag(a), as_frag(bb), kv);
    }
    const int d = r - 2 * w;
    const int dd = w ? (d >= 2 ? d - 2 : 16 + d) : d;
    float* dst = (float*)(P.ws + OFF_KV) + (2 * hp + w) * 400 + dd;
    const bool dok = d >= 0 && d < 18;
#pragma unroll
    for (int i = 0; i < 12; ++i) {
      const int row = (i & 3) + 8 * (i >> 2) + 4 * h;
      const int e = row == 20 ? 18 : row - 2 * w;
      if (dok && (row == 20 || (e >= 0 && e < 18 && row < 20))) atomicAdd(dst + e * 20, kv[i]);
    }
    return;
  }
  const int mt = mb * 4 + wid;
  u32x4 xf[36];
  const char* xsrc = P.ws + OFF_XN + (size_t)mt * 36864 + lane * 16;
#pragma unroll
  for (int s = 0; s < 18; ++s) xf[s] = *(const u32x4*)(xsrc + s * 1024);
  f32x16 acc[3], accq;
#pragma unroll
  for (int i = 0; i < 16; ++i) { acc[0][i] = 0.f; acc[1][i] = 0.f; acc[2][i] = 0.f; accq[i] = 0.f; }
  lds_char* wa = lds + K1_W + (h * 76 + r) * 16;
  lds_char* wa2 = lds + K1_W + (h * 76 + 64 + (r < 12 ? r : 11)) * 16;
  u32x4 af[2][3];
#pragma unroll
  for (int s = 0; s < 36; ++s) {
    if (s % 9 == 0) {
      BAR();
      if (s < 18) {
#pragma unroll
        for (int k = s + 18; k < s + 27; ++k) xf[k] = *(const u32x4*)(xsrc + k * 1024);
      }
      af[s & 1][0] = *(lds_u32x4*)(wa + s * 2432); af[s & 1][1] = *(lds_u32x4*)(wa + s * 2432 + 512); af[s & 1][2] = *(lds_u32x4*)(wa2 + s * 2432);
    }
    if (s % 9 != 8) {
      af[(s + 1) & 1][0] = *(lds_u32x4*)(wa + (s + 1) * 2432); af[(s + 1) & 1][1] = *(lds_u32x4*)(wa + (s + 1) * 2432 + 512);
      af[(s + 1) & 1][2] = *(lds_u32x4*)(wa2 + (s + 1) * 2432);
    }
    SB0();
#pragma unroll
    for (int t = 0; t < 3; ++t) acc[t] = MFMA32(as_frag(af[s & 1][t]), as_frag(xf[s]), acc[t]);
    SB0();
  }
  ST();
  BAR();
  const int pq = (int)OFF_PQ + mt * 36864;
  lds_char* kvrow = lds + (wid * 32 + r) * KVP;
  lds_char* wq = lds + K1_W + WQT_Q + lane * 16;
  u32x4 aq[2][4];
  u32x2 qrem;
#pragma unroll
  for (int i = 0; i < 4; ++i) aq[0][i] = *(lds_u32x4*)(wq + i * 1024);
#pragma unroll
  for (int gi = 0; gi < 10; ++gi) {
    if (gi < 8) {
#pragma unroll
      for (int i = 0; i < 4; ++i) aq[(gi + 1) & 1][i] = *(lds_u32x4*)(wq + (4 * gi + 4 + i) * 1024);
    }
    if (gi < 9) {
#pragma unroll
      for (int i = 0; i < 4; ++i) accq = MFMA32(as_frag(aq[gi & 1][i]), as_frag(xf[4 * gi + i]), accq);
    }
    const int t = gi >> 2, g = gi & 3, qd0 = 2 * gi;
    u32x2 o;
    if (qd0 < 10) {
      o[0] = pk2(phi(acc[t][4 * g]), phi(acc[t][4 * g + 1])); o[1] = pk2(phi(acc[t][4 * g + 2]), phi(acc[t][4 * g + 3]));
    } else {
      o[0] = pk2(acc[t][4 * g], acc[t][4 * g + 1]); o[1] = pk2(acc[t][4 * g + 2], acc[t][4 * g + 3]);
    }
    if (qd0 == 18 && h == 1) { o[0] = 0x3f803f80u; o[1] = 0x3f803f80u; }
    *(lds_u32x2*)(kvrow + (qd0 + h) * 8) = o;
    if (qd0 == 0) qrem = o;
    if (gi < 5) {
#pragma unroll
      for (int i = 0; i < 4; ++i) {
        __builtin_amdgcn_sched_group_barrier(0x100, 1, 0);
        __builtin_amdgcn_sched_group_barrier(0x008, 1, 0);
        __builtin_amdgcn_sched_group_barrier(0x002, 7, 0);
      }
    }
    SB0();
    if (gi == 4) {
#pragma unroll
      for (int g2 = 5; g2 < 10; ++g2) {
        const int t2 = g2 >> 2, gg = g2 & 3;
        u32x2 o2 = {pk2(acc[t2][4 * gg], acc[t2][4 * gg + 1]), pk2(acc[t2][4 * gg + 2], acc[t2][4 * gg + 3])};
        if (g2 == 9 && h == 1) { o2[0] = 0x3f803f80u; o2[1] = 0x3f803f80u; }
        *(lds_u32x2*)(kvrow + (2 * g2 + h) * 8) = o2;
      }
      WAITL0();
      if (lane == 0) __hip_atomic_fetch_add((lds_uint*)cnt, 1u, __ATOMIC_RELAXED, __HIP_MEMORY_SCOPE_WORKGROUP);
      ST();
#pragma unroll
      for (int g3 = 5; g3 < 9; ++g3) {
        if (g3 < 8) {
#pragma unroll
          for (int i = 0; i < 4; ++i) aq[(g3 + 1) & 1][i] = *(lds_u32x4*)(wq + (4 * g3 + 4 + i) * 1024);
        }
        SB0();
#pragma unroll
        for (int i = 0; i < 4; ++i) accq = MFMA32(as_frag(aq[g3 & 1][i]), as_frag(xf[4 * g3 + i]), accq);
        SB0();
      }
      break;
    }
  }
  ST();
#pragma unroll
  for (int e = 0; e < 2; ++e) {
    u32x4 o;
#pragma unroll
    for (int k = 0; k < 4; ++k) o[k] = pk2(phi(accq[8 * e + 2 * k]), phi(accq[8 * e + 2 * k + 1]));
    WS_ST16(P, pq + (2 * hp + e) * 1024 + lane * 16, o, ST_AUX);
  }
  if (h == 0) WS_ST8(P, pq + (32 + (hp >> 2)) * 1024 + (r + 32 * ((hp >> 1) & 1)) * 16 + 8 * (hp & 1), qrem, ST_AUX);
}

constexpr int K2_O = 0;
constexpr int K2_RED = 36864;
constexpr int K2_MEAN = K2_RED + 32768;
constexpr int K2_ITEM = K2_MEAN + 2048;
constexpr int K2_LDS = K2_ITEM + 4 * ITEM_LDS;

__global__ __launch_bounds__(768, 3) void k2_attn(Ptrs P) {
  extern __shared__ __attribute__((aligned(16))) char smem[];
  lds_char* lds = (lds_char*)smem;
  const unsigned lds_a = (unsigned)(uintptr_t)lds;
  const int lane = threadIdx.x & 63, wid = __builtin_amdgcn_readfirstlane(threadIdx.x >> 6);
  const int b = blockIdx.x, cb = (b >> 3) & 3, mt = (b & 7) * 8 + (b >> 5);
  const int r = lane & 31, h = lane >> 5;
  ST_DECL; ST();
  if (wid >= 8) {
    const int w = wid - 8;
    const int x = b & 7, j0 = N0_SUB + N1_SUB + ((b >> 3) * 4 + w) * 2;
    const bool on = j0 < SUB_PER_XCD;
    if (on) {
      sub_load(P, x, j0, lane, lds_a + K2_ITEM + w * ITEM_LDS);
      sub_load(P, x, j0 + 1, lane, lds_a + K2_ITEM + w * ITEM_LDS + 4096);
    }
    ST();
    BAR();
    ST();
    WAITV(0);
    ST();
    if (on) {
      sub_store(P, x, j0, lane, lds + K2_ITEM + w * ITEM_LDS);
      sub_store(P, x, j0 + 1, lane, lds + K2_ITEM + w * ITEM_LDS + 4096);
    }
    ST();
    BAR();
    return;
  }
  const int t = wid & 3, kh = wid >> 2;
  const float* kvb = (const float*)(P.ws + OFF_KV) + (4 * wid) * 400;
  const int e = r < 16 ? r : (r == 16 ? 16 : (r == 20 ? 17 : 18));
  const bool rok = r <= 17 || r == 20 || r == 21;
  f32x4 k0[4], k1[4]; f32x2 k2[4];
#pragma unroll
  for (int i = 0; i < 4; ++i) {
    const float* kp = kvb + i * 400 + e * 20;
    k0[i] = *(const f32x4*)(kp + 4 * h); k1[i] = *(const f32x4*)(kp + 8 + 4 * h); k2[i] = *(const f32x2*)(kp + 16);
  }
  const char* pq = P.ws + OFF_PQ + (size_t)mt * 36864 + lane * 16;
  u32x4 qf[4];
#pragma unroll
  for (int i = 0; i < 4; ++i) qf[i] = *(const u32x4*)(pq + (4 * wid + i) * 1024);
  const u32x4 qr = *(const u32x4*)(pq + (32 + (wid >> 1)) * 1024);
  const char* wo = P.ws + OFF_WOT + (size_t)cb * WOT_CB + h * 2336;
  u32x4 af[18], a4[5];
#pragma unroll
  for (int i = 0; i < 13; ++i) af[i] = *(const u32x4*)(wo + (18 * kh + i) * 4672 + (32 * t + r) * 16);
  const int k4 = wid < 4 ? 5 * wid : 20 + 4 * (wid - 4), n4 = wid < 4 ? 5 : 4;
  float orem[4];
#pragma unroll
  for (int i = 0; i < 4; ++i) {
    u32x4 a1 = {pk2(k0[i][0], k0[i][1]), pk2(k0[i][2], k0[i][3]), pk2(k1[i][0], k1[i][1]), pk2(k1[i][2], k1[i][3])};
    u32x4 a2 = {0u, 0u, 0u, 0u};
    a2[i] = (h == (wid & 1)) ? pk2(k2[i][0], k2[i][1]) : 0u;
    if (!rok) { a1 = (u32x4){0u, 0u, 0u, 0u}; a2[i] = 0u; }
    f32x16 c;
#pragma unroll
    for (int j = 0; j < 16; ++j) c[j] = 0.f;
    c = MFMA32(as_frag(a1), as_frag(qf[i]), c);
    c = MFMA32(as_frag(a2), as_frag(qr), c);
    const float inv = __builtin_amdgcn_rcpf(c[9] + 1e-6f);
    float o[9];
#pragma unroll
    for (int j = 0; j < 9; ++j) o[j] = c[j] * inv;
    u32x4 of = {pk2(o[0], o[1]), pk2(o[2], o[3]), pk2(o[4], o[5]), pk2(o[6], o[7])};
    *(lds_u32x4*)(lds + K2_O + (4 * wid + i) * 1024 + lane * 16) = of;
    orem[i] = o[8];
    if (i == 1) SB0();
  }
  {
    u32x2 o2 = {pk2(orem[0], orem[1]), pk2(orem[2], orem[3])};
    *(lds_u32x2*)(lds + K2_O + (32 + (wid >> 1)) * 1024 + lane * 16 + 8 * (wid & 1)) = o2;
  }
  SB0();
  ST();
  WAITL0();
  BAR();
#pragma unroll
  for (int i = 13; i < 18; ++i) af[i] = *(const u32x4*)(wo + (18 * kh + i) * 4672 + (32 * t + r) * 16);
#pragma unroll
  for (int i = 0; i < 5; ++i) a4[i] = *(const u32x4*)(wo + (k4 + (i < n4 ? i : 0)) * 4672 + (128 + (r < 17 ? r : 17)) * 16);
  const int row = mt * 32 + r;
  const int s0 = cb * 9 + 2 * (wid < 4 ? wid : 4);
  const u32x4 xv0 = *(const u32x4*)(P.x + (size_t)row * 1152 + s0 * 32 + h * 16);
  const u32x4 xv1 = *(const u32x4*)(P.x + (size_t)row * 1152 + (wid < 4 ? s0 + 1 : s0) * 32 + h * 16);
  float mean = ((const float*)(P.ws + OFF_MEANX))[row];
  SB0();
  ST();
  f32x16 acc, acc4;
#pragma unroll
  for (int j = 0; j < 16; ++j) { acc[j] = 0.f; acc4[j] = 0.f; }
  lds_char* ob = lds + K2_O + lane * 16;
#pragma unroll
  for (int i = 0; i < 18; ++i) {
    acc = MFMA32(as_frag(af[i]), as_frag(*(lds_u32x4*)(ob + (18 * kh + i) * 1024)), acc);
    if (i % 6 == 5) SB0();
  }
  if (n4 == 4) a4[4] = (u32x4){0u, 0u, 0u, 0u};
#pragma unroll
  for (int i = 0; i < 5; ++i) acc4 = MFMA32(as_frag(a4[i]), as_frag(*(lds_u32x4*)(ob + (k4 + (i < n4 ? i : 0)) * 1024)), acc4);
  {
    *(lds_float*)(lds + K2_MEAN + (wid * 64 + lane) * 4) = acc4[8] + acc4[9];
    lds_char* p4 = lds + K2_RED + 16384 + wid * 2048 + lane * 16;
    *(lds_f32x4*)(p4) = (f32x4){acc4[0], acc4[1], acc4[2], acc4[3]};
    *(lds_f32x4*)(p4 + 1024) = (f32x4){acc4[4], acc4[5], acc4[6], acc4[7]};
    if (kh == 1) {
      lds_char* pr = lds + K2_RED + t * 4096 + lane * 16;
#pragma unroll
      for (int g = 0; g < 4; ++g) *(lds_f32x4*)(pr + g * 1024) = (f32x4){acc[4 * g], acc[4 * g + 1], acc[4 * g + 2], acc[4 * g + 3]};
    }
  }
  WAITL0();
  BAR();
  if (wid > 4) return;
  ST();
#pragma unroll
  for (int w = 0; w < 8; ++w) mean += *(lds_float*)(lds + K2_MEAN + (w * 64 + r) * 4);
  if (wid < 4) {
    lds_char* pr = lds + K2_RED + t * 4096 + lane * 16;
#pragma unroll
    for (int g = 0; g < 4; ++g) {
      f32x4 v = *(lds_f32x4*)(pr + g * 1024);
#pragma unroll
      for (int j = 0; j < 4; ++j) acc[4 * g + j] += v[j];
    }
  } else {
#pragma unroll
    for (int j = 0; j < 16; ++j) acc[j] = 0.f;
#pragma unroll
    for (int w = 0; w < 8; ++w) {
      lds_char* p4 = lds + K2_RED + 16384 + w * 2048 + lane * 16;
      f32x4 v0 = *(lds_f32x4*)(p4), v1 = *(lds_f32x4*)(p4 + 1024);
#pragma unroll
      for (int j = 0; j < 4; ++j) { acc[j] += v0[j]; acc[4 + j] += v1[j]; }
    }
  }
  float ssq = 0.f;
#pragma unroll
  for (int i = 0; i < 2; ++i) {
    if (wid == 4 && i == 1) break;
    float v[8];
#pragma unroll
    for (int j = 0; j < 4; ++j) {
      auto sw = __builtin_amdgcn_permlane32_swap(__float_as_uint(acc[8 * i + j]), __float_as_uint(acc[8 * i + 4 + j]), false, false);
      v[j] = __uint_as_float(sw[0]); v[4 + j] = __uint_as_float(sw[1]);
    }
    const u32x4 xv = i ? xv1 : xv0;
    u32x4 o;
#pragma unroll
    for (int k = 0; k < 4; ++k) {
      const float a = v[2 * k] + bflo(xv[k]) - mean, c = v[2 * k + 1] + bfhi(xv[k]) - mean;
      ssq += a * a + c * c;
      o[k] = pk2(a, c);
    }
    const int d8 = 2 * (s0 + i) + h;
    WS_ST16(P, OFF_HC + (((row >> 4) * 18 + (d8 >> 2)) * 64 + (row & 15) + 16 * (d8 & 3)) * 16, o, ST_AUX);
  }
  ssq += __shfl_xor(ssq, 32);
  if (h == 0) {
    WS_ST4(P, OFF_SSQ + ((mt * 20 + cb * 5 + wid) * 32 + r) * 4, __float_as_uint(ssq), ST_AUX);
    if (cb == 0 && wid == 0) WS_ST4(P, OFF_MEANH + row * 4, __float_as_uint(mean), ST_AUX);
  }
}

constexpr int K3_HH = 0;
constexpr int K3_RING = 73728;
constexpr int K3_SSQ = K3_RING + 3 * 24576;
constexpr int K3_LDS = K3_SSQ + 4 * 3072;
#define K3_MAP 1
#define K3_ROT 0
#define PART_AUX 16

constexpr int p2_tile(int sl) {
  if (sl < 81) return sl % 9;
  const int x = sl - 81;
  if (x >= 45) return 4 + (x - 45) / 9;
  const int p = x < 30 ? x / 6 : (x - 30) / 3;
  return p == 0 ? 8 : p - 1;
}
constexpr int p2_u(int sl) {
  if (sl < 81) return sl / 9;
  const int x = sl - 81;
  return x >= 45 ? (x - 45) % 9 : (x < 30 ? x % 6 : 6 + (x - 30) % 3);
}
constexpr int p2_og(int sl) { return sl >= 145 ? 2 : sl >= 127 ? 1 : sl >= 121 ? 0 : sl >= 115 ? 4 : -1; }
constexpr int p2_o0(int sl) { return 2 * (sl - (sl >= 145 ? 145 : sl >= 127 ? 127 : sl >= 121 ? 121 : 115)); }
constexpr int p2_ld(int sl) {
  return sl == 11 ? 0 : sl == 15 ? 1 : sl == 24 ? 2 : sl == 28 ? 3 : sl == 35 ? 4 : sl == 40 ? 5 : sl == 71 ? 6 : sl == 73 ? 7 : sl == 76 ? 8 : -1;
}
constexpr int p2_wr(int sl) {
  return sl == 36 ? 0 : sl == 39 ? 1 : sl == 48 ? 2 : sl == 52 ? 3 : sl == 59 ? 4 : sl == 63 ? 5 : sl == 95 ? 6 : sl == 97 ? 7 : sl == 100 ? 8 : -1;
}
constexpr int p2_rd(int sl) {
  return sl == 55 ? 0 : sl == 61 ? 1 : sl == 65 ? 2 : sl == 72 ? 3 : sl == 75 ? 4 : sl == 79 ? 5 : sl == 106 ? 6 : sl == 107 ? 7 : sl == 108 ? 8 : -1;
}
constexpr int p2_pre(int sl) { return (p2_og(sl) >= 0 && (p2_o0(sl) == 4 || p2_o0(sl) == 8)) ? 1 : 0; }
constexpr int p2_dma(int sl) { return (sl < 141 && (sl & 3) == 2) ? 1 : 0; }
constexpr int p2_post(int sl) { return (sl == 23 || sl == 53 || p2_ld(sl) >= 0) ? 1 : 0; }
constexpr int p2_wait(int sl) {
  if (sl == 9) return 3;
  int n = p2_post(sl - 15);
  for (int i = sl - 14; i < sl; ++i) n += p2_pre(i) + p2_dma(i) + p2_post(i);
  return n;
}
template <int I, int N, class F> DI void static_for(F&& f) {
  if constexpr (I < N) { f(std::integral_constant<int, I>{}); static_for<I + 1, N>(f); }
}
DI void waitv_n(const int n) {
  switch (n) {
    case 0: WAITV(0); break;
    case 1: WAITV(1); break;
    case 2: WAITV(2); break;
    case 3: WAITV(3); break;
    case 4: WAITV(4); break;
    case 5: WAITV(5); break;
    case 6: WAITV(6); break;
    case 7: WAITV(7); break;
    case 8: WAITV(8); break;
    case 9: WAITV(9); break;
    case 10: WAITV(10); break;
    case 11: WAITV(11); break;
    case 12: WAITV(12); break;
    case 13: WAITV(13); break;
    case 14: WAITV(14); break;
    case 15: WAITV(15); break;
    case 16: WAITV(16); break;
    case 17: WAITV(17); break;
    case 18: WAITV(18); break;
    case 19: WAITV(19); break;
    case 20: WAITV(20); break;
    case 21: WAITV(21); break;
    case 22: WAITV(22); break;
    case 23: WAITV(23); break;
    case 24: WAITV(24); break;
    default: WAITV(0); break;
  }
}
DI void dma1_nosave(const void* sbase, unsigned voff, unsigned lds_dst) {
  asm volatile("s_mov_b32 m0, %2\n\ts_nop 0\n\tglobal_load_lds_dwordx4 %0, %1" :: "v"(voff), "s"(sbase), "s"(lds_dst) : "memory");
}

__global__ __launch_bounds__(512, 2) void k3_ffn(Ptrs P) {
  extern __shared__ __attribute__((aligned(16))) char smem[];
  lds_char* lds = (lds_char*)smem;
  const unsigned lds_a = (unsigned)(uintptr_t)lds;
  const int lane = threadIdx.x & 63, wid = __builtin_amdgcn_readfirstlane(threadIdx.x >> 6);
  const int b = blockIdx.x;
  const int c = ((b & 7) >> 1) * 4 + ((b >> 3) & 3), mb = (b & 1) * 8 + (b >> 5);
  const int mi = wid & 3, fj = wid >> 2;
  const int mt = mb * 4 + mi;
  ST_DECL; ST();
  const unsigned voff = lane * 16;
  const char* dsrc = P.ws + OFF_WF + (size_t)c * WF_CH + wid * 3072;
  const unsigned ring_w = lds_a + K3_RING + wid * 3072;
  const char* hsrc = P.ws + OFF_HC + (size_t)mt * 36864;
  const char* hmine = hsrc + fj * 18432;
  constexpr int ST0 = K3_RING + 2 * 24576, ST1 = K3_HH, ST2 = K3_HH + 24576;
  dma16x3(hmine, voff, lds_a + ST0 + (mi * 2 + fj) * 3072);
  dma16x3(dsrc, voff, ring_w);
  dma16x3(hmine + 3072, voff, lds_a + ST1 + (mi * 2 + fj) * 3072);
  dma16x3(hmine + 6144, voff, lds_a + ST2 + (mi * 2 + fj) * 6144);
  dma16x3(hmine + 9216, voff, lds_a + ST2 + (mi * 2 + fj) * 6144 + 3072);
  u32x4 hf[2][18];
  WAITV(6);
  BAR();
  lds_char* ring_rd = lds + K3_RING + fj * 1024 + lane * 16;
  lds_char* hh_rd = lds + K3_HH + mi * 18432 + lane * 16;
  u32x4 a[6];
#pragma unroll
  for (int i = 0; i < 6; ++i) hf[i & 1][i >> 1] = *(lds_u32x4*)(lds + ST0 + (mi * 2 + (i & 1)) * 3072 + (i >> 1) * 1024 + lane * 16);
#pragma unroll
  for (int j = 0; j < 3; ++j) a[j] = *(lds_u32x4*)(ring_rd + j * 2048);
#pragma unroll
  for (int i = 0; i < 6; ++i) hf[i & 1][3 + (i >> 1)] = *(lds_u32x4*)(lds + ST1 + (mi * 2 + (i & 1)) * 3072 + (i >> 1) * 1024 + lane * 16);
  WAITL0();
  BAR();
  dma16x3(dsrc + 24576, voff, ring_w + 24576);
  dsrc += 2 * 24576;
  ST();
  float sigma[2], gsc[2];
  struct Acc { f32x4 v[2][2]; };

#define K3_SLOT_HEAD(WAITS, RD_OK)                                                                                       \
      const int nr = sl + 3;                                                                                             \
      const bool bar = (sl % 12) == 9;                                                                                   \
      if (bar) { WAITS; WAITL0(); BAR(); __builtin_amdgcn_s_setprio(3); }                                                \
      else if (RD_OK) a[nr % 6] = *(lds_u32x4*)(ring_rd + ((nr / 12) % 3) * 24576 + (nr % 12) * 2048);                   \
      if (nr % 12 == 6) __builtin_amdgcn_s_setprio(2);                                                                   \
      if (nr % 12 == 9) __builtin_amdgcn_s_setprio(1);                                                                   \
      if (nr % 12 == 11) __builtin_amdgcn_s_setprio(0);                                                                  \
      SB0();
#define K3_MFMA(acc, mh, B, ZERO)                                                                                        \
      if (ZERO) asm volatile("v_mfma_f32_16x16x32_bf16 %0, %1, %2, 0" : "=&v"(acc.v[sl & 1][mh]) : "v"(a[sl % 6]), "v"(B)); \
      else asm volatile("v_mfma_f32_16x16x32_bf16 %0, %1, %2, %0" : "+v"(acc.v[sl & 1][mh]) : "v"(a[sl % 6]), "v"(B));   \
      SB0();
#define K3_NEXT_SLOT()                                                                                                   \
      dsrc += 24576;                                                                                                     \
      asm volatile("s_mov_b32 m0, %0" :: "s"(ring_w + ((nr / 12 + 2) % 3) * 24576) : "memory");                          \
      SB0();
#define K3_DMA(ON)                                                                                                       \
      if ((ON) && (sl & 3) == 2) {                                                                                       \
        const int pc = ((sl + 2) % 12) >> 2;                                                                             \
        if (pc == 0) asm volatile("global_load_lds_dwordx4 %0, %1" :: "v"(voff), "s"(dsrc) : "memory");                  \
        if (pc == 1) asm volatile("global_load_lds_dwordx4 %0, %1 offset:1024" :: "v"(voff), "s"(dsrc) : "memory");      \
        if (pc == 2) asm volatile("global_load_lds_dwordx4 %0, %1 offset:2048" :: "v"(voff), "s"(dsrc) : "memory");      \
        SB0();                                                                                                           \
      }
#define MFMA_DRAIN() do { asm volatile("s_nop 7\n\ts_nop 3" ::: "memory"); SB0(); } while (0)

  lds_char* hh_wr = hh_rd + 8 * fj;
  u32x4 hpk;
  const auto wsr = WS_RSRC(P);
  const int fl_mine = (int)OFF_FLAG + b * 128;
  unsigned prog = 0;
  int xh_off = (int)OFF_XH + ((b * 4 + mi) * 2 + fj) * 9216;
  auto epi = [&](Acc& ap, const int g) {
    if (g < 0 || g >= 48) return;
    const int mh = g / 24, q = g % 24;
    if (q < 20) {
      const int e = (q / 10) * 2 + (q & 1), ph = (q % 10) >> 1;
      float tv = ap.v[0][mh][e];
      if (ph == 0) tv = tv * gsc[mh];
      if (ph == 1) tv = __builtin_amdgcn_exp2f(tv);
      if (ph == 2) tv = __builtin_fmaf(tv, sigma[mh], sigma[mh]);
      if (ph == 3) tv = __builtin_amdgcn_rcpf(tv);
      if (ph == 4) tv = tv * ap.v[1][mh][e];
      asm volatile("" : "+v"(tv));
      ap.v[0][mh][e] = tv;
    } else if (q < 22) {
      unsigned t = pk2(ap.v[0][mh][2 * (q - 20)], ap.v[0][mh][2 * (q - 20) + 1]);
      asm volatile("" : "+v"(t));
      hpk[2 * mh + q - 20] = t;
    } else if (q == 22) {
      u32x2 hv = {hpk[2 * mh], hpk[2 * mh + 1]};
      *(lds_u32x2*)(hh_wr + mh * 9216) = hv;
    } else if (mh == 1) {
      __builtin_amdgcn_raw_buffer_store_b128(hpk, wsr, voff, xh_off, 16);
    }
    SB0();
  };
  auto unit = [&](Acc& acc, Acc& accp, auto first_c) __attribute__((always_inline)) {
    constexpr bool FIRST = decltype(first_c)::value;
    static_for<0, 36>([&](auto ic) __attribute__((always_inline)) {
      constexpr int sl = decltype(ic)::value;
      K3_SLOT_HEAD({
        if (FIRST && sl == 9) WAITV(15); else if (sl == 9 || sl == 33) WAITV(4); else WAITV(3);
        if (FIRST && sl == 21)
          asm volatile("" : "+v"(hf[0][15]), "+v"(hf[0][16]), "+v"(hf[0][17]), "+v"(hf[1][15]), "+v"(hf[1][16]), "+v"(hf[1][17]));
      }, true)
      K3_MFMA(acc, 0, hf[0][sl >> 1], sl < 2)
      if (!FIRST) epi(accp, (sl & 1) ? 3 * (sl >> 1) - 2 : 3 * (sl >> 1) - 3);
      K3_MFMA(acc, 1, hf[1][sl >> 1], sl < 2)
      if (bar) {
        a[nr % 6] = *(lds_u32x4*)(ring_rd + ((nr / 12) % 3) * 24576 + (nr % 12) * 2048);
        if (FIRST && sl == 9) {
#pragma unroll
          for (int i = 0; i < 12; ++i) hf[i & 1][6 + (i >> 1)] = *(lds_u32x4*)(lds + ST2 + (mi * 2 + (i & 1)) * 6144 + (i >> 1) * 1024 + lane * 16);
        }
        if (FIRST && sl == 21) {
#pragma unroll
          for (int i = 0; i < 6; ++i) hf[i & 1][12 + (i >> 1)] = *(lds_u32x4*)(lds + ST1 + (mi * 2 + (i & 1)) * 3072 + (i >> 1) * 1024 + lane * 16);
        }
        K3_NEXT_SLOT()
      }
      K3_DMA(!FIRST || sl > 9)
      if (FIRST && sl == 1) { dma16x3(hmine + 12288, voff, lds_a + ST1 + (mi * 2 + fj) * 3072); SB0(); }
      if (FIRST && sl == 3) { dma16x3(dsrc, voff, ring_w + 2 * 24576); SB0(); }
      if (FIRST && sl == 5) {
        asm volatile("global_load_dwordx4 %0, %6, %7\n\tglobal_load_dwordx4 %1, %6, %7 offset:1024\n\tglobal_load_dwordx4 %2, %6, %7 offset:2048\n\t"
                     "global_load_dwordx4 %3, %6, %8\n\tglobal_load_dwordx4 %4, %6, %8 offset:1024\n\tglobal_load_dwordx4 %5, %6, %8 offset:2048"
                     : "=&v"(hf[0][15]), "=&v"(hf[0][16]), "=&v"(hf[0][17]), "=&v"(hf[1][15]), "=&v"(hf[1][16]), "=&v"(hf[1][17])
                     : "v"(voff), "s"(hsrc + 15 * 1024), "s"(hsrc + 33 * 1024) : "memory");
        SB0();
      }
      if (FIRST && sl == 7) { dma16x3(P.ws + OFF_SSQ + mt * 2560, voff, lds_a + K3_SSQ + mi * 3072); SB0(); }
      if (!FIRST && (sl & 1)) epi(accp, 3 * (sl >> 1) - 1);
      if (sl == 23) { __builtin_amdgcn_raw_buffer_store_b32(prog, wsr, 0, fl_mine, 16); SB0(); }
      if (FIRST && sl == 33) { __builtin_amdgcn_raw_buffer_store_b128(a[0], wsr, voff, xh_off, 16); SB0(); }
      if (FIRST && sl == 27) {
#pragma unroll
        for (int mh = 0; mh < 2; ++mh) {
          float ssq = 0.f;
#pragma unroll
          for (int i = 0; i < 20; ++i) ssq += *(lds_float*)(lds + K3_SSQ + mi * 3072 + (i * 32 + mh * 16 + (lane & 15)) * 4);
          sigma[mh] = sqrtf(ssq * (1.f / D_) + LN_EPS);
          gsc[mh] = -1.4426950408889634f / sigma[mh];
          asm volatile("" : "+v"(sigma[mh]), "+v"(gsc[mh]));
        }
        SB0();
      }
    });
    if (!FIRST) { hh_wr += 1024; xh_off += 1024; ++prog; }
  };
  Acc accA, accB;
  unit(accA, accB, std::true_type{});
  ST();
#pragma unroll 1
  for (int up = 1; up < 9; up += 2) {
    unit(accB, accA, std::false_type{});
    unit(accA, accB, std::false_type{});
  }
  ST();
  const char* fl_other = P.ws + OFF_FLAG + (b ^ 8) * 128;
  unsigned pollv;
  asm volatile("global_load_dword %0, %1, %2 sc1" : "=&v"(pollv) : "v"(0u), "s"(fl_other) : "memory");
  MFMA_DRAIN();
#pragma unroll
  for (int mh = 0; mh < 2; ++mh) {
    float t[4];
#pragma unroll
    for (int e = 0; e < 4; ++e) {
      const float ex = __builtin_amdgcn_exp2f(accA.v[0][mh][e] * gsc[mh]);
      t[e] = accA.v[1][mh][e] * __builtin_amdgcn_rcpf(__builtin_fmaf(ex, sigma[mh], sigma[mh]));
    }
    hpk[2 * mh] = pk2(t[0], t[1]); hpk[2 * mh + 1] = pk2(t[2], t[3]);
    u32x2 hv = {hpk[2 * mh], hpk[2 * mh + 1]};
    *(lds_u32x2*)(hh_wr + mh * 9216) = hv;
  }
  __builtin_amdgcn_raw_buffer_store_b128(hpk, wsr, voff, xh_off, 16);
  u32x4 ho[2][2];
  ho[0][0] = *(lds_u32x4*)(hh_rd); ho[0][1] = *(lds_u32x4*)(hh_rd + 9216);
  ST();

  f32x4 oc[9][2];
  u32x4 hp[9][2];
  u32x4 xl[9];
  u32x4 ov;
  const int pbase = (int)OFF_PART + ((mt * 36 + 9 * (c & 1)) * 8 + (c >> 1)) * 1024;
  const char* xsrc = P.ws + OFF_XH + (size_t)(((b ^ 8) * 4 + mi) * 2 + fj) * 9216;
  lds_char* hx_wr = hh_rd + 8 * fj;
  auto outp = [&](const int pr, const int g) {
    if (g < 0 || g >= 10) return;
    const int mh = g / 5, q = g % 5;
    if (pr < 4) {
      if (q < 4) {
        unsigned t = pk2(oc[2 * pr + (q >> 1)][mh][2 * (q & 1)], oc[2 * pr + (q >> 1)][mh][2 * (q & 1) + 1]);
        asm volatile("" : "+v"(t));
        ov[q] = t;
      } else {
        __builtin_amdgcn_raw_buffer_store_b128(ov, wsr, voff, pbase + (5 * fj + pr + 18 * mh) * 8192, PART_AUX);
      }
    } else {
      if (q < 2) {
        unsigned t = pk2(oc[8][mh][2 * q], oc[8][mh][2 * q + 1]);
        asm volatile("" : "+v"(t));
        ov[q] = t;
      } else if (q == 4) {
        u32x2 o2 = {ov[0], ov[1]};
        __builtin_amdgcn_raw_buffer_store_b64(o2, wsr, voff, pbase + (4 + 18 * mh) * 8192 + 8 * fj, PART_AUX);
      } else return;
    }
    SB0();
  };
  static_for<0, 162>([&](auto ic) __attribute__((always_inline)) {
    constexpr int sl = decltype(ic)::value;
    K3_SLOT_HEAD({
      waitv_n(p2_wait(sl));
      if (sl == 9 || sl == 69) {
        constexpr unsigned need = sl == 9 ? 6u : 9u;
        asm volatile("" : "+v"(pollv));
        if (__builtin_amdgcn_readfirstlane(pollv) < need) {
          _Pragma("unroll 1") for (int spin = 0; spin < (1 << 22); ++spin) {
            asm volatile("global_load_dword %0, %1, %2 sc1\n\ts_waitcnt vmcnt(0)" : "=&v"(pollv) : "v"(0u), "s"(fl_other) : "memory");
            if (__builtin_amdgcn_readfirstlane(pollv) >= need) break;
            __builtin_amdgcn_s_sleep(2);
          }
        }
      }
    }, nr < 162)

    constexpr bool own = sl < 81;
    constexpr int U = p2_u(sl), ti = p2_tile(sl), pr = p2_og(sl), og = p2_o0(sl);
    if (own && U == 0) asm volatile("v_mfma_f32_16x16x32_bf16 %0, %1, %2, 0" : "=&v"(oc[ti][0]) : "v"(a[sl % 6]), "v"(ho[0][0]));
    else asm volatile("v_mfma_f32_16x16x32_bf16 %0, %1, %2, %0" : "+v"(oc[ti][0]) : "v"(a[sl % 6]), "v"(own ? ho[U & 1][0] : hp[U][0]));
    SB0();
    if (pr >= 0) outp(pr, og);
    if (own && U == 0) asm volatile("v_mfma_f32_16x16x32_bf16 %0, %1, %2, 0" : "=&v"(oc[ti][1]) : "v"(a[sl % 6]), "v"(ho[0][1]));
    else asm volatile("v_mfma_f32_16x16x32_bf16 %0, %1, %2, %0" : "+v"(oc[ti][1]) : "v"(a[sl % 6]), "v"(own ? ho[U & 1][1] : hp[U][1]));
    SB0();
    if (pr >= 0) outp(pr, og + 1);
    if (bar) {
      a[nr % 6] = *(lds_u32x4*)(ring_rd + ((nr / 12) % 3) * 24576 + (nr % 12) * 2048);
      K3_NEXT_SLOT()
    }
    K3_DMA(sl < 141)
    if (own && U < 8 && ti == 4) {
      ho[(U + 1) & 1][0] = *(lds_u32x4*)(hh_rd + (U + 1) * 1024); ho[(U + 1) & 1][1] = *(lds_u32x4*)(hh_rd + 9216 + (U + 1) * 1024);
      SB0();
    }
    if (sl == 23) { __builtin_amdgcn_raw_buffer_store_b32(9u, wsr, 0, fl_mine, 16); SB0(); }
    if (sl == 53) { asm volatile("global_load_dword %0, %1, %2 sc1" : "=&v"(pollv) : "v"(0u), "s"(fl_other) : "memory"); SB0(); }
    if constexpr (p2_ld(sl) >= 0) {
      constexpr int u = p2_ld(sl);
      asm volatile("global_load_dwordx4 %0, %1, %2 sc1" : "=&v"(xl[u]) : "v"(voff), "s"(xsrc + u * 1024) : "memory");
      SB0();
    }
    if constexpr (p2_wr(sl) >= 0) {
      constexpr int u = p2_wr(sl);
      asm volatile("" : "+v"(xl[u]));
      u32x2 lo = {xl[u][0], xl[u][1]}, hi = {xl[u][2], xl[u][3]};
      *(lds_u32x2*)(hx_wr + u * 1024) = lo; *(lds_u32x2*)(hx_wr + 9216 + u * 1024) = hi;
      SB0();
    }
    if constexpr (p2_rd(sl) >= 0) {
      constexpr int u = p2_rd(sl);
      hp[u][0] = *(lds_u32x4*)(hh_rd + u * 1024); hp[u][1] = *(lds_u32x4*)(hh_rd + 9216 + u * 1024);
      SB0();
    }
  });
  ST();
  MFMA_DRAIN();
#pragma unroll
  for (int g = 0; g < 10; ++g) outp(3, g);
  const int j = c >> 1, t16 = mb * 8 + j, f0 = t16 * 18 + 9 * (c & 1);
  const int row = t16 * 16 + (lane & 15), row8 = t16 * 16 + ((8 * wid + lane) & 15);
  const u32x4 hv = *(const u32x4*)(P.ws + OFF_HC + (size_t)(f0 + wid) * 1024 + lane * 16);
  const u32x4 hv8 = *(const u32x4*)(P.ws + OFF_HC + (size_t)(f0 + 8) * 1024 + (8 * wid + (lane & 7)) * 16);
  const float mean = ((const float*)(P.ws + OFF_MEANH))[row], mean8 = ((const float*)(P.ws + OFF_MEANH))[row8];
  WAITV(0);
  BAR();
  ST();
  if (wid == 0) {
    unsigned* cnt = (unsigned*)(P.ws + OFF_CNT + (mb * 2 + (c & 1)) * 128);
    if (lane == 0) __hip_atomic_fetch_add(cnt, 1u, __ATOMIC_RELAXED, __HIP_MEMORY_SCOPE_AGENT);
#pragma unroll 1
    for (int spin = 0; spin < (1 << 22); ++spin) {
      asm volatile("global_load_dword %0, %1, %2 sc1\n\ts_waitcnt vmcnt(0)" : "=&v"(pollv) : "v"(0u), "s"(cnt) : "memory");
      if (__builtin_amdgcn_readfirstlane(pollv) >= 8u) break;
      __builtin_amdgcn_s_sleep(1);
    }
  }
  BAR();
  ST();
  u32x4 pv[8], p8;
#pragma unroll
  for (int k = 0; k < 8; ++k) pv[k] = __builtin_amdgcn_raw_buffer_load_b128(wsr, voff, (int)OFF_PART + ((f0 + wid) * 8 + k) * 1024, 16);
  p8 = __builtin_amdgcn_raw_buffer_load_b128(wsr, (lane >> 3) * 1024 + (8 * wid + (lane & 7)) * 16, (int)OFF_PART + (f0 + 8) * 8192, 16);
  float o[8], o8[8];
#pragma unroll
  for (int k = 0; k < 4; ++k) { o[2 * k] = bflo(hv[k]) + mean; o[2 * k + 1] = bfhi(hv[k]) + mean; }
#pragma unroll
  for (int q = 0; q < 8; ++q)
#pragma unroll
    for (int k = 0; k < 4; ++k) { o[2 * k] += bflo(pv[q][k]); o[2 * k + 1] += bfhi(pv[q][k]); }
  {
    u32x4 w = {pk2(o[0], o[1]), pk2(o[2], o[3]), pk2(o[4], o[5]), pk2(o[6], o[7])};
    *(u32x4*)(P.out + (size_t)row * 1152 + (9 * (c & 1) + wid) * 64 + (lane >> 4) * 16) = w;
  }
#pragma unroll
  for (int k = 0; k < 4; ++k) { o8[2 * k] = bflo(p8[k]); o8[2 * k + 1] = bfhi(p8[k]); }
#pragma unroll
  for (int k = 0; k < 8; ++k) { o8[k] += __shfl_xor(o8[k], 8); o8[k] += __shfl_xor(o8[k], 16); o8[k] += __shfl_xor(o8[k], 32); }
  if (lane < 8) {
    u32x4 w;
#pragma unroll
    for (int k = 0; k < 4; ++k) w[k] = pk2(o8[2 * k] + bflo(hv8[k]) + mean8, o8[2 * k + 1] + bfhi(hv8[k]) + mean8);
    *(u32x4*)(P.out + (size_t)row8 * 1152 + (9 * (c & 1) + 8) * 64 + (wid >> 1) * 16) = w;
  }
}

constexpr int K0_IMG = 4608;
constexpr int K0_RED = 3 * K0_IMG;
constexpr int K0_LDS = K0_RED + 9 * 8 * 8;

DI float dpp_sum8(float v) {
  v += __shfl_xor(v, 1); v += __shfl_xor(v, 2); v += __shfl_xor(v, 4);
  return v;
}

__global__ __launch_bounds__(768) void k0_prep(Ptrs P) {
  extern __shared__ __attribute__((aligned(16))) char smem[];
  lds_char* lds = (lds_char*)smem;
  const int lane = threadIdx.x & 63, wid = __builtin_amdgcn_readfirstlane(threadIdx.x >> 6);
  const int b = blockIdx.x;
  ST_DECL; ST();
  if (wid < 9) {
    const int row = b * 8 + (lane >> 3), i = lane & 7;
    const u32x4 v = *(const u32x4*)(P.x + (size_t)row * 1152 + wid * 128 + i * 16);
    float e[8];
#pragma unroll
    for (int k = 0; k < 4; ++k) { e[2 * k] = bflo(v[k]); e[2 * k + 1] = bfhi(v[k]); }
    const float sum = dpp_sum8(((e[0] + e[1]) + (e[2] + e[3])) + ((e[4] + e[5]) + (e[6] + e[7])));
    const float lm = sum * (1.f / 64.f);
    float m2 = 0.f;
#pragma unroll
    for (int k = 0; k < 8; ++k) { const float d = e[k] - lm; m2 = __builtin_fmaf(d, d, m2); }
    m2 = dpp_sum8(m2);
    if (i == 0) *(lds_f32x2*)(lds + K0_RED + (wid * 8 + (lane >> 3)) * 8) = (f32x2){lm, m2};
    WAITL0();
    BAR();
    ST();
    f32x2 pr[9];
#pragma unroll
    for (int w = 0; w < 9; ++w) pr[w] = *(lds_f32x2*)(lds + K0_RED + (w * 8 + (lane >> 3)) * 8);
    float mean = 0.f;
#pragma unroll
    for (int w = 0; w < 9; ++w) mean += pr[w][0];
    mean *= (1.f / 9.f);
    float tot = 0.f;
#pragma unroll
    for (int w = 0; w < 9; ++w) { const float d = pr[w][0] - mean; tot += __builtin_fmaf(64.f * d, d, pr[w][1]); }
    const float rstd = rsqrtf(tot * (1.f / D_) + LN_EPS);
    u32x4 o;
#pragma unroll
    for (int k = 0; k < 4; ++k) o[k] = pk2((e[2 * k] - mean) * rstd, (e[2 * k + 1] - mean) * rstd);
    WS_ST16(P, (int)OFF_XN + ((row >> 5) * 36 * 64 + (row & 31) + 32 * (i & 1) + (4 * wid + (i >> 1)) * 64) * 16, o, ST_AUX);
    if (wid == 0 && i == 0) WS_ST4(P, OFF_MEANX + row * 4, __float_as_uint(mean), ST_AUX);
    return;
  }
  const int id = (wid - 9) * 256 + b;
  lds_char* img = lds + (wid - 9) * K0_IMG;
  const unsigned img_a = (unsigned)(uintptr_t)img;
  const int g = lane >> 4, q = (lane >> 2) & 3, p = lane & 3;
  unsigned pf_sink = 0;
  if (wid == 10 && b < 64) {
    unsigned long long pc, k1a;
    asm volatile("s_getpc_b64 %0" : "=s"(pc));
    asm volatile("s_getpc_b64 s[40:41]\n\ts_add_u32 s40, s40, _Z6k1_qkv4Ptrs@rel32@lo+4\n\ts_addc_u32 s41, s41, _Z6k1_qkv4Ptrs@rel32@hi+12\n\t"
                 "s_mov_b64 %0, s[40:41]" : "=s"(k1a) :: "s40", "s41", "scc");
    const char* pp = (const char*)k1a + ((b >> 3) * 64 + lane) * 128;
    pp = pp < (const char*)pc ? pp : (const char*)k1a;
    asm volatile("global_load_dword %0, %1, off" : "=&v"(pf_sink) : "v"(pp) : "memory");
  }
  if (id < 576) {
    const int s = id >> 4, h = (id >> 3) & 1, hp0 = (id & 7) * 2;
    const char* src = P.wqkv + (size_t)(16 * s + 8 * h) * 3456 + hp0 * 72;
#pragma unroll
    for (int it = 0; it < 4; ++it) {
      int e = it * 64 + lane; e = e < 216 ? e : 215;
      const int sgm = e / 9, ch = e - sgm * 9;
      dma16x1(src, sgm * 1152 + ch * 16, img_a + it * 1024);
    }
    BAR();
    ST();
    WAITV(0);
    asm volatile("" :: "v"(pf_sink));
    ST();
#pragma unroll
    for (int it = 0; it < 4; ++it) {
      int Q = 16 * it + 4 * g + p; Q = Q < 54 ? Q : 53;
      const int hl = Q >= 27 ? 1 : 0, sg = Q - 27 * hl;
      const int cb = sg < 4 ? 8 * sg : (sg < 8 ? 8 * sg + 8 : (sg == 8 ? 32 : (sg < 18 ? 8 * sg + 72 : 8 * sg + 144)));
      lds_char* a = img + q * 432 + cb + 72 * hl;
      u32x4 v = tr_frag(a, a + 4 * 432);
      const int o = it * 64 + lane, ol = o >= 108 ? 1 : 0, slot = o - 108 * ol;
      const int off = slot < 32 ? WQT_Q + s * 1024 + (h * 32 + slot) * 16 : s * 2432 + (h * 76 + slot - 32) * 16;
      if (o < 216) WS_ST16(P, OFF_WQT + (hp0 + ol) * (int)WQT_HP + off, v, ST_AUX);
    }
  } else if (id < 720) {
    const int j = id - 576, ks = j >> 2, cb = j & 3;
#pragma unroll
    for (int it = 0; it < 5; ++it) {
      int e = it * 64 + lane; e = e < 288 ? e : 287;
      const int kk = e / 18, ch = e - kk * 18, h = kk >> 3, jj = kk & 7;
      const int row = ks < 32 ? 18 * ks + (jj & 3) + 8 * (jj >> 2) + 4 * h : 18 * (8 * (ks - 32) + jj) + 16 + h;
      if (it < 4 || lane < 32) dma16x1(P.wo + cb * 288, row * 1152 + ch * 16, img_a + it * 1024);
    }
    BAR();
    WAITV(0);
#pragma unroll
    for (int it = 0; it < 5; ++it) {
      int G16 = 4 * it + g; G16 = G16 < 18 ? G16 : 17;
      const int h = G16 >= 9 ? 1 : 0, sg = G16 - 9 * h;
      lds_char* a = img + (8 * h + q) * 288 + (16 * sg + 4 * p) * 2;
      u32x4 v = tr_frag(a, a + 4 * 288);
      if (it < 4 || lane < 32) WS_ST16(P, OFF_WOT + cb * (int)WOT_CB + ks * 4672 + (h * 146 + 16 * sg + (lane & 15)) * 16, v, ST_AUX);
    }
  } else if (id < 756) {
    const int row = (id - 720) * 16 + (lane >> 2), i = lane & 3;
    const char* src = P.wo + (size_t)row * 1152 + i * 16;
    u32x4 v[18];
#pragma unroll
    for (int j = 0; j < 18; ++j) v[j] = *(const u32x4*)(src + j * 64);
    BAR();
    float s4[4] = {0.f, 0.f, 0.f, 0.f};
    const bf16x2 one2 = __builtin_bit_cast(bf16x2, 0x3f803f80u);
#pragma unroll
    for (int j = 0; j < 18; ++j)
#pragma unroll
      for (int k = 0; k < 4; ++k) { const unsigned t = v[j][k]; s4[k] = __builtin_amdgcn_fdot2_f32_bf16(__builtin_bit_cast(bf16x2, t), one2, s4[k], false); }
    float sum = (s4[0] + s4[1]) + (s4[2] + s4[3]);
    sum += __shfl_xor(sum, 1); sum += __shfl_xor(sum, 2);
    const float mean = sum * (1.f / D_);
    const int hd = row / 18, e = row % 18;
    const int ks = e < 16 ? hd : 32 + (hd >> 3), jj = e < 16 ? (e & 3) + 4 * (e >> 3) : (hd & 7), hh = e < 16 ? (e >> 2) & 1 : e - 16;
    const unsigned hi = pk2(mean, 0.f) & 0xffffu;
    const unsigned lo = pk2(mean - bflo(hi), 0.f) & 0xffffu;
    unsigned short* d = (unsigned short*)(P.ws + OFF_WOT + (size_t)i * WOT_CB + ks * 4672 + (hh * 146 + 144) * 16 + jj * 2);
    d[0] = (unsigned short)hi; d[8] = (unsigned short)lo;
  } else {
    const u32x4 z = {0u, 0u, 0u, 0u};
    for (int k = id - 756; k < 50; k += 12) WS_ST16(P, OFF_KV + k * 1024 + lane * 16, z, ST_AUX);
    for (int k = id - 756; k < 36; k += 12) WS_ST16(P, OFF_FLAG + k * 1024 + lane * 16, z, ST_AUX);
    BAR();
  }
}

extern "C" void kernel_launch(void* const* d_in, const int* in_sizes, int n_in, void* d_out, int out_size, void* d_ws,
                              size_t ws_size, hipStream_t stream) {
  Ptrs P;
  P.x = (const char*)d_in[0]; P.wqkv = (const char*)d_in[1]; P.wo = (const char*)d_in[2];
  P.wg = (const char*)d_in[4]; P.wu = (const char*)d_in[5]; P.wd = (const char*)d_in[6];
  P.ws = (char*)d_ws; P.out = (char*)d_out;
  (void)hipFuncSetAttribute((const void*)k1_qkv, hipFuncAttributeMaxDynamicSharedMemorySize, K1_LDS);
  (void)hipFuncSetAttribute((const void*)k2_attn, hipFuncAttributeMaxDynamicSharedMemorySize, K2_LDS);
  (void)hipFuncSetAttribute((const void*)k3_ffn, hipFuncAttributeMaxDynamicSharedMemorySize, K3_LDS);
  k0_prep<<<256, 768, K0_LDS, stream>>>(P);
  k1_qkv<<<256, 512, K1_LDS, stream>>>(P);
  k2_attn<<<256, 768, K2_LDS, stream>>>(P);
  k3_ffn<<<256, 512, K3_LDS, stream>>>(P);
}
```

```cpp
#include <hip/hip_runtime.h>
#include <hip/hip_bf16.h>
#include <stdint.h>
#include <type_traits>

typedef __attribute__((ext_vector_type(8))) short bf16x8;
typedef __attribute__((ext_vector_type(4))) short s16x4;
typedef __attribute__((ext_vector_type(16))) float f32x16;
typedef __attribute__((ext_vector_type(4))) float f32x4;
typedef __attribute__((ext_vector_type(2))) float f32x2;
typedef __attribute__((ext_vector_type(2))) __bf16 bf16x2;
typedef __attribute__((ext_vector_type(4))) unsigned u32x4;
typedef __attribute__((ext_vector_type(2))) unsigned u32x2;
typedef __attribute__((address_space(3))) char lds_char;
typedef __attribute__((address_space(3))) s16x4 lds_s16x4;
typedef __attribute__((address_space(3))) u32x4 lds_u32x4;
typedef __attribute__((address_space(3))) u32x2 lds_u32x2;
typedef __attribute__((address_space(3))) f32x4 lds_f32x4;
typedef __attribute__((address_space(3))) float lds_float;
typedef __attribute__((address_space(3))) f32x2 lds_f32x2;
typedef __attribute__((address_space(3))) unsigned lds_uint;

#define DI __device__ __forceinline__
#define MFMA32(a, b, c) __builtin_amdgcn_mfma_f32_32x32x16_bf16((a), (b), (c), 0, 0, 0)
#define MFMA16(a, b, c) __builtin_amdgcn_mfma_f32_16x16x32_bf16((a), (b), (c), 0, 0, 0)
#define WAITV(n) asm volatile("s_waitcnt vmcnt(" #n ")" ::: "memory")
#define WAITL0() asm volatile("s_waitcnt lgkmcnt(0)" ::: "memory")
#define BAR() asm volatile("s_barrier" ::: "memory")
#define SB0() __builtin_amdgcn_sched_barrier(0)

constexpr int S_ = 2048, D_ = 576, F_ = 4608;
constexpr float LN_EPS = 1e-5f;

constexpr size_t FLB = (size_t)S_ * D_ * 2;
constexpr size_t WQT_HP = 36 * 3456;
constexpr int WQT_Q = 36 * 2432;
constexpr size_t WOT_CB = 36 * 4672;
constexpr size_t WF_CH = 972 * 1024;
constexpr size_t OFF_XN = 0;
constexpr size_t OFF_PQ = OFF_XN + FLB;
constexpr size_t OFF_HC = OFF_PQ + FLB;
constexpr size_t OFF_MEANX = OFF_HC + FLB;
constexpr size_t OFF_MEANH = OFF_MEANX + 8192;
constexpr size_t OFF_SSQ = OFF_MEANH + 8192;
constexpr size_t OFF_WBAR = OFF_SSQ + 20 * 8192;
constexpr size_t OFF_KV = OFF_WBAR + 4096;
constexpr size_t OFF_WQT = OFF_KV + 53248;
constexpr size_t OFF_WOT = OFF_WQT + 16 * WQT_HP + 16384;
constexpr size_t OFF_WF = OFF_WOT + 4 * WOT_CB + 4096;
constexpr size_t OFF_PART = OFF_WF + 16 * WF_CH + 65536;
constexpr size_t OFF_XH = OFF_PART + 8 * FLB;
constexpr size_t OFF_FLAG = OFF_XH + 256 * 73728;
constexpr size_t OFF_CNT = OFF_FLAG + 256 * 128;
constexpr size_t WS_END = OFF_CNT + 32 * 128;

#define ST_DECL
#define ST()
#define ST_OUT(k, slot)
DI unsigned pk2(float a, float b) {
  f32x2 v = {a, b};
  bf16x2 r = __builtin_convertvector(v, bf16x2);
  return __builtin_bit_cast(unsigned, r);
}
DI float bflo(unsigned u) { return __uint_as_float(u << 16); }
DI float bfhi(unsigned u) { return __uint_as_float(u & 0xffff0000u); }
DI float phi(float t) {
  const float e = __builtin_amdgcn_fmed3f(__builtin_amdgcn_exp2f(t * 1.4426950408889634f), 0.f, 1.f);
  return (__builtin_amdgcn_fmed3f(t, 0.f, __builtin_inff()) + 1e-6f) + e;
}
DI bf16x8 as_frag(u32x4 v) { return __builtin_bit_cast(bf16x8, v); }

DI void dma16(const void* gsrc, unsigned lds_dst) {
  unsigned keep;
  asm volatile("s_mov_b32 %0, m0\n\ts_mov_b32 m0, %2\n\ts_nop 0\n\tglobal_load_lds_dwordx4 %1, off\n\ts_mov_b32 m0, %0"
               : "=&s"(keep) : "v"(gsrc), "s"(lds_dst) : "memory");
}
DI void dma16x3(const void* sbase, unsigned voff, unsigned lds_dst) {
  unsigned keep;
  asm volatile("s_mov_b32 %0, m0\n\ts_mov_b32 m0, %3\n\ts_nop 0\n\t"
               "global_load_lds_dwordx4 %1, %2\n\t"
               "global_load_lds_dwordx4 %1, %2 offset:1024\n\t"
               "global_load_lds_dwordx4 %1, %2 offset:2048\n\t"
               "s_mov_b32 m0, %0"
               : "=&s"(keep) : "v"(voff), "s"(sbase), "s"(lds_dst) : "memory");
}
DI void dma16x1(const void* sbase, unsigned voff, unsigned lds_dst) {
  unsigned keep;
  asm volatile("s_mov_b32 %0, m0\n\ts_mov_b32 m0, %3\n\ts_nop 0\n\t"
               "global_load_lds_dwordx4 %1, %2\n\t"
               "s_mov_b32 m0, %0"
               : "=&s"(keep) : "v"(voff), "s"(sbase), "s"(lds_dst) : "memory");
}
DI u32x4 tr_frag(lds_char* p0, lds_char* p1) {
  s16x4 lo = __builtin_amdgcn_ds_read_tr16_b64_v4i16((lds_s16x4*)p0);
  s16x4 hi = __builtin_amdgcn_ds_read_tr16_b64_v4i16((lds_s16x4*)p1);
  u32x2 a = __builtin_bit_cast(u32x2, lo), b = __builtin_bit_cast(u32x2, hi);
  u32x4 r = {a[0], a[1], b[0], b[1]};
  return r;
}

#define ST_AUX 16
#define WF_AUX 16
#define WS_RSRC(P) __builtin_amdgcn_make_buffer_rsrc((P).ws, 0, (int)WS_END, 0x00020000)
#define WS_ST16(P, off, v, aux) __builtin_amdgcn_raw_buffer_store_b128((v), WS_RSRC(P), (int)(off), 0, (aux))
#define WS_ST8(P, off, v, aux) __builtin_amdgcn_raw_buffer_store_b64((v), WS_RSRC(P), (int)(off), 0, (aux))
#define WS_ST4(P, off, v, aux) __builtin_amdgcn_raw_buffer_store_b32((v), WS_RSRC(P), (int)(off), 0, (aux))
struct Ptrs {
  const char* x; const char* wqkv; const char* wo; const char* wg; const char* wu; const char* wd;
  char* ws; char* out;
};

constexpr int ITEM_LDS = 9216;
constexpr int SUB_PER_XCD = 486;

DI void sub_load(const Ptrs& P, int x, int j, int lane, unsigned lds_base) {
  const int rl = lane >> 3, cp = lane & 7;
  if (j < 324) {
    const int mat = j / 162, r = j % 162, S = r / 9, cb = r % 9;
    const char* base = (mat ? P.wu : P.wg) + (size_t)(32 * S) * (F_ * 2) + (576 * x + 64 * cb) * 2;
#pragma unroll
    for (int it = 0; it < 4; ++it) {
      const int X = ((rl >> 1) & 1) | ((it & 1) << 1);
      dma16x1(base + (size_t)(8 * it) * (F_ * 2), rl * (F_ * 2) + (((cp >> 1) ^ X) * 32) + (cp & 1) * 16, lds_base + it * 1024);
    }
  } else {
    const int jj = j - 324, Ug = jj / 9, db = jj % 9;
    const char* base = P.wd + (size_t)(576 * x + 32 * Ug) * (D_ * 2) + db * 128;
#pragma unroll
    for (int it = 0; it < 4; ++it)
      dma16x1(base + (8 * it) * (D_ * 2), rl * (D_ * 2) + (((cp >> 1) ^ ((rl >> 1) & 3)) * 32) + (cp & 1) * 16, lds_base + it * 1024);
  }
}
DI void sub_store(const Ptrs& P, int x, int j, int lane, lds_char* img) {
  const int g = lane >> 4, q = (lane >> 2) & 3, p = lane & 3;
  const int wf = (int)OFF_WF + lane * 16;
  const int X = ((q >> 1) & 1) | ((g & 1) << 1);
  if (j < 324) {
    const int mat = j / 162, r = j % 162, S = r / 9, cb = r % 9;
#pragma unroll
    for (int u4 = 0; u4 < 4; ++u4) {
      lds_char* a = img + (8 * g + q) * 128 + ((u4 ^ X) * 32) + 8 * p;
      u32x4 v = tr_frag(a, a + 512);
      const int ug = 4 * cb + u4, cc = ug >= 18 ? 1 : 0, u = ug - 18 * cc, U = u >> 1;
      const int n = U < 8 ? (U >> 1) * 72 + S * 4 + mat * 2 + (U & 1) : 288 + 2 * S + mat;
      WS_ST16(P, wf + (2 * x + cc) * (int)WF_CH + (2 * n + (u & 1)) * 1024, v, WF_AUX);
    }
  } else {
    const int jj = j - 324, Ug = jj / 9, dbk = jj % 9;
    const int cs = Ug >= 9 ? 1 : 0, U = Ug - 9 * cs;
#pragma unroll
    for (int k = 0; k < 4; ++k) {
      lds_char* a = img + (4 * g + q) * 128 + ((k ^ X) * 32) + 8 * p;
      u32x4 v = tr_frag(a, a + 2048);
      const int T = 4 * dbk + k, pd = T >= 18 ? 1 : 0, Qr = (T - 18 * pd) >> 1, t01 = T & 1;
      const bool sg = Qr == (pd ? 0 : 8);
      const int db = sg ? 4 : (Qr - pd) >> 1, fj = sg ? t01 : (Qr - pd) & 1;
      int s2;
      if (pd == cs) s2 = 9 * U + (sg ? 8 : 2 * db + t01);
      else if (db == 2 || db == 3) s2 = 126 + (db - 2) * 18 + 2 * U + t01;
      else if (U < 6) s2 = 81 + 5 * U + (sg ? 0 : 1 + 2 * db + t01);
      else if (db == 1) s2 = 120 + 2 * (U - 6) + t01;
      else s2 = 111 + 3 * (U - 6) + (sg ? 0 : 1 + t01);
      WS_ST16(P, wf + (2 * x + pd) * (int)WF_CH + (2 * (324 + s2) + fj) * 1024, v, WF_AUX);
    }
  }
}

#define CODE_PF 1
#define FRONT_PF 1
#define N0_SUB 0
#define N1_SUB 256
constexpr int K0_ITEMS = 704 + 8 * (N0_SUB / 2);

constexpr int K1_W = 0;
constexpr int K1_ITEM = 124 * 1024;
constexpr int K1_CNT = K1_ITEM + 32768;
constexpr int K1_LDS = K1_CNT + 64;
constexpr int KVP = 192;

__global__ __launch_bounds__(512, 2) void k1_qkv(Ptrs P) {
  extern __shared__ __attribute__((aligned(16))) char smem[];
  lds_char* lds = (lds_char*)smem;
  const unsigned lds_a = (unsigned)(uintptr_t)lds;
  const int lane = threadIdx.x & 63, wid = __builtin_amdgcn_readfirstlane(threadIdx.x >> 6);
  const int b = blockIdx.x, hp = ((b & 7) >> 1) * 4 + ((b >> 3) & 3), mb = (b & 1) * 8 + (b >> 5);
  const int r = lane & 31, h = lane >> 5;
  volatile lds_uint* cnt = (volatile lds_uint*)(lds + K1_CNT);
  ST_DECL; ST();
  if (wid >= 4) {
    const int w = wid - 4;
    const char* wsrc = P.ws + OFF_WQT + (size_t)hp * WQT_HP + w * 1024;
    const unsigned wdst = lds_a + K1_W + w * 1024;
    const int x = b & 7, jl = ((b >> 3) * 2 + (w & 1)) * 4, j0 = N0_SUB + jl;
    const bool on = jl < N1_SUB;
    if (w == 0 && lane == 0) *cnt = 0u;
#pragma unroll
    for (int i = 0; i < 14; ++i) dma16x1(wsrc + i * 4096, lane * 16, wdst + i * 4096);
    WAITV(8); WAITL0(); BAR();
    ST();
#pragma unroll
    for (int i = 14; i < 20; ++i) dma16x1(wsrc + i * 4096, lane * 16, wdst + i * 4096);
    WAITV(9); BAR();
#pragma unroll
    for (int i = 20; i < 26; ++i) dma16x1(wsrc + i * 4096, lane * 16, wdst + i * 4096);
    WAITV(9); BAR();
#pragma unroll
    for (int i = 26; i < 31; ++i) dma16x1(wsrc + i * 4096, lane * 16, wdst + i * 4096);
    WAITV(9); BAR();
    if (w >= 2) {
#pragma unroll
      for (int i = 0; i < 4; ++i) sub_load(P, x, j0 + i, lane, lds_a + K1_ITEM + (w & 1) * 16384 + i * 4096);
      WAITV(16);
      BAR();
      WAITV(0);
#pragma unroll
      for (int i = 0; i < 4; ++i) sub_store(P, x, j0 + i, lane, lds + K1_ITEM + (w & 1) * 16384 + i * 4096);
      return;
    }
    WAITV(0); BAR();
    ST();
    const int g = lane >> 4, q = (lane >> 2) & 3, p = lane & 3, op = 4 * (g & 1) + p;
    const int vq = op >= 5 ? 19 : (w ? 14 : 10) + op;
    const int kq = (w ? 5 : 1) + op;
    lds_char* va = lds + (8 * (g >> 1) + q) * KVP + vq * 8;
    lds_char* ka = lds + (8 * (g >> 1) + q) * KVP + kq * 8;
    while (__builtin_amdgcn_readfirstlane(*cnt) < 4u) __builtin_amdgcn_s_sleep(1);
    asm volatile("" ::: "memory");
    ST();
    f32x16 kv;
#pragma unroll
    for (int i = 0; i < 16; ++i) kv[i] = 0.f;
#pragma unroll
    for (int ks = 0; ks < 8; ++ks) {
      u32x4 a = tr_frag(va + ks * 16 * KVP, va + ks * 16 * KVP + 4 * KVP);
      u32x4 bb = tr_frag(ka + ks * 16 * KVP, ka + ks * 16 * KVP + 4 * KVP);
      kv = MFMA32(as_frag(a), as_frag(bb), kv);
    }
    const int d = r - 2 * w;
    const int dd = w ? (d >= 2 ? d - 2 : 16 + d) : d;
    float* dst = (float*)(P.ws + OFF_KV) + (2 * hp + w) * 400 + dd;
    const bool dok = d >= 0 && d < 18;
#pragma unroll
    for (int i = 0; i < 12; ++i) {
      const int row = (i & 3) + 8 * (i >> 2) + 4 * h;
      const int e = row == 20 ? 18 : row - 2 * w;
      if (dok && (row == 20 || (e >= 0 && e < 18 && row < 20))) atomicAdd(dst + e * 20, kv[i]);
    }
    return;
  }
  const int mt = mb * 4 + wid;
  u32x4 xf[36];
  const char* xsrc = P.ws + OFF_XN + (size_t)mt * 36864 + lane * 16;
#pragma unroll
  for (int s = 0; s < 18; ++s) xf[s] = *(const u32x4*)(xsrc + s * 1024);
  f32x16 acc[3], accq;
#pragma unroll
  for (int i = 0; i < 16; ++i) { acc[0][i] = 0.f; acc[1][i] = 0.f; acc[2][i] = 0.f; accq[i] = 0.f; }
  lds_char* wa = lds + K1_W + (h * 76 + r) * 16;
  lds_char* wa2 = lds + K1_W + (h * 76 + 64 + (r < 12 ? r : 11)) * 16;
  u32x4 af[2][3];
#pragma unroll
  for (int s = 0; s < 36; ++s) {
    if (s % 9 == 0) {
      BAR();
      if (s < 18) {
#pragma unroll
        for (int k = s + 18; k < s + 27; ++k) xf[k] = *(const u32x4*)(xsrc + k * 1024);
      }
      af[s & 1][0] = *(lds_u32x4*)(wa + s * 2432); af[s & 1][1] = *(lds_u32x4*)(wa + s * 2432 + 512); af[s & 1][2] = *(lds_u32x4*)(wa2 + s * 2432);
    }
    if (s % 9 != 8) {
      af[(s + 1) & 1][0] = *(lds_u32x4*)(wa + (s + 1) * 2432); af[(s + 1) & 1][1] = *(lds_u32x4*)(wa + (s + 1) * 2432 + 512);
      af[(s + 1) & 1][2] = *(lds_u32x4*)(wa2 + (s + 1) * 2432);
    }
    SB0();
#pragma unroll
    for (int t = 0; t < 3; ++t) acc[t] = MFMA32(as_frag(af[s & 1][t]), as_frag(xf[s]), acc[t]);
    SB0();
  }
  ST();
  BAR();
  const int pq = (int)OFF_PQ + mt * 36864;
  lds_char* kvrow = lds + (wid * 32 + r) * KVP;
  lds_char* wq = lds + K1_W + WQT_Q + lane * 16;
  u32x4 aq[2][4];
  u32x2 qrem;
#pragma unroll
  for (int i = 0; i < 4; ++i) aq[0][i] = *(lds_u32x4*)(wq + i * 1024);
#pragma unroll
  for (int gi = 0; gi < 10; ++gi) {
    if (gi < 8) {
#pragma unroll
      for (int i = 0; i < 4; ++i) aq[(gi + 1) & 1][i] = *(lds_u32x4*)(wq + (4 * gi + 4 + i) * 1024);
    }
    if (gi < 9) {
#pragma unroll
      for (int i = 0; i < 4; ++i) accq = MFMA32(as_frag(aq[gi & 1][i]), as_frag(xf[4 * gi + i]), accq);
    }
    const int t = gi >> 2, g = gi & 3, qd0 = 2 * gi;
    u32x2 o;
    if (qd0 < 10) {
      o[0] = pk2(phi(acc[t][4 * g]), phi(acc[t][4 * g + 1])); o[1] = pk2(phi(acc[t][4 * g + 2]), phi(acc[t][4 * g + 3]));
    } else {
      o[0] = pk2(acc[t][4 * g], acc[t][4 * g + 1]); o[1] = pk2(acc[t][4 * g + 2], acc[t][4 * g + 3]);
    }
    if (qd0 == 18 && h == 1) { o[0] = 0x3f803f80u; o[1] = 0x3f803f80u; }
    *(lds_u32x2*)(kvrow + (qd0 + h) * 8) = o;
    if (qd0 == 0) qrem = o;
    if (gi < 5) {
#pragma unroll
      for (int i = 0; i < 4; ++i) {
        __builtin_amdgcn_sched_group_barrier(0x100, 1, 0);
        __builtin_amdgcn_sched_group_barrier(0x008, 1, 0);
        __builtin_amdgcn_sched_group_barrier(0x002, 7, 0);
      }
    }
    SB0();
    if (gi == 4) {
#pragma unroll
      for (int g2 = 5; g2 < 10; ++g2) {
        const int t2 = g2 >> 2, gg = g2 & 3;
        u32x2 o2 = {pk2(acc[t2][4 * gg], acc[t2][4 * gg + 1]), pk2(acc[t2][4 * gg + 2], acc[t2][4 * gg + 3])};
        if (g2 == 9 && h == 1) { o2[0] = 0x3f803f80u; o2[1] = 0x3f803f80u; }
        *(lds_u32x2*)(kvrow + (2 * g2 + h) * 8) = o2;
      }
      WAITL0();
      if (lane == 0) __hip_atomic_fetch_add((lds_uint*)cnt, 1u, __ATOMIC_RELAXED, __HIP_MEMORY_SCOPE_WORKGROUP);
      ST();
#pragma unroll
      for (int g3 = 5; g3 < 9; ++g3) {
        if (g3 < 8) {
#pragma unroll
          for (int i = 0; i < 4; ++i) aq[(g3 + 1) & 1][i] = *(lds_u32x4*)(wq + (4 * g3 + 4 + i) * 1024);
        }
        SB0();
#pragma unroll
        for (int i = 0; i < 4; ++i) accq = MFMA32(as_frag(aq[g3 & 1][i]), as_frag(xf[4 * g3 + i]), accq);
        SB0();
      }
      break;
    }
  }
  ST();
#pragma unroll
  for (int e = 0; e < 2; ++e) {
    u32x4 o;
#pragma unroll
    for (int k = 0; k < 4; ++k) o[k] = pk2(phi(accq[8 * e + 2 * k]), phi(accq[8 * e + 2 * k + 1]));
    WS_ST16(P, pq + (2 * hp + e) * 1024 + lane * 16, o, ST_AUX);
  }
  if (h == 0) WS_ST8(P, pq + (32 + (hp >> 2)) * 1024 + (r + 32 * ((hp >> 1) & 1)) * 16 + 8 * (hp & 1), qrem, ST_AUX);
}

constexpr int K2_O = 0;
constexpr int K2_RED = 36864;
constexpr int K2_MEAN = K2_RED + 32768;
constexpr int K2_ITEM = K2_MEAN + 2048;
constexpr int K2_LDS = K2_ITEM + 4 * ITEM_LDS;

__global__ __launch_bounds__(768, 3) void k2_attn(Ptrs P) {
  extern __shared__ __attribute__((aligned(16))) char smem[];
  lds_char* lds = (lds_char*)smem;
  const unsigned lds_a = (unsigned)(uintptr_t)lds;
  const int lane = threadIdx.x & 63, wid = __builtin_amdgcn_readfirstlane(threadIdx.x >> 6);
  const int b = blockIdx.x, cb = (b >> 3) & 3, mt = (b & 7) * 8 + (b >> 5);
  const int r = lane & 31, h = lane >> 5;
  ST_DECL; ST();
  if (wid >= 8) {
    const int w = wid - 8;
    const int x = b & 7, j0 = N0_SUB + N1_SUB + ((b >> 3) * 4 + w) * 2;
    const bool on = j0 < SUB_PER_XCD;
    if (on) {
      sub_load(P, x, j0, lane, lds_a + K2_ITEM + w * ITEM_LDS);
      sub_load(P, x, j0 + 1, lane, lds_a + K2_ITEM + w * ITEM_LDS + 4096);
    }
    ST();
    BAR();
    ST();
    WAITV(0);
    ST();
    if (on) {
      sub_store(P, x, j0, lane, lds + K2_ITEM + w * ITEM_LDS);
      sub_store(P, x, j0 + 1, lane, lds + K2_ITEM + w * ITEM_LDS + 4096);
    }
    ST();
    BAR();
    return;
  }
  const int t = wid & 3, kh = wid >> 2;
  const float* kvb = (const float*)(P.ws + OFF_KV) + (4 * wid) * 400;
  const int e = r < 16 ? r : (r == 16 ? 16 : (r == 20 ? 17 : 18));
  const bool rok = r <= 17 || r == 20 || r == 21;
  f32x4 k0[4], k1[4]; f32x2 k2[4];
#pragma unroll
  for (int i = 0; i < 4; ++i) {
    const float* kp = kvb + i * 400 + e * 20;
    k0[i] = *(const f32x4*)(kp + 4 * h); k1[i] = *(const f32x4*)(kp + 8 + 4 * h); k2[i] = *(const f32x2*)(kp + 16);
  }
  const char* pq = P.ws + OFF_PQ + (size_t)mt * 36864 + lane * 16;
  u32x4 qf[4];
#pragma unroll
  for (int i = 0; i < 4; ++i) qf[i] = *(const u32x4*)(pq + (4 * wid + i) * 1024);
  const u32x4 qr = *(const u32x4*)(pq + (32 + (wid >> 1)) * 1024);
  const char* wo = P.ws + OFF_WOT + (size_t)cb * WOT_CB + h * 2336;
  u32x4 af[18], a4[5];
#pragma unroll
  for (int i = 0; i < 13; ++i) af[i] = *(const u32x4*)(wo + (18 * kh + i) * 4672 + (32 * t + r) * 16);
  const int k4 = wid < 4 ? 5 * wid : 20 + 4 * (wid - 4), n4 = wid < 4 ? 5 : 4;
  float orem[4];
#pragma unroll
  for (int i = 0; i < 4; ++i) {
    u32x4 a1 = {pk2(k0[i][0], k0[i][1]), pk2(k0[i][2], k0[i][3]), pk2(k1[i][0], k1[i][1]), pk2(k1[i][2], k1[i][3])};
    u32x4 a2 = {0u, 0u, 0u, 0u};
    a2[i] = (h == (wid & 1)) ? pk2(k2[i][0], k2[i][1]) : 0u;
    if (!rok) { a1 = (u32x4){0u, 0u, 0u, 0u}; a2[i] = 0u; }
    f32x16 c;
#pragma unroll
    for (int j = 0; j < 16; ++j) c[j] = 0.f;
    c = MFMA32(as_frag(a1), as_frag(qf[i]), c);
    c = MFMA32(as_frag(a2), as_frag(qr), c);
    const float inv = __builtin_amdgcn_rcpf(c[9] + 1e-6f);
    float o[9];
#pragma unroll
    for (int j = 0; j < 9; ++j) o[j] = c[j] * inv;
    u32x4 of = {pk2(o[0], o[1]), pk2(o[2], o[3]), pk2(o[4], o[5]), pk2(o[6], o[7])};
    *(lds_u32x4*)(lds + K2_O + (4 * wid + i) * 1024 + lane * 16) = of;
    orem[i] = o[8];
    if (i == 1) SB0();
  }
  {
    u32x2 o2 = {pk2(orem[0], orem[1]), pk2(orem[2], orem[3])};
    *(lds_u32x2*)(lds + K2_O + (32 + (wid >> 1)) * 1024 + lane * 16 + 8 * (wid & 1)) = o2;
  }
  SB0();
  ST();
  WAITL0();
  BAR();
#pragma unroll
  for (int i = 13; i < 18; ++i) af[i] = *(const u32x4*)(wo + (18 * kh + i) * 4672 + (32 * t + r) * 16);
#pragma unroll
  for (int i = 0; i < 5; ++i) a4[i] = *(const u32x4*)(wo + (k4 + (i < n4 ? i : 0)) * 4672 + (128 + (r < 17 ? r : 17)) * 16);
  const int row = mt * 32 + r;
  const int s0 = cb * 9 + 2 * (wid < 4 ? wid : 4);
  const u32x4 xv0 = *(const u32x4*)(P.x + (size_t)row * 1152 + s0 * 32 + h * 16);
  const u32x4 xv1 = *(const u32x4*)(P.x + (size_t)row * 1152 + (wid < 4 ? s0 + 1 : s0) * 32 + h * 16);
  float mean = ((const float*)(P.ws + OFF_MEANX))[row];
  SB0();
  ST();
  f32x16 acc, acc4;
#pragma unroll
  for (int j = 0; j < 16; ++j) { acc[j] = 0.f; acc4[j] = 0.f; }
  lds_char* ob = lds + K2_O + lane * 16;
#pragma unroll
  for (int i = 0; i < 18; ++i) {
    acc = MFMA32(as_frag(af[i]), as_frag(*(lds_u32x4*)(ob + (18 * kh + i) * 1024)), acc);
    if (i % 6 == 5) SB0();
  }
  if (n4 == 4) a4[4] = (u32x4){0u, 0u, 0u, 0u};
#pragma unroll
  for (int i = 0; i < 5; ++i) acc4 = MFMA32(as_frag(a4[i]), as_frag(*(lds_u32x4*)(ob + (k4 + (i < n4 ? i : 0)) * 1024)), acc4);
  {
    *(lds_float*)(lds + K2_MEAN + (wid * 64 + lane) * 4) = acc4[8] + acc4[9];
    lds_char* p4 = lds + K2_RED + 16384 + wid * 2048 + lane * 16;
    *(lds_f32x4*)(p4) = (f32x4){acc4[0], acc4[1], acc4[2], acc4[3]};
    *(lds_f32x4*)(p4 + 1024) = (f32x4){acc4[4], acc4[5], acc4[6], acc4[7]};
    if (kh == 1) {
      lds_char* pr = lds + K2_RED + t * 4096 + lane * 16;
#pragma unroll
      for (int g = 0; g < 4; ++g) *(lds_f32x4*)(pr + g * 1024) = (f32x4){acc[4 * g], acc[4 * g + 1], acc[4 * g + 2], acc[4 * g + 3]};
    }
  }
  WAITL0();
  BAR();
  if (wid > 4) return;
  ST();
#pragma unroll
  for (int w = 0; w < 8; ++w) mean += *(lds_float*)(lds + K2_MEAN + (w * 64 + r) * 4);
  if (wid < 4) {
    lds_char* pr = lds + K2_RED + t * 4096 + lane * 16;
#pragma unroll
    for (int g = 0; g < 4; ++g) {
      f32x4 v = *(lds_f32x4*)(pr + g * 1024);
#pragma unroll
      for (int j = 0; j < 4; ++j) acc[4 * g + j] += v[j];
    }
  } else {
#pragma unroll
    for (int j = 0; j < 16; ++j) acc[j] = 0.f;
#pragma unroll
    for (int w = 0; w < 8; ++w) {
      lds_char* p4 = lds + K2_RED + 16384 + w * 2048 + lane * 16;
      f32x4 v0 = *(lds_f32x4*)(p4), v1 = *(lds_f32x4*)(p4 + 1024);
#pragma unroll
      for (int j = 0; j < 4; ++j) { acc[j] += v0[j]; acc[4 + j] += v1[j]; }
    }
  }
  float ssq = 0.f;
#pragma unroll
  for (int i = 0; i < 2; ++i) {
    if (wid == 4 && i == 1) break;
    float v[8];
#pragma unroll
    for (int j = 0; j < 4; ++j) {
      auto sw = __builtin_amdgcn_permlane32_swap(__float_as_uint(acc[8 * i + j]), __float_as_uint(acc[8 * i + 4 + j]), false, false);
      v[j] = __uint_as_float(sw[0]); v[4 + j] = __uint_as_float(sw[1]);
    }
    const u32x4 xv = i ? xv1 : xv0;
    u32x4 o;
#pragma unroll
    for (int k = 0; k < 4; ++k) {
      const float a = v[2 * k] + bflo(xv[k]) - mean, c = v[2 * k + 1] + bfhi(xv[k]) - mean;
      ssq += a * a + c * c;
      o[k] = pk2(a, c);
    }
    const int d8 = 2 * (s0 + i) + h;
    WS_ST16(P, OFF_HC + (((row >> 4) * 18 + (d8 >> 2)) * 64 + (row & 15) + 16 * (d8 & 3)) * 16, o, ST_AUX);
  }
  ssq += __shfl_xor(ssq, 32);
  if (h == 0) {
    WS_ST4(P, OFF_SSQ + ((mt * 20 + cb * 5 + wid) * 32 + r) * 4, __float_as_uint(ssq), ST_AUX);
    if (cb == 0 && wid == 0) WS_ST4(P, OFF_MEANH + row * 4, __float_as_uint(mean), ST_AUX);
  }
}

constexpr int K3_RING = 0;
constexpr int K3_HH = 73728;
constexpr int K3_SSQ = K3_HH + 73728;
constexpr int K3_LDS = K3_SSQ + 4 * 3072;
#define DSTEP 2
#define XOFF 1
#define DMA0 1
#define SLOW0 7
constexpr int NS = 486, PH = 288, P2 = 324;

struct Sch {
  signed char own[162] = {}, u[162] = {}, db[162] = {}, t01[162] = {};
  bool bar[NS] = {};
  signed char dslot[NS] = {}, dk[NS] = {}, prio[NS] = {};
  signed char e0[NS] = {}, ne0[NS] = {}, ne1[NS] = {}, eg[NS] = {};
  signed char og[NS] = {}, o0[NS] = {};
  signed char xrd[NS] = {}, cbar[NS] = {}, xst[NS] = {}, ost[NS] = {}, xop[NS] = {};
  signed char flag[NS] = {}, fbar[NS] = {}, poll[NS] = {}, chk[NS] = {}, ld[NS] = {}, wr[NS] = {}, rd[NS] = {};
  signed char wl[NS] = {}, wf[NS] = {};
  bool ok = true;
};
constexpr int rel12(int n) { return (n + 3) % 12; }
constexpr Sch make_sch() {
  Sch s;
  int n2 = 0;
  auto put = [&](int own, int u, int db) {
    for (int t = 0; t < (db == 4 ? 1 : 2); ++t) { s.own[n2] = own; s.u[n2] = u; s.db[n2] = db; s.t01[n2] = t; ++n2; }
  };
  for (int u = 0; u < 9; ++u) for (int d = 0; d < 5; ++d) put(1, u, d);
  for (int u = 0; u < 6; ++u) { put(0, u, 4); put(0, u, 0); put(0, u, 1); }
  for (int u = 6; u < 9; ++u) { put(0, u, 4); put(0, u, 0); }
  for (int u = 6; u < 9; ++u) put(0, u, 1);
  for (int d = 2; d < 4; ++d) for (int u = 0; u < 9; ++u) put(0, u, d);
  for (int n = 0; n < NS; ++n) {
    s.bar[n] = rel12(n) == 0 && n + 3 < NS;
    s.dslot[n] = -1; s.prio[n] = -1; s.eg[n] = -1; s.og[n] = -1; s.flag[n] = -1; s.fbar[n] = -1; s.poll[n] = 0; s.chk[n] = -1;
    s.ld[n] = -1; s.wr[n] = -1; s.rd[n] = -1; s.wl[n] = -1; s.wf[n] = -1; s.xst[n] = -1; s.ost[n] = -1; s.xrd[n] = -1; s.cbar[n] = -1;
    const int r = rel12(n), tgt = (n + 3) / 12 + 2;
    if (n > 9 && r >= DMA0 && (r - DMA0) % DSTEP == 0 && (r - DMA0) / DSTEP < 6 && tgt <= 40) { s.dslot[n] = tgt; s.dk[n] = (r - DMA0) / DSTEP; }
    if (n > 9) s.prio[n] = r == 6 ? 2 : r == 9 ? 1 : r == 11 ? 0 : -1;
  }
  auto late = [&](int n) { while (rel12(n) < SLOW0 || rel12(n) > 10) ++n; return n; };
  for (int g = 0; g < 5; ++g) {
    const int start = (g < 4 ? 72 * (g + 1) : P2) + 2, tot = g == 4 ? 46 : 92;
    int e = 0;
    for (int n = start; e < tot; ++n) {
      const int c0 = g == 4 ? 2 : 1, c1 = g >= 3 ? 2 : ((n - start) & 1);
      if (s.eg[n] >= 0) s.ok = false;
      s.eg[n] = g; s.e0[n] = e; s.ne0[n] = c0; s.ne1[n] = c1;
      for (int i = e; i < e + c0 + c1 && i < tot; ++i) if (i % 46 == 45) { int m = n + 1; while (!s.bar[m]) ++m; m += XOFF; while (s.xrd[m] >= 0) m += 3; s.xrd[m] = 2 * g + i / 46; s.xst[m + 2] = 2 * g + i / 46; }
      e += c0 + c1;
      if (g < 3 && n >= start + 68) s.ok = false;
    }
  }
  s.og[P2 + 119] = 4; s.o0[P2 + 119] = 0; s.og[P2 + 120] = 4; s.o0[P2 + 120] = 4; s.ost[P2 + 124] = 4;
  const int opk[3] = {121, 127, 145}, ostl[3] = {125, 136, 149};
  for (int k = 0; k < 3; ++k) {
    for (int i = 0; i < 4; ++i) { const int n = P2 + opk[k] + i; s.og[n] = k; s.o0[n] = 2 * i; }
    s.ost[P2 + ostl[k]] = k;
  }
  const int lds_[9] = {16, 18, 24, 36, 38, 48, 76, 78, 80}, cb[9] = {45, 45, 45, 57, 57, 69, 105, 105, 105}, rds[9] = {50, 56, 62, 68, 74, 84, 106, 108, 110};
  for (int u = 0; u < 9; ++u) { s.ld[P2 + lds_[u]] = u; s.rd[P2 + rds[u]] = u; s.cbar[P2 + cb[u]] = u; if (!s.bar[P2 + cb[u]] || rds[u] < cb[u]) s.ok = false; }
  s.poll[P2 + 3] = 1; s.chk[P2 + 15] = 6;
  s.poll[P2 + 63] = 1; s.chk[P2 + 75] = 9;
  s.xop[28] = 3; s.xop[40] = 3;
  const int fb[2] = {PH + 21, P2 + 45}, fv[2] = {6, 9};
  for (int k = 0; k < 2; ++k) { if (!s.bar[fb[k]]) s.ok = false; s.fbar[fb[k]] = fv[k]; s.flag[fb[k] + 1] = fv[k]; }
  int cl[NS + 1] = {}, cf[NS + 1] = {};
  for (int n = 9; n < NS; ++n) {
    const int both = (s.ost[n] >= 0 ? 2 : 0) + s.xop[n];
    cl[n + 1] = cl[n] + both + (s.dslot[n] >= 0 ? 1 : 0);
    cf[n + 1] = cf[n] + both + (s.xst[n] >= 0 ? 2 : 0) + (s.flag[n] >= 0 ? 1 : 0) + s.poll[n] + (s.ld[n] >= 0 ? 2 : 0);
  }
  auto need = [&](int n, int ul_, int uf_) {
    if (ul_ >= 0) { const int w = cl[n] - ul_; if (s.wl[n] < 0 || w < s.wl[n]) s.wl[n] = w; }
    if (uf_ >= 0) { const int w = cf[n] - uf_; if (s.wf[n] < 0 || w < s.wf[n]) s.wf[n] = w; }
  };
  for (int n = 21; n < NS; ++n) {
    if (s.bar[n]) {
      int last = -1;
      for (int i = 9; i < n; ++i) if (s.dslot[i] == (n + 3) / 12) last = i;
      need(n, last >= 0 ? cl[last] + 1 : 0, n == 21 ? 0 : -1);
      if (n == 45) need(n, cl[29], cf[29]);
      if (n == 57) need(n, cl[41], cf[41]);
      if (s.fbar[n] >= 0) {
        int at = -1;
        for (int i = 9; i < n; ++i) if (s.xst[i] == s.fbar[n] - 1) at = i;
        if (at < 0) s.ok = false;
        for (int i = 9; i < n; ++i) if (s.xst[i] >= 0 && s.xst[i] < s.fbar[n] && i > at) at = i;
        need(n, -1, cf[at + 1]);
      }
    }
    if (s.chk[n] >= 0) { int i = n; while (!s.poll[i]) --i; need(n, -1, cf[i + 1]); }
    if (s.cbar[n] >= 0) { int i = n; while (s.ld[i] != s.cbar[n]) --i; need(n, -1, cf[i + 1]); }
  }
  for (int u = 0; u < 9; ++u) {
    int own_rd = u == 0 ? P2 - 1 : P2 + 9 * (u - 1) + 4;
    for (int n = 0; n < NS; ++n) if (s.xrd[n] == u && n > own_rd) own_rd = n;
    bool b1 = false;
    for (int n = own_rd + 1; n <= P2 + lds_[u]; ++n) b1 = b1 || s.bar[n];
    int use = 81; while (s.u[use] != u) ++use;
    if (!b1 || rds[u] + 2 > use) s.ok = false;
  }
  return s;
}
constexpr Sch SCH = make_sch();
static_assert(SCH.ok, "schedule");

template <int I, int N, class F> DI void static_for(F&& f) {
  if constexpr (N - I == 1) f(std::integral_constant<int, I>{});
  else if constexpr (N - I > 1) { static_for<I, (I + N) / 2>(f); static_for<(I + N) / 2, N>(f); }
}
template <int N> DI void waitv() { asm volatile("s_waitcnt vmcnt(%0)" :: "n"(N) : "memory"); }
#define MFMA_DRAIN() do { asm volatile("s_nop 7\n\ts_nop 3" ::: "memory"); SB0(); } while (0)
typedef __attribute__((ext_vector_type(4))) int srd_t;
DI srd_t make_srd(const void* p, unsigned bytes) {
  const unsigned long long a = (unsigned long long)p;
  srd_t r = {(int)(unsigned)a, (int)((unsigned)(a >> 32) & 0xffffu), (int)bytes, 0x00020000};
  return r;
}
DI void bdma(srd_t srd, unsigned voff, unsigned soff, unsigned lds_dst) {
  asm volatile("s_mov_b32 m0, %3\n\ts_nop 0\n\tbuffer_load_dwordx4 %0, %1, %2 offen lds" :: "v"(voff), "s"(srd), "s"(soff), "s"(lds_dst) : "memory");
}
template <int LOFF, int SOFF> DI void bdma_i(srd_t srd, unsigned voff, unsigned sbase, unsigned lbase) {
  unsigned so;
  asm volatile("s_add_u32 m0, %4, %6\n\ts_add_u32 %0, %3, %5\n\tbuffer_load_dwordx4 %1, %2, %0 offen lds"
               : "=&s"(so) : "v"(voff), "s"(srd), "s"(sbase), "s"(lbase), "n"(SOFF), "n"(LOFF) : "memory", "scc");
}

template <int OFF> DI void bdma_sc1(srd_t srd, unsigned voff, unsigned lbase) {
  unsigned so;
  asm volatile("s_add_u32 m0, %3, %4\n\ts_mov_b32 %0, %4\n\tbuffer_load_dwordx4 %1, %2, %0 offen sc1 lds"
               : "=&s"(so) : "v"(voff), "s"(srd), "s"(lbase), "n"(OFF) : "memory", "scc");
}

template <int FJ>
DI void k3_body(const Ptrs& P, lds_char* lds, const int lane, const int mi, const int b, const int c, const int mb) {
  constexpr int fj = FJ;
  const unsigned lds_a = (unsigned)(uintptr_t)lds;
  const int mt = mb * 4 + mi, p = c & 1;
  const int g = lane >> 4;
  ST_DECL; ST();
  const unsigned voff = lane * 16;
  const srd_t sw = make_srd(P.ws + OFF_WF + (size_t)c * WF_CH, (unsigned)WF_CH);
  const char* hsrc = P.ws + OFF_HC + (size_t)mt * 36864;
  const char* hmine = hsrc + fj * 18432;
  constexpr int ST0 = K3_RING + 2 * 24576, ST1 = K3_HH, ST2 = K3_HH + 24576;
  const unsigned ring_w = lds_a + K3_RING + mi * 6144;
  const unsigned sw0 = mi * 6144;
  auto first_slot = [&](auto tc) __attribute__((always_inline)) {
    constexpr int t = decltype(tc)::value;
    static_for<3 * FJ, 3 * FJ + 3>([&](auto kc) __attribute__((always_inline)) {
      constexpr int k = decltype(kc)::value;
      bdma_i<t * 24576 + k * 1024, t * 24576 + k * 1024>(sw, voff, sw0, ring_w);
    });
  };
  dma16x3(hmine, voff, lds_a + ST0 + (mi * 2 + fj) * 3072);
  first_slot(std::integral_constant<int, 0>{});
  dma16x3(hmine + 3072, voff, lds_a + ST1 + (mi * 2 + fj) * 3072);
  first_slot(std::integral_constant<int, 1>{});
  dma16x3(hmine + 6144, voff, lds_a + ST2 + (mi * 2 + fj) * 6144);
  dma16x3(hmine + 9216, voff, lds_a + ST2 + (mi * 2 + fj) * 6144 + 3072);
  u32x4 hf[2][18];
  WAITV(12);
  BAR();
  lds_char* ring_rd = lds + K3_RING + fj * 1024 + lane * 16;
  lds_char* hh_rd = lds + K3_HH + mi * 18432 + lane * 16;
  u32x4 a[6];
  auto read_frag = [&](auto nc) __attribute__((always_inline)) {
    constexpr int n = decltype(nc)::value;
    if constexpr (n < NS) a[n % 6] = *(lds_u32x4*)(ring_rd + ((n / 12) % 3) * 24576 + (n % 12) * 2048);
  };
#pragma unroll
  for (int i = 0; i < 6; ++i) hf[i & 1][i >> 1] = *(lds_u32x4*)(lds + ST0 + (mi * 2 + (i & 1)) * 3072 + (i >> 1) * 1024 + lane * 16);
  static_for<0, 3>(read_frag);
  WAITL0();
  BAR();
  first_slot(std::integral_constant<int, 2>{});
  ST();
  const auto wsr = WS_RSRC(P);
  const int fl_mine = (int)OFF_FLAG + b * 128;
  const char* fl_other = P.ws + OFF_FLAG + (b ^ 8) * 128;
  const int xh_off = (int)OFF_XH + (b * 4 + mi) * 18432;
  const srd_t sx = make_srd(P.ws + OFF_XH + (size_t)((b ^ 8) * 4 + mi) * 18432, 18432);
  const int pbase = (int)OFF_PART + ((mt * 36 + 9 * p) * 8 + (c >> 1)) * 1024;
  lds_char* hh_wr = hh_rd + 8 * fj;
  float sigma[2], gsc[2];
  f32x4 acc[2][2][2][2];
  f32x4 oc[9][2];
  u32x4 ov[2], ho[2][2], hp[9][2], xl[2];
  u32x2 hpk;
  u32x2 ovs[2];
  unsigned pollv;

  auto epi = [&](auto gc, auto ic) __attribute__((always_inline)) {
    constexpr int G = decltype(gc)::value, i = decltype(ic)::value;
    if constexpr (i < (G == 4 ? 46 : 92)) {
      constexpr int uu = i / 46, mh = (i % 46) / 23, r = i % 23, set = G & 1, U = 2 * G + uu;
      if constexpr (r < 20) {
        constexpr int e = (r / 10) * 2 + (r & 1), ph = (r % 10) >> 1;
        float tv = acc[set][uu][0][mh][e];
        if (ph == 0) tv = tv * gsc[mh];
        if (ph == 1) tv = __builtin_amdgcn_exp2f(tv);
        if (ph == 2) tv = __builtin_fmaf(tv, sigma[mh], sigma[mh]);
        if (ph == 3) tv = __builtin_amdgcn_rcpf(tv);
        if (ph == 4) tv = tv * acc[set][uu][1][mh][e];
        asm volatile("" : "+v"(tv));
        acc[set][uu][0][mh][e] = tv;
      } else if constexpr (r < 22) {
        unsigned t = pk2(acc[set][uu][0][mh][2 * (r - 20)], acc[set][uu][0][mh][2 * (r - 20) + 1]);
        asm volatile("" : "+v"(t));
        hpk[r - 20] = t;
      } else {
        *(lds_u32x2*)(hh_wr + mh * 9216 + U * 1024) = hpk;
      }
      SB0();
    }
  };
  auto opack = [&](const int pr, const int i) __attribute__((always_inline)) {
    const int mh = i >> 2, r = i & 3;
    if (pr == 4 && r >= 2) return;
    const int ti = pr == 4 ? 8 : 2 * pr + (r >> 1), e = pr == 4 ? 2 * r : 2 * (r & 1);
    unsigned t = pk2(oc[ti][mh][e], oc[ti][mh][e + 1]);
    asm volatile("" : "+v"(t));
    if (pr == 4) ovs[mh][r & 1] = t; else ov[mh][r] = t;
    SB0();
  };
  auto ostore = [&](const int pr) __attribute__((always_inline)) {
#pragma unroll
    for (int mh = 0; mh < 2; ++mh) {
      if (pr < 4) __builtin_amdgcn_raw_buffer_store_b128(ov[mh], wsr, voff, pbase + (p + 2 * pr + fj + 18 * mh) * 8192, 16);
      else { __builtin_amdgcn_raw_buffer_store_b64(ovs[mh], wsr, voff, pbase + ((p ? 0 : 8) + 18 * mh) * 8192 + 8 * fj, 16); }
    }
    SB0();
  };

  static_for<0, NS>([&](auto nc) __attribute__((always_inline)) {
    constexpr int n = decltype(nc)::value;
    constexpr bool bar = SCH.bar[n];
    if constexpr (n == 9) WAITV(12);
    else if constexpr (FJ == 0 && SCH.wl[n] >= 0) waitv<SCH.wl[n]>();
    else if constexpr (FJ == 1 && SCH.wf[n] >= 0) waitv<SCH.wf[n]>();
    if constexpr (FJ == 1 && SCH.chk[n] >= 0) {
      asm volatile("" : "+v"(pollv));
      if (__builtin_amdgcn_readfirstlane(pollv) < (unsigned)SCH.chk[n]) {
#pragma unroll 1
        for (int spin = 0; spin < (1 << 22); ++spin) {
          asm volatile("global_load_dword %0, %1, %2 sc1\n\ts_waitcnt vmcnt(0)" : "=&v"(pollv) : "v"(0u), "s"(fl_other) : "memory");
          if (__builtin_amdgcn_readfirstlane(pollv) >= (unsigned)SCH.chk[n]) break;
          __builtin_amdgcn_s_sleep(2);
        }
      }
    }
    if constexpr (bar) { WAITL0(); BAR(); __builtin_amdgcn_s_setprio(3); }
    const u32x4 af = a[n % 6];
    if constexpr (SCH.prio[n] >= 0) __builtin_amdgcn_s_setprio(SCH.prio[n]);
    if constexpr (n == 72 || n == 144 || n == PH || n == P2 || n == P2 + 27 || n == P2 + 54 || n == P2 + 81 || n == P2 + 108 || n == P2 + 135) ST();
    SB0();
    static_for<0, 2>([&](auto mc) __attribute__((always_inline)) {
      constexpr int mh = decltype(mc)::value;
      if constexpr (n < P2) {
        constexpr int y = n % 72;
        constexpr int set = (n / 72) & 1, S = n >= PH ? y >> 1 : y >> 2, mat = n >= PH ? y & 1 : (y >> 1) & 1, uu = n >= PH ? 0 : y & 1;
        if constexpr (S == 0) asm volatile("v_mfma_f32_16x16x32_bf16 %0, %1, %2, 0" : "=&v"(acc[set][uu][mat][mh]) : "v"(af), "v"(hf[mh][S]));
        else asm volatile("v_mfma_f32_16x16x32_bf16 %0, %1, %2, %0" : "+v"(acc[set][uu][mat][mh]) : "v"(af), "v"(hf[mh][S]));
      } else {
        constexpr int s2 = n - P2, U = SCH.u[s2], ti = SCH.db[s2] == 4 ? 8 : 2 * SCH.db[s2] + SCH.t01[s2];
        if constexpr (s2 < 9) asm volatile("v_mfma_f32_16x16x32_bf16 %0, %1, %2, 0" : "=&v"(oc[ti][mh]) : "v"(af), "v"(ho[0][mh]));
        else if constexpr (SCH.own[s2]) asm volatile("v_mfma_f32_16x16x32_bf16 %0, %1, %2, %0" : "+v"(oc[ti][mh]) : "v"(af), "v"(ho[U & 1][mh]));
        else asm volatile("v_mfma_f32_16x16x32_bf16 %0, %1, %2, %0" : "+v"(oc[ti][mh]) : "v"(af), "v"(hp[U][mh]));
      }
      SB0();
      if constexpr (mh == 0) { read_frag(std::integral_constant<int, n + 3>{}); SB0(); }
      if constexpr (SCH.eg[n] >= 0) {
        constexpr int f = SCH.e0[n] + (mh ? SCH.ne0[n] : 0), cnt = mh ? SCH.ne1[n] : SCH.ne0[n];
        static_for<f, f + cnt>([&](auto ic) __attribute__((always_inline)) { epi(std::integral_constant<int, SCH.eg[n]>{}, ic); });
      }
      if constexpr (SCH.og[n] >= 0) opack(SCH.og[n], SCH.o0[n] + mh);
    });
    if constexpr (FJ == 0 && SCH.dslot[n] >= 0) {
      constexpr int t = SCH.dslot[n], k = SCH.dk[n];
      if constexpr (k % 3 == 0) bdma_i<(t % 3) * 24576 + k * 1024, t * 24576 + k * 1024>(sw, voff, sw0, ring_w);
      else asm volatile("buffer_load_dwordx4 %0, %1, %2 offen offset:%3 lds" :: "v"(voff), "s"(sw), "s"(sw0 + t * 24576 + (k / 3) * 3072), "n"((k % 3) * 1024) : "memory");
      SB0();
    }
    if constexpr (SCH.ost[n] >= 0) ostore(SCH.ost[n]);
    if constexpr (n == 5) { dma16x3(P.ws + OFF_SSQ + mt * 2560, voff, lds_a + K3_SSQ + mi * 3072); SB0(); }
    if constexpr (n == 28) { dma16x3(hmine + 12288, voff, lds_a + ST1 + (mi * 2 + fj) * 3072); SB0(); }
    if constexpr (n == 40) { dma16x3(hmine + 15360, voff, lds_a + ST2 + (mi * 2 + fj) * 3072); SB0(); }
    if constexpr (n == 9 || n == 45 || n == 57) {
      constexpr int s0 = n == 9 ? 3 : (n == 45 ? 12 : 15), stg = n == 57 ? ST2 : ST1;
#pragma unroll
      for (int i = 0; i < 6; ++i) hf[i & 1][s0 + (i >> 1)] = *(lds_u32x4*)(lds + stg + (mi * 2 + (i & 1)) * 3072 + (i >> 1) * 1024 + lane * 16);
      SB0();
    }
    if constexpr (n == 21) {
#pragma unroll
      for (int i = 0; i < 12; ++i) hf[i & 1][6 + (i >> 1)] = *(lds_u32x4*)(lds + ST2 + (mi * 2 + (i & 1)) * 6144 + (i >> 1) * 1024 + lane * 16);
      SB0();
    }
    if constexpr (n == 50) {
#pragma unroll
      for (int mh = 0; mh < 2; ++mh) {
        float ssq = 0.f;
#pragma unroll
        for (int i = 0; i < 20; ++i) ssq += *(lds_float*)(lds + K3_SSQ + mi * 3072 + (i * 32 + mh * 16 + (lane & 15)) * 4);
        sigma[mh] = sqrtf(ssq * (1.f / D_) + LN_EPS);
        gsc[mh] = -1.4426950408889634f / sigma[mh];
        asm volatile("" : "+v"(sigma[mh]), "+v"(gsc[mh]));
      }
      SB0();
    }
    if constexpr (n == P2 - 1) { ho[0][0] = *(lds_u32x4*)(hh_rd); ho[0][1] = *(lds_u32x4*)(hh_rd + 9216); SB0(); }
    if constexpr (n >= P2 && n < P2 + 72 && (n - P2) % 9 == 4) {
      constexpr int U = (n - P2) / 9 + 1;
      ho[U & 1][0] = *(lds_u32x4*)(hh_rd + U * 1024); ho[U & 1][1] = *(lds_u32x4*)(hh_rd + 9216 + U * 1024);
      SB0();
    }
    if constexpr (FJ == 1) {
      if constexpr (SCH.xst[n] >= 0) {
        __builtin_amdgcn_raw_buffer_store_b128(xl[0], wsr, voff, xh_off + SCH.xst[n] * 1024, 16);
        __builtin_amdgcn_raw_buffer_store_b128(xl[1], wsr, voff, xh_off + 9216 + SCH.xst[n] * 1024, 16);
        SB0();
      }
      if constexpr (SCH.flag[n] >= 0) { __builtin_amdgcn_raw_buffer_store_b32((unsigned)SCH.flag[n], wsr, 0, fl_mine, 16); SB0(); }
      if constexpr (SCH.poll[n]) { asm volatile("global_load_dword %0, %1, %2 sc1" : "=&v"(pollv) : "v"(0u), "s"(fl_other) : "memory"); SB0(); }
      if constexpr (SCH.ld[n] >= 0) {
        constexpr int u = SCH.ld[n];
        bdma_sc1<u * 1024>(sx, voff, lds_a + K3_HH + mi * 18432);
        bdma_sc1<9216 + u * 1024>(sx, voff, lds_a + K3_HH + mi * 18432);
        SB0();
      }
      if constexpr (SCH.xrd[n] >= 0) {
        xl[0] = *(lds_u32x4*)(hh_rd + SCH.xrd[n] * 1024); xl[1] = *(lds_u32x4*)(hh_rd + 9216 + SCH.xrd[n] * 1024);
        SB0();
      }
    }
    if constexpr (SCH.rd[n] >= 0) {
      constexpr int u = SCH.rd[n];
      hp[u][0] = *(lds_u32x4*)(hh_rd + u * 1024); hp[u][1] = *(lds_u32x4*)(hh_rd + 9216 + u * 1024);
      SB0();
    }
  });
  ST();
  MFMA_DRAIN();
#pragma unroll
  for (int i = 0; i < 8; ++i) opack(3, i);
  ostore(3);
  const int wid = fj * 4 + mi;
  const int t16 = mb * 8 + (c >> 1), f0 = t16 * 18 + 9 * p;
  const int l8 = 8 * wid + (lane & 7);
  const int row = t16 * 16 + (lane & 15), row8 = t16 * 16 + (l8 & 15), g8 = l8 >> 4;
  const char* hb = P.ws + OFF_HC + (size_t)(f0 + wid) * 1024 + ((lane & 15) + 16 * (g >> 1)) * 16 + 8 * (g & 1);
  const char* hb8 = P.ws + OFF_HC + (size_t)(f0 + 8) * 1024 + ((l8 & 15) + 16 * (g8 >> 1)) * 16 + 8 * (g8 & 1);
  const u32x2 h0 = *(const u32x2*)hb, h1 = *(const u32x2*)(hb + 512), h80 = *(const u32x2*)hb8, h81 = *(const u32x2*)(hb8 + 512);
  const float mean = ((const float*)(P.ws + OFF_MEANH))[row], mean8 = ((const float*)(P.ws + OFF_MEANH))[row8];
  WAITV(0);
  BAR();
  ST();
  if (FJ == 0 && mi == 0) {
    unsigned* cnt = (unsigned*)(P.ws + OFF_CNT + (mb * 2 + p) * 128);
    if (lane == 0) __hip_atomic_fetch_add(cnt, 1u, __ATOMIC_RELAXED, __HIP_MEMORY_SCOPE_AGENT);
#pragma unroll 1
    for (int spin = 0; spin < (1 << 22); ++spin) {
      asm volatile("global_load_dword %0, %1, %2 sc1\n\ts_waitcnt vmcnt(0)" : "=&v"(pollv) : "v"(0u), "s"(cnt) : "memory");
      if (__builtin_amdgcn_readfirstlane(pollv) >= 8u) break;
      __builtin_amdgcn_s_sleep(1);
    }
  }
  BAR();
  ST();
  u32x4 pv[8], p8;
#pragma unroll
  for (int k = 0; k < 8; ++k) pv[k] = __builtin_amdgcn_raw_buffer_load_b128(wsr, voff, (int)OFF_PART + ((f0 + wid) * 8 + k) * 1024, 16);
  p8 = __builtin_amdgcn_raw_buffer_load_b128(wsr, (lane >> 3) * 1024 + l8 * 16, (int)OFF_PART + (f0 + 8) * 8192, 16);
  float o[8], o8[8];
  o[0] = bflo(h0[0]); o[1] = bfhi(h0[0]); o[2] = bflo(h0[1]); o[3] = bfhi(h0[1]);
  o[4] = bflo(h1[0]); o[5] = bfhi(h1[0]); o[6] = bflo(h1[1]); o[7] = bfhi(h1[1]);
#pragma unroll
  for (int k = 0; k < 8; ++k) o[k] += mean;
#pragma unroll
  for (int r = 0; r < 8; ++r)
#pragma unroll
    for (int k = 0; k < 4; ++k) { o[2 * k] += bflo(pv[r][k]); o[2 * k + 1] += bfhi(pv[r][k]); }
  {
    char* d = P.out + (size_t)row * 1152 + (9 * p + wid) * 64 + g * 8;
    *(u32x2*)d = (u32x2){pk2(o[0], o[1]), pk2(o[2], o[3])};
    *(u32x2*)(d + 32) = (u32x2){pk2(o[4], o[5]), pk2(o[6], o[7])};
  }
#pragma unroll
  for (int k = 0; k < 4; ++k) { o8[2 * k] = bflo(p8[k]); o8[2 * k + 1] = bfhi(p8[k]); }
#pragma unroll
  for (int k = 0; k < 8; ++k) { o8[k] += __shfl_xor(o8[k], 8); o8[k] += __shfl_xor(o8[k], 16); o8[k] += __shfl_xor(o8[k], 32); }
  if (lane < 8) {
    char* d = P.out + (size_t)row8 * 1152 + (9 * p + 8) * 64 + g8 * 8;
    *(u32x2*)d = (u32x2){pk2(o8[0] + bflo(h80[0]) + mean8, o8[1] + bfhi(h80[0]) + mean8), pk2(o8[2] + bflo(h80[1]) + mean8, o8[3] + bfhi(h80[1]) + mean8)};
    *(u32x2*)(d + 32) = (u32x2){pk2(o8[4] + bflo(h81[0]) + mean8, o8[5] + bfhi(h81[0]) + mean8), pk2(o8[6] + bflo(h81[1]) + mean8, o8[7] + bfhi(h81[1]) + mean8)};
  }
}

__global__ __launch_bounds__(512, 2) void k3_ffn(Ptrs P) {
  extern __shared__ __attribute__((aligned(16))) char smem[];
  const int lane = threadIdx.x & 63, wid = __builtin_amdgcn_readfirstlane(threadIdx.x >> 6);
  const int b = blockIdx.x;
  const int c = ((b & 7) >> 1) * 4 + ((b >> 3) & 3), mb = (b & 1) * 8 + (b >> 5);
  if (wid < 4) k3_body<0>(P, (lds_char*)smem, lane, wid, b, c, mb);
  else k3_body<1>(P, (lds_char*)smem, lane, wid - 4, b, c, mb);
}

constexpr int K0_IMG = 4608;
constexpr int K0_RED = 3 * K0_IMG;
constexpr int K0_LDS = K0_RED + 9 * 8 * 8;

DI float dpp_sum8(float v) {
  v += __shfl_xor(v, 1); v += __shfl_xor(v, 2); v += __shfl_xor(v, 4);
  return v;
}

__global__ __launch_bounds__(768) void k0_prep(Ptrs P) {
  extern __shared__ __attribute__((aligned(16))) char smem[];
  lds_char* lds = (lds_char*)smem;
  const int lane = threadIdx.x & 63, wid = __builtin_amdgcn_readfirstlane(threadIdx.x >> 6);
  const int b = blockIdx.x;
  ST_DECL; ST();
  if (wid < 9) {
    const int row = b * 8 + (lane >> 3), i = lane & 7;
    const u32x4 v = *(const u32x4*)(P.x + (size_t)row * 1152 + wid * 128 + i * 16);
    float e[8];
#pragma unroll
    for (int k = 0; k < 4; ++k) { e[2 * k] = bflo(v[k]); e[2 * k + 1] = bfhi(v[k]); }
    const float sum = dpp_sum8(((e[0] + e[1]) + (e[2] + e[3])) + ((e[4] + e[5]) + (e[6] + e[7])));
    const float lm = sum * (1.f / 64.f);
    float m2 = 0.f;
#pragma unroll
    for (int k = 0; k < 8; ++k) { const float d = e[k] - lm; m2 = __builtin_fmaf(d, d, m2); }
    m2 = dpp_sum8(m2);
    if (i == 0) *(lds_f32x2*)(lds + K0_RED + (wid * 8 + (lane >> 3)) * 8) = (f32x2){lm, m2};
    WAITL0();
    BAR();
    ST();
    f32x2 pr[9];
#pragma unroll
    for (int w = 0; w < 9; ++w) pr[w] = *(lds_f32x2*)(lds + K0_RED + (w * 8 + (lane >> 3)) * 8);
    float mean = 0.f;
#pragma unroll
    for (int w = 0; w < 9; ++w) mean += pr[w][0];
    mean *= (1.f / 9.f);
    float tot = 0.f;
#pragma unroll
    for (int w = 0; w < 9; ++w) { const float d = pr[w][0] - mean; tot += __builtin_fmaf(64.f * d, d, pr[w][1]); }
    const float rstd = rsqrtf(tot * (1.f / D_) + LN_EPS);
    u32x4 o;
#pragma unroll
    for (int k = 0; k < 4; ++k) o[k] = pk2((e[2 * k] - mean) * rstd, (e[2 * k + 1] - mean) * rstd);
    WS_ST16(P, (int)OFF_XN + ((row >> 5) * 36 * 64 + (row & 31) + 32 * (i & 1) + (4 * wid + (i >> 1)) * 64) * 16, o, ST_AUX);
    if (wid == 0 && i == 0) WS_ST4(P, OFF_MEANX + row * 4, __float_as_uint(mean), ST_AUX);
    return;
  }
  const int id = (wid - 9) * 256 + b;
  lds_char* img = lds + (wid - 9) * K0_IMG;
  const unsigned img_a = (unsigned)(uintptr_t)img;
  const int g = lane >> 4, q = (lane >> 2) & 3, p = lane & 3;
  unsigned pf_sink = 0;
  if (wid == 10 && b < 96) {
    unsigned long long pc, k1a;
    asm volatile("s_getpc_b64 %0" : "=s"(pc));
    asm volatile("s_getpc_b64 s[40:41]\n\ts_add_u32 s40, s40, _Z6k1_qkv4Ptrs@rel32@lo+4\n\ts_addc_u32 s41, s41, _Z6k1_qkv4Ptrs@rel32@hi+12\n\t"
                 "s_mov_b64 %0, s[40:41]" : "=s"(k1a) :: "s40", "s41", "scc");
    const char* pp = (const char*)k1a + ((b >> 3) * 64 + lane) * 128;
    pp = pp < (const char*)pc ? pp : (const char*)k1a;
    asm volatile("global_load_dword %0, %1, off" : "=&v"(pf_sink) : "v"(pp) : "memory");
  }
  if (id < 576) {
    const int s = id >> 4, h = (id >> 3) & 1, hp0 = (id & 7) * 2;
    const char* src = P.wqkv + (size_t)(16 * s + 8 * h) * 3456 + hp0 * 72;
#pragma unroll
    for (int it = 0; it < 4; ++it) {
      int e = it * 64 + lane; e = e < 216 ? e : 215;
      const int sgm = e / 9, ch = e - sgm * 9;
      dma16x1(src, sgm * 1152 + ch * 16, img_a + it * 1024);
    }
    BAR();
    ST();
    WAITV(0);
    asm volatile("" :: "v"(pf_sink));
    ST();
#pragma unroll
    for (int it = 0; it < 4; ++it) {
      int Q = 16 * it + 4 * g + p; Q = Q < 54 ? Q : 53;
      const int hl = Q >= 27 ? 1 : 0, sg = Q - 27 * hl;
      const int cb = sg < 4 ? 8 * sg : (sg < 8 ? 8 * sg + 8 : (sg == 8 ? 32 : (sg < 18 ? 8 * sg + 72 : 8 * sg + 144)));
      lds_char* a = img + q * 432 + cb + 72 * hl;
      u32x4 v = tr_frag(a, a + 4 * 432);
      const int o = it * 64 + lane, ol = o >= 108 ? 1 : 0, slot = o - 108 * ol;
      const int off = slot < 32 ? WQT_Q + s * 1024 + (h * 32 + slot) * 16 : s * 2432 + (h * 76 + slot - 32) * 16;
      if (o < 216) WS_ST16(P, OFF_WQT + (hp0 + ol) * (int)WQT_HP + off, v, ST_AUX);
    }
  } else if (id < 720) {
    const int j = id - 576, ks = j >> 2, cb = j & 3;
#pragma unroll
    for (int it = 0; it < 5; ++it) {
      int e = it * 64 + lane; e = e < 288 ? e : 287;
      const int kk = e / 18, ch = e - kk * 18, h = kk >> 3, jj = kk & 7;
      const int row = ks < 32 ? 18 * ks + (jj & 3) + 8 * (jj >> 2) + 4 * h : 18 * (8 * (ks - 32) + jj) + 16 + h;
      if (it < 4 || lane < 32) dma16x1(P.wo + cb * 288, row * 1152 + ch * 16, img_a + it * 1024);
    }
    BAR();
    WAITV(0);
#pragma unroll
    for (int it = 0; it < 5; ++it) {
      int G16 = 4 * it + g; G16 = G16 < 18 ? G16 : 17;
      const int h = G16 >= 9 ? 1 : 0, sg = G16 - 9 * h;
      lds_char* a = img + (8 * h + q) * 288 + (16 * sg + 4 * p) * 2;
      u32x4 v = tr_frag(a, a + 4 * 288);
      if (it < 4 || lane < 32) WS_ST16(P, OFF_WOT + cb * (int)WOT_CB + ks * 4672 + (h * 146 + 16 * sg + (lane & 15)) * 16, v, ST_AUX);
    }
  } else if (id < 756) {
    const int row = (id - 720) * 16 + (lane >> 2), i = lane & 3;
    const char* src = P.wo + (size_t)row * 1152 + i * 16;
    u32x4 v[18];
#pragma unroll
    for (int j = 0; j < 18; ++j) v[j] = *(const u32x4*)(src + j * 64);
    BAR();
    float s4[4] = {0.f, 0.f, 0.f, 0.f};
    const bf16x2 one2 = __builtin_bit_cast(bf16x2, 0x3f803f80u);
#pragma unroll
    for (int j = 0; j < 18; ++j)
#pragma unroll
      for (int k = 0; k < 4; ++k) { const unsigned t = v[j][k]; s4[k] = __builtin_amdgcn_fdot2_f32_bf16(__builtin_bit_cast(bf16x2, t), one2, s4[k], false); }
    float sum = (s4[0] + s4[1]) + (s4[2] + s4[3]);
    sum += __shfl_xor(sum, 1); sum += __shfl_xor(sum, 2);
    const float mean = sum * (1.f / D_);
    const int hd = row / 18, e = row % 18;
    const int ks = e < 16 ? hd : 32 + (hd >> 3), jj = e < 16 ? (e & 3) + 4 * (e >> 3) : (hd & 7), hh = e < 16 ? (e >> 2) & 1 : e - 16;
    const unsigned hi = pk2(mean, 0.f) & 0xffffu;
    const unsigned lo = pk2(mean - bflo(hi), 0.f) & 0xffffu;
    unsigned short* d = (unsigned short*)(P.ws + OFF_WOT + (size_t)i * WOT_CB + ks * 4672 + (hh * 146 + 144) * 16 + jj * 2);
    d[0] = (unsigned short)hi; d[8] = (unsigned short)lo;
  } else {
    const u32x4 z = {0u, 0u, 0u, 0u};
    for (int k = id - 756; k < 50; k += 12) WS_ST16(P, OFF_KV + k * 1024 + lane * 16, z, ST_AUX);
    for (int k = id - 756; k < 36; k += 12) WS_ST16(P, OFF_FLAG + k * 1024 + lane * 16, z, ST_AUX);
    BAR();
  }
}

extern "C" void kernel_launch(void* const* d_in, const int* in_sizes, int n_in, void* d_out, int out_size, void* d_ws,
                              size_t ws_size, hipStream_t stream) {
  Ptrs P;
  P.x = (const char*)d_in[0]; P.wqkv = (const char*)d_in[1]; P.wo = (const char*)d_in[2];
  P.wg = (const char*)d_in[4]; P.wu = (const char*)d_in[5]; P.wd = (const char*)d_in[6];
  P.ws = (char*)d_ws; P.out = (char*)d_out;
  (void)hipFuncSetAttribute((const void*)k1_qkv, hipFuncAttributeMaxDynamicSharedMemorySize, K1_LDS);
  (void)hipFuncSetAttribute((const void*)k2_attn, hipFuncAttributeMaxDynamicSharedMemorySize, K2_LDS);
  (void)hipFuncSetAttribute((const void*)k3_ffn, hipFuncAttributeMaxDynamicSharedMemorySize, K3_LDS);
  k0_prep<<<256, 768, K0_LDS, stream>>>(P);
  k1_qkv<<<256, 512, K1_LDS, stream>>>(P);
  k2_attn<<<256, 768, K2_LDS, stream>>>(P);
  k3_ffn<<<256, 512, K3_LDS, stream>>>(P);
}
```

```cpp
#include <hip/hip_runtime.h>
#include <hip/hip_bf16.h>
#include <stdint.h>
#include <type_traits>

typedef __attribute__((ext_vector_type(8))) short bf16x8;
typedef __attribute__((ext_vector_type(4))) short s16x4;
typedef __attribute__((ext_vector_type(16))) float f32x16;
typedef __attribute__((ext_vector_type(4))) float f32x4;
typedef __attribute__((ext_vector_type(2))) float f32x2;
typedef __attribute__((ext_vector_type(2))) __bf16 bf16x2;
typedef __attribute__((ext_vector_type(4))) unsigned u32x4;
typedef __attribute__((ext_vector_type(2))) unsigned u32x2;
typedef __attribute__((address_space(3))) char lds_char;
typedef __attribute__((address_space(3))) s16x4 lds_s16x4;
typedef __attribute__((address_space(3))) u32x4 lds_u32x4;
typedef __attribute__((address_space(3))) u32x2 lds_u32x2;
typedef __attribute__((address_space(3))) f32x4 lds_f32x4;
typedef __attribute__((address_space(3))) float lds_float;
typedef __attribute__((address_space(3))) f32x2 lds_f32x2;
typedef __attribute__((address_space(3))) unsigned lds_uint;

#define DI __device__ __forceinline__
#define MFMA32(a, b, c) __builtin_amdgcn_mfma_f32_32x32x16_bf16((a), (b), (c), 0, 0, 0)
#define MFMA16(a, b, c) __builtin_amdgcn_mfma_f32_16x16x32_bf16((a), (b), (c), 0, 0, 0)
#define WAITV(n) asm volatile("s_waitcnt vmcnt(" #n ")" ::: "memory")
#define WAITL0() asm volatile("s_waitcnt lgkmcnt(0)" ::: "memory")
#define BAR() asm volatile("s_barrier" ::: "memory")
#define SB0() __builtin_amdgcn_sched_barrier(0)

constexpr int S_ = 2048, D_ = 576, F_ = 4608;
constexpr float LN_EPS = 1e-5f;

constexpr size_t FLB = (size_t)S_ * D_ * 2;
constexpr size_t WQT_HP = 36 * 3456;
constexpr int WQT_Q = 36 * 2432;
constexpr size_t WOT_CB = 36 * 4672;
constexpr size_t WF_CH = 648 * 1024;
constexpr size_t OFF_XN = 0;
constexpr size_t OFF_PQ = OFF_XN + FLB;
constexpr size_t OFF_HC = OFF_PQ + FLB;
constexpr size_t OFF_MEANX = OFF_HC + FLB;
constexpr size_t OFF_MEANH = OFF_MEANX + 8192;
constexpr size_t OFF_SSQ = OFF_MEANH + 8192;
constexpr size_t OFF_WBAR = OFF_SSQ + 20 * 8192;
constexpr size_t OFF_KV = OFF_WBAR + 4096;
constexpr size_t OFF_WQT = OFF_KV + 53248;
constexpr size_t OFF_WOT = OFF_WQT + 16 * WQT_HP + 16384;
constexpr size_t OFF_WF = OFF_WOT + 4 * WOT_CB + 4096;
constexpr size_t OFF_PART = OFF_WF + 16 * WF_CH + 65536;
constexpr size_t OFF_XH = OFF_PART + 8 * FLB;
constexpr size_t OFF_FLAG = OFF_XH + 256 * 73728;
constexpr size_t OFF_CNT = OFF_FLAG + 256 * 128;
constexpr size_t OFF_G1 = OFF_CNT + 32 * 128;
constexpr size_t OFF_G3 = OFF_G1 + 256 * 128;
constexpr size_t OFF_GW = OFF_G3 + 16 * 128;
constexpr size_t WS_END = OFF_GW + 8 * 128;

#define ST_DECL
#define ST()
#define ST_OUT(k, slot)
DI unsigned pk2(float a, float b) {
  f32x2 v = {a, b};
  bf16x2 r = __builtin_convertvector(v, bf16x2);
  return __builtin_bit_cast(unsigned, r);
}
DI float bflo(unsigned u) { return __uint_as_float(u << 16); }
DI float bfhi(unsigned u) { return __uint_as_float(u & 0xffff0000u); }
DI float phi(float t) {
  const float e = __builtin_amdgcn_fmed3f(__builtin_amdgcn_exp2f(t * 1.4426950408889634f), 0.f, 1.f);
  return (__builtin_amdgcn_fmed3f(t, 0.f, __builtin_inff()) + 1e-6f) + e;
}
DI bf16x8 as_frag(u32x4 v) { return __builtin_bit_cast(bf16x8, v); }

DI void dma16(const void* gsrc, unsigned lds_dst) {
  unsigned keep;
  asm volatile("s_mov_b32 %0, m0\n\ts_mov_b32 m0, %2\n\ts_nop 0\n\tglobal_load_lds_dwordx4 %1, off\n\ts_mov_b32 m0, %0"
               : "=&s"(keep) : "v"(gsrc), "s"(lds_dst) : "memory");
}
DI void dma16x3(const void* sbase, unsigned voff, unsigned lds_dst) {
  unsigned keep;
  asm volatile("s_mov_b32 %0, m0\n\ts_mov_b32 m0, %3\n\ts_nop 0\n\t"
               "global_load_lds_dwordx4 %1, %2\n\t"
               "global_load_lds_dwordx4 %1, %2 offset:1024\n\t"
               "global_load_lds_dwordx4 %1, %2 offset:2048\n\t"
               "s_mov_b32 m0, %0"
               : "=&s"(keep) : "v"(voff), "s"(sbase), "s"(lds_dst) : "memory");
}
DI void dma16x3s(const void* sbase, unsigned voff, unsigned lds_dst) {
  unsigned keep;
  asm volatile("s_mov_b32 %0, m0\n\ts_mov_b32 m0, %3\n\ts_nop 0\n\t"
               "global_load_lds_dwordx4 %1, %2 sc1\n\t"
               "global_load_lds_dwordx4 %1, %2 offset:1024 sc1\n\t"
               "global_load_lds_dwordx4 %1, %2 offset:2048 sc1\n\t"
               "s_mov_b32 m0, %0"
               : "=&s"(keep) : "v"(voff), "s"(sbase), "s"(lds_dst) : "memory");
}
DI void dma16x1(const void* sbase, unsigned voff, unsigned lds_dst) {
  unsigned keep;
  asm volatile("s_mov_b32 %0, m0\n\ts_mov_b32 m0, %3\n\ts_nop 0\n\t"
               "global_load_lds_dwordx4 %1, %2\n\t"
               "s_mov_b32 m0, %0"
               : "=&s"(keep) : "v"(voff), "s"(sbase), "s"(lds_dst) : "memory");
}
DI u32x4 tr_frag(lds_char* p0, lds_char* p1) {
  s16x4 lo = __builtin_amdgcn_ds_read_tr16_b64_v4i16((lds_s16x4*)p0);
  s16x4 hi = __builtin_amdgcn_ds_read_tr16_b64_v4i16((lds_s16x4*)p1);
  u32x2 a = __builtin_bit_cast(u32x2, lo), b = __builtin_bit_cast(u32x2, hi);
  u32x4 r = {a[0], a[1], b[0], b[1]};
  return r;
}

#define ST_AUX 16
#define WF_AUX 16
#define WS_RSRC(P) __builtin_amdgcn_make_buffer_rsrc((P).ws, 0, (int)WS_END, 0x00020000)
#define WS_ST16(P, off, v, aux) __builtin_amdgcn_raw_buffer_store_b128((v), WS_RSRC(P), (int)(off), 0, (aux))
#define WS_ST8(P, off, v, aux) __builtin_amdgcn_raw_buffer_store_b64((v), WS_RSRC(P), (int)(off), 0, (aux))
#define WS_ST4(P, off, v, aux) __builtin_amdgcn_raw_buffer_store_b32((v), WS_RSRC(P), (int)(off), 0, (aux))
template <class R> DI void st128(u32x4 v, R rsrc, int voff, int soff) {
  __builtin_amdgcn_raw_buffer_store_b128(v, rsrc, voff, soff, 16);
  asm volatile("s_nop 1" :: "v"(v));
}
struct Ptrs {
  const char* x; const char* wqkv; const char* wo; const char* wg; const char* wu; const char* wd;
  char* ws; char* out;
};

constexpr int ITEM_LDS = 9216;
constexpr int SUB_PER_XCD = 486;

DI void sub_load(const Ptrs& P, int x, int j, int lane, unsigned lds_base) {
  const int rl = lane >> 3, cp = lane & 7;
  const int cb = j / 36, r = j % 36, S = r >> 1, mat = r & 1;
  const char* base = (mat ? P.wu : P.wg) + (size_t)(32 * S) * (F_ * 2) + (576 * x + 64 * cb) * 2;
#pragma unroll
  for (int it = 0; it < 4; ++it) {
    const int X = ((rl >> 1) & 1) | ((it & 1) << 1);
    dma16x1(base + (size_t)(8 * it) * (F_ * 2), rl * (F_ * 2) + (((cp >> 1) ^ X) * 32) + (cp & 1) * 16, lds_base + it * 1024);
  }
}
DI void sub_store(const Ptrs& P, int x, int j, int lane, lds_char* img) {
  const int g = lane >> 4, q = (lane >> 2) & 3, p = lane & 3;
  const int X = ((q >> 1) & 1) | ((g & 1) << 1);
  const int cb = j / 36, r = j % 36, S = r >> 1, mat = r & 1;
#pragma unroll
  for (int u4 = 0; u4 < 4; ++u4) {
    lds_char* a = img + (8 * g + q) * 128 + ((u4 ^ X) * 32) + 8 * p;
    u32x4 v = tr_frag(a, a + 512);
    const int ug = 4 * cb + u4, cc = ug >= 18 ? 1 : 0, u = ug - 18 * cc, U = u >> 1;
    const int n = U < 8 ? (U >> 1) * 72 + S * 4 + mat * 2 + (U & 1) : 288 + 2 * S + mat;
    st128(v, WS_RSRC(P), lane * 16, (int)OFF_WF + (2 * x + cc) * (int)WF_CH + (2 * n + (u & 1)) * 1024);
  }
}

#define CODE_PF 1
#define FRONT_PF 1
#define N0_SUB 0
#define N1_SUB 256
constexpr int K0_ITEMS = 704 + 8 * (N0_SUB / 2);

constexpr int K1_W = 0;
constexpr int K1_ITEM = 124 * 1024;
constexpr int KVP = 192;
constexpr int K2_O = 49152;
constexpr int K2_RED = K2_O + 36864;
constexpr int K2_MEAN = K2_RED + 32768;
static_assert(K2_MEAN + 2048 <= K1_ITEM, "lds");

DI void poll_counters(const char* base, const int off, const unsigned need) {
#pragma unroll 1
  for (int spin = 0; spin < (1 << 22); ++spin) {
    unsigned v;
    asm volatile("global_load_dword %0, %1, %2 sc1\n\ts_waitcnt vmcnt(0)" : "=&v"(v) : "v"(off), "s"(base) : "memory");
    if (__builtin_amdgcn_ballot_w64(v < need) == 0ull) break;
    __builtin_amdgcn_s_sleep(3);
  }
}

constexpr int K3_RING = 0;
constexpr int K3_HH = 73728;
constexpr int K3_SSQ = K3_HH + 73728;
constexpr int K3_LDS = K3_SSQ + 4 * 3072;
#define DSTEP 2
#define XOFF 1
#define DMA0 1
#define SLOW0 7
constexpr int NS = 486, PH = 288, P2 = 324;

struct Sch {
  signed char own[162] = {}, u[162] = {}, db[162] = {}, t01[162] = {};
  short rs[NS + 4] = {}, lo[NS + 4] = {};
  signed char sown[90] = {}, su[90] = {}, sdb[90] = {}, pf[NS] = {};
  bool bar[NS] = {};
  signed char dslot[NS] = {}, dk[NS] = {}, prio[NS] = {};
  signed char e0[NS] = {}, ne0[NS] = {}, ne1[NS] = {}, eg[NS] = {};
  signed char og[NS] = {}, o0[NS] = {};
  signed char xrd[NS] = {}, cbar[NS] = {}, xst[NS] = {}, ost[NS] = {}, xop[NS] = {};
  signed char flag[NS] = {}, fbar[NS] = {}, poll[NS] = {}, chk[NS] = {}, ld[NS] = {}, wr[NS] = {}, rd[NS] = {};
  signed char wl[NS] = {}, wf[NS] = {};
  bool ok = true;
};
constexpr Sch make_sch() {
  Sch s;
  int n2 = 0, nb = 0;
  auto put = [&](int own, int u, int db) {
    s.sown[nb] = own; s.su[nb] = u; s.sdb[nb] = db;
    for (int t = 0; t < (db == 4 ? 1 : 2); ++t) {
      s.own[n2] = own; s.u[n2] = u; s.db[n2] = db; s.t01[n2] = t;
      s.rs[P2 + n2] = 27 + nb / 6; s.lo[P2 + n2] = ((27 + nb / 6) % 3) * 24 + (nb % 6) * 4; ++n2;
    }
    ++nb;
  };
  for (int u = 0; u < 9; ++u) for (int d = 0; d < 5; ++d) put(1, u, d);
  for (int u = 0; u < 6; ++u) { put(0, u, 4); put(0, u, 0); put(0, u, 1); }
  for (int u = 6; u < 9; ++u) { put(0, u, 4); put(0, u, 0); }
  for (int u = 6; u < 9; ++u) put(0, u, 1);
  for (int d = 2; d < 4; ++d) for (int u = 0; u < 9; ++u) put(0, u, d);
  for (int n = 0; n < P2; ++n) { s.rs[n] = n / 12; s.lo[n] = ((n / 12) % 3) * 24 + (n % 12) * 2; }
  for (int n = NS; n < NS + 4; ++n) { s.rs[n] = s.rs[NS - 1]; s.lo[n] = s.lo[NS - 1]; }
  for (int n = 0; n < NS; ++n) {
    s.bar[n] = s.rs[n + 3] != s.rs[n + 2];
    s.dslot[n] = -1; s.prio[n] = -1; s.eg[n] = -1; s.og[n] = -1; s.flag[n] = -1; s.fbar[n] = -1; s.poll[n] = 0; s.chk[n] = -1;
    s.ld[n] = -1; s.rd[n] = -1; s.wl[n] = -1; s.wf[n] = -1; s.xst[n] = -1; s.ost[n] = -1; s.xrd[n] = -1; s.cbar[n] = -1; s.pf[n] = 0;
  }
  for (int n = 0; n < NS; ++n) if (s.bar[n]) {
    int e = n + 1; while (e < NS && !s.bar[e]) ++e;
    const int L = e - n, tgt = s.rs[n + 3] + 2;
    if (tgt <= 41) for (int k = 0; k < 6; ++k) { s.dslot[n + (k * L) / 6 + 1] = tgt; s.dk[n + (k * L) / 6 + 1] = k; if ((k * L) / 6 + 1 >= L) s.ok = false; }
    if (e < NS) { s.prio[n + L / 2] = 2; s.prio[e - 3] = 1; s.prio[e - 1] = 0; }
  }
  auto nbar = [&](int n) { while (!s.bar[n]) ++n; return n; };
  for (int g = 0; g < 5; ++g) {
    const int start = (g < 4 ? 72 * (g + 1) : P2) + 2, tot = g == 4 ? 46 : 92;
    int e = 0;
    for (int n = start; e < tot; ++n) {
      const int c0 = g == 4 ? 2 : 1, c1 = g >= 3 ? 2 : ((n - start) & 1);
      if (s.eg[n] >= 0) s.ok = false;
      s.eg[n] = g; s.e0[n] = e; s.ne0[n] = c0; s.ne1[n] = c1;
      for (int i = e; i < e + c0 + c1 && i < tot; ++i) if (i % 46 == 45) { int m = n + 1; while (!s.bar[m]) ++m; m += XOFF; while (s.xrd[m] >= 0) m += 3; s.xrd[m] = 2 * g + i / 46; s.xst[m + 2] = 2 * g + i / 46; }
      e += c0 + c1;
      if (g < 3 && n >= start + 68) s.ok = false;
    }
  }
  s.og[P2 + 119] = 4; s.o0[P2 + 119] = 0; s.og[P2 + 120] = 4; s.o0[P2 + 120] = 4; s.ost[P2 + 124] = 4;
  const int opk[3] = {121, 127, 145}, ostl[3] = {125, 136, 149};
  for (int k = 0; k < 3; ++k) {
    for (int i = 0; i < 4; ++i) { const int n = P2 + opk[k] + i; s.og[n] = k; s.o0[n] = 2 * i; }
    s.ost[P2 + ostl[k]] = k;
  }
  int lds_[9] = {16, 18, 24, 34, 43, 44, 76, 78, 80}, rds[9] = {};
  for (int u = 0; u < 9; ++u) {
    const int cbn = nbar(P2 + lds_[(u / 3) * 3 + 2] + 17);
    rds[u] = cbn - P2 + 1 + (u < 6 ? 3 : 2) * (u % 3);
    s.ld[P2 + lds_[u]] = u; s.rd[P2 + rds[u]] = u; s.cbar[cbn] = u;
  }
  s.poll[P2 + 3] = 1; s.chk[P2 + 15] = 6;
  s.poll[P2 + 63] = 1; s.chk[P2 + 75] = 9;
  s.xop[28] = 3; s.xop[40] = 3;
  s.pf[100] = 1;
  const int fb[2] = {PH + 21, nbar(P2 + 40)}, fv[2] = {6, 9};
  for (int k = 0; k < 2; ++k) { if (!s.bar[fb[k]]) s.ok = false; s.fbar[fb[k]] = fv[k]; s.flag[fb[k] + 1] = fv[k]; }
  int cl[NS + 1] = {}, cf[NS + 1] = {};
  for (int n = 9; n < NS; ++n) {
    const int both = (s.ost[n] >= 0 ? 2 : 0) + s.xop[n];
    cl[n + 1] = cl[n] + both + (s.dslot[n] >= 0 ? 1 : 0);
    cf[n + 1] = cf[n] + both + (s.xst[n] >= 0 ? 2 : 0) + (s.flag[n] >= 0 ? 1 : 0) + s.poll[n] + (s.ld[n] >= 0 ? 2 : 0) + 2 * s.pf[n];
  }
  auto need = [&](int n, int ul_, int uf_) {
    if (ul_ >= 0) { const int w = cl[n] - ul_; if (s.wl[n] < 0 || w < s.wl[n]) s.wl[n] = w; }
    if (uf_ >= 0) { const int w = cf[n] - uf_; if (s.wf[n] < 0 || w < s.wf[n]) s.wf[n] = w; }
  };
  for (int n = 21; n < NS; ++n) {
    if (s.bar[n]) {
      int last = -1;
      for (int i = 9; i < n; ++i) if (s.dslot[i] == s.rs[n + 3]) last = i;
      need(n, last >= 0 ? cl[last] + 1 : 0, n == 21 ? 0 : -1);
      if (n == 45) need(n, cl[29], cf[29]);
      if (n == 57) need(n, cl[41], cf[41]);
      if (s.fbar[n] >= 0) {
        int at = -1;
        for (int i = 9; i < n; ++i) if (s.xst[i] == s.fbar[n] - 1) at = i;
        if (at < 0) s.ok = false;
        for (int i = 9; i < n; ++i) if (s.xst[i] >= 0 && s.xst[i] < s.fbar[n] && i > at) at = i;
        need(n, -1, cf[at + 1]);
      }
    }
    if (s.chk[n] >= 0) { int i = n; while (!s.poll[i]) --i; need(n, -1, cf[i + 1]); }
    if (s.cbar[n] >= 0) { int i = n; while (s.ld[i] != s.cbar[n]) --i; need(n, -1, cf[i + 1]); }
  }
  for (int u = 0; u < 9; ++u) {
    int own_rd = u == 0 ? P2 - 1 : P2 + 9 * (u - 1) + 4;
    for (int n = 0; n < NS; ++n) if (s.xrd[n] == u && n > own_rd) own_rd = n;
    bool b1 = false;
    for (int n = own_rd + 1; n <= P2 + lds_[u]; ++n) b1 = b1 || s.bar[n];
    int use = 81; while (s.u[use] != u) ++use;
    if (!b1 || rds[u] + 2 > use) s.ok = false;
  }
  return s;
}
constexpr Sch SCH = make_sch();
static_assert(SCH.ok, "schedule");

template <int I, int N, class F> DI void static_for(F&& f) {
  if constexpr (N - I == 1) f(std::integral_constant<int, I>{});
  else if constexpr (N - I > 1) { static_for<I, (I + N) / 2>(f); static_for<(I + N) / 2, N>(f); }
}
template <int N> DI void waitv() { asm volatile("s_waitcnt vmcnt(%0)" :: "n"(N) : "memory"); }
#define MFMA_DRAIN() do { asm volatile("s_nop 7\n\ts_nop 3" ::: "memory"); SB0(); } while (0)
typedef __attribute__((ext_vector_type(4))) int srd_t;
DI srd_t make_srd(const void* p, unsigned bytes) {
  const unsigned long long a = (unsigned long long)p;
  srd_t r = {(int)(unsigned)a, (int)((unsigned)(a >> 32) & 0xffffu), (int)bytes, 0x00020000};
  return r;
}
DI void bdma(srd_t srd, unsigned voff, unsigned soff, unsigned lds_dst) {
  asm volatile("s_mov_b32 m0, %3\n\ts_nop 0\n\tbuffer_load_dwordx4 %0, %1, %2 offen lds" :: "v"(voff), "s"(srd), "s"(soff), "s"(lds_dst) : "memory");
}
template <int LOFF, int SOFF> DI void bdma_i(srd_t srd, unsigned voff, unsigned sbase, unsigned lbase) {
  unsigned so;
  asm volatile("s_add_u32 m0, %4, %6\n\ts_add_u32 %0, %3, %5\n\tbuffer_load_dwordx4 %1, %2, %0 offen sc1 lds"
               : "=&s"(so) : "v"(voff), "s"(srd), "s"(sbase), "s"(lbase), "n"(SOFF), "n"(LOFF) : "memory", "scc");
}

template <int OFF> DI void bdma_sc1(srd_t srd, unsigned voff, unsigned lbase) {
  unsigned so;
  asm volatile("s_add_u32 m0, %3, %4\n\ts_mov_b32 %0, %4\n\tbuffer_load_dwordx4 %1, %2, %0 offen sc1 lds"
               : "=&s"(so) : "v"(voff), "s"(srd), "s"(lbase), "n"(OFF) : "memory", "scc");
}

template <int FJ>
DI void k3_body(const Ptrs& P, lds_char* lds, const int lane, const int mi, const int b, const int c, const int mb) {
  constexpr int fj = FJ;
  const unsigned lds_a = (unsigned)(uintptr_t)lds;
  const int mt = mb * 4 + mi, p = c & 1;
  const int g = lane >> 4;
  ST_DECL; ST();
  const unsigned voff = lane * 16;
  const srd_t sd = make_srd(P.wd, D_ * F_ * 2);
  const srd_t sw = make_srd(P.ws + OFF_WF + (size_t)c * WF_CH, (unsigned)WF_CH);
  const auto wsr = WS_RSRC(P);
  const int wid = fj * 4 + mi;
  {
    const int r = lane & 31, h = lane >> 5;
    const int hp = c;
    const int mt2 = mb * 4 + (c >> 2), cb = c & 3;
    u32x4 af[18], a4[5], xv0, xv1;
    float mean;
    const int k4 = FJ == 0 ? 5 * mi : 20 + 4 * mi;
    constexpr int n4 = FJ == 0 ? 5 : 4;
    const int row = mt2 * 32 + r, s0 = cb * 9 + 2 * (FJ == 0 ? mi : 4);
    auto prefetch2 = [&]() __attribute__((always_inline)) {
      const char* wo = P.ws + OFF_WOT + (size_t)cb * WOT_CB + h * 2336;
#pragma unroll
      for (int i = 0; i < 18; ++i) af[i] = *(const u32x4*)(wo + (18 * FJ + i) * 4672 + (32 * mi + r) * 16);
#pragma unroll
      for (int i = 0; i < n4; ++i) a4[i] = *(const u32x4*)(wo + (k4 + i) * 4672 + (128 + (r < 17 ? r : 17)) * 16);
      xv0 = *(const u32x4*)(P.x + (size_t)row * 1152 + s0 * 32 + h * 16);
      xv1 = *(const u32x4*)(P.x + (size_t)row * 1152 + (FJ == 0 ? s0 + 1 : s0) * 32 + h * 16);
      mean = ((const float*)(P.ws + OFF_MEANX))[row];
      SB0();
    };
    const int xj = c >> 1, j0 = (mb * 2 + p) * 10 + (mi & 1) * 5;
    const bool xtra = mi == 2 && mb < 2;
    const int stg5 = 32768 + (mi & 1) * 8192;
    if constexpr (FJ == 1) {
      const char* wsrc = P.ws + OFF_WQT + (size_t)hp * WQT_HP + mi * 1024;
      const unsigned wdst = lds_a + K1_W + mi * 1024;
#pragma unroll
      for (int i = 0; i < 14; ++i) dma16x1(wsrc + i * 4096, voff, wdst + i * 4096);
      WAITV(8); BAR();
#pragma unroll
      for (int i = 14; i < 20; ++i) dma16x1(wsrc + i * 4096, voff, wdst + i * 4096);
      WAITV(9); BAR();
#pragma unroll
      for (int i = 20; i < 26; ++i) dma16x1(wsrc + i * 4096, voff, wdst + i * 4096);
      WAITV(9); BAR();
#pragma unroll
      for (int i = 26; i < 31; ++i) dma16x1(wsrc + i * 4096, voff, wdst + i * 4096);
      WAITV(9); BAR();
      WAITV(0);
      if (mi >= 2) {
#pragma unroll
        for (int i = 0; i < 3; ++i) sub_load(P, xj, j0 + i, lane, lds_a + K1_ITEM + (mi & 1) * 16384 + i * 4096);
      } else {
        prefetch2();
      }
      BAR();
      ST();
      if (mi >= 2) {
        sub_load(P, xj, j0 + 3, lane, lds_a + K1_ITEM + (mi & 1) * 16384 + 3 * 4096);
        sub_load(P, xj, j0 + 4, lane, lds_a + stg5);
        sub_load(P, xj, xtra ? 320 + mb * 2 + p : j0 + 4, lane, lds_a + stg5 + 4096);
        WAITV(12);
#pragma unroll
        for (int i = 0; i < 3; ++i) sub_store(P, xj, j0 + i, lane, lds + K1_ITEM + (mi & 1) * 16384 + i * 4096);
      }
      if (mi < 2) {
        __builtin_amdgcn_s_setprio(3);
        const int q = (lane >> 2) & 3, op = 4 * (g & 1) + (lane & 3);
        const int vq = op >= 5 ? 19 : (mi ? 14 : 10) + op, kq = (mi ? 5 : 1) + op;
        lds_char* va = lds + (8 * (g >> 1) + q) * KVP + vq * 8;
        lds_char* ka = lds + (8 * (g >> 1) + q) * KVP + kq * 8;
        f32x16 kv;
#pragma unroll
        for (int i = 0; i < 16; ++i) kv[i] = 0.f;
#pragma unroll
        for (int ks = 0; ks < 8; ++ks) {
          u32x4 a = tr_frag(va + ks * 16 * KVP, va + ks * 16 * KVP + 4 * KVP);
          u32x4 bb = tr_frag(ka + ks * 16 * KVP, ka + ks * 16 * KVP + 4 * KVP);
          kv = MFMA32(as_frag(a), as_frag(bb), kv);
        }
        const int d = r - 2 * mi;
        const int dd = mi ? (d >= 2 ? d - 2 : 16 + d) : d;
        float* dst = (float*)(P.ws + OFF_KV) + (2 * hp + mi) * 400 + dd;
        const bool dok = d >= 0 && d < 18;
#pragma unroll
        for (int i = 0; i < 12; ++i) {
          const int rw = (i & 3) + 8 * (i >> 2) + 4 * h;
          const int e = rw == 20 ? 18 : rw - 2 * mi;
          if (dok && (rw == 20 || (e >= 0 && e < 18 && rw < 20))) atomicAdd(dst + e * 20, kv[i]);
        }
        WAITV(0);
      }
    } else {
      u32x4 xf[36];
      const char* xsrc = P.ws + OFF_XN + (size_t)mt * 36864 + lane * 16;
#pragma unroll
      for (int s = 0; s < 18; ++s) xf[s] = *(const u32x4*)(xsrc + s * 1024);
      f32x16 acc[3], accq;
#pragma unroll
      for (int i = 0; i < 16; ++i) { acc[0][i] = 0.f; acc[1][i] = 0.f; acc[2][i] = 0.f; accq[i] = 0.f; }
      lds_char* wa = lds + K1_W + (h * 76 + r) * 16;
      lds_char* wa2 = lds + K1_W + (h * 76 + 64 + (r < 12 ? r : 11)) * 16;
      u32x4 wf[2][3];
#pragma unroll
      for (int s = 0; s < 36; ++s) {
        if (s % 9 == 0) {
          BAR();
          if (s < 18) {
#pragma unroll
            for (int k = s + 18; k < s + 27; ++k) xf[k] = *(const u32x4*)(xsrc + k * 1024);
          }
          wf[s & 1][0] = *(lds_u32x4*)(wa + s * 2432); wf[s & 1][1] = *(lds_u32x4*)(wa + s * 2432 + 512); wf[s & 1][2] = *(lds_u32x4*)(wa2 + s * 2432);
        }
        if (s % 9 != 8) {
          wf[(s + 1) & 1][0] = *(lds_u32x4*)(wa + (s + 1) * 2432); wf[(s + 1) & 1][1] = *(lds_u32x4*)(wa + (s + 1) * 2432 + 512);
          wf[(s + 1) & 1][2] = *(lds_u32x4*)(wa2 + (s + 1) * 2432);
        }
        SB0();
#pragma unroll
        for (int t = 0; t < 3; ++t) acc[t] = MFMA32(as_frag(wf[s & 1][t]), as_frag(xf[s]), acc[t]);
        SB0();
      }
      ST();
      lds_char* kvrow = lds + (mi * 32 + r) * KVP;
      u32x2 qrem;
#pragma unroll
      for (int gi = 0; gi < 10; ++gi) {
        const int t = gi >> 2, gg = gi & 3;
        u32x2 o;
        if (gi < 5) { o[0] = pk2(phi(acc[t][4 * gg]), phi(acc[t][4 * gg + 1])); o[1] = pk2(phi(acc[t][4 * gg + 2]), phi(acc[t][4 * gg + 3])); }
        else { o[0] = pk2(acc[t][4 * gg], acc[t][4 * gg + 1]); o[1] = pk2(acc[t][4 * gg + 2], acc[t][4 * gg + 3]); }
        if (gi == 9 && h == 1) { o[0] = 0x3f803f80u; o[1] = 0x3f803f80u; }
        *(lds_u32x2*)(kvrow + (2 * gi + h) * 8) = o;
        if (gi == 0) qrem = o;
      }
      WAITL0();
      BAR();
      lds_char* wq = lds + K1_W + WQT_Q + lane * 16;
      u32x4 aq[2][4];
#pragma unroll
      for (int i = 0; i < 4; ++i) aq[0][i] = *(lds_u32x4*)(wq + i * 1024);
#pragma unroll
      for (int g3 = 0; g3 < 9; ++g3) {
        if (g3 < 8) {
#pragma unroll
          for (int i = 0; i < 4; ++i) aq[(g3 + 1) & 1][i] = *(lds_u32x4*)(wq + (4 * g3 + 4 + i) * 1024);
        }
        SB0();
#pragma unroll
        for (int i = 0; i < 4; ++i) accq = MFMA32(as_frag(aq[g3 & 1][i]), as_frag(xf[4 * g3 + i]), accq);
        SB0();
      }
      const int pq = (int)OFF_PQ + mt * 36864;
#pragma unroll
      for (int e = 0; e < 2; ++e) {
        u32x4 o;
#pragma unroll
        for (int k = 0; k < 4; ++k) o[k] = pk2(phi(accq[8 * e + 2 * k]), phi(accq[8 * e + 2 * k + 1]));
        WS_ST16(P, pq + (2 * hp + e) * 1024 + lane * 16, o, 16);
      }
      if (h == 0) WS_ST8(P, pq + (32 + (hp >> 2)) * 1024 + (r + 32 * ((hp >> 1) & 1)) * 16 + 8 * (hp & 1), qrem, 16);
      WAITV(0);
    }
    BAR();
    ST();
    if (FJ == 1 && mi == 0) {
      if (lane < 16) __hip_atomic_fetch_add((unsigned*)(P.ws + OFF_G1 + lane * 2048 + (b & 15) * 128), 1u, __ATOMIC_RELAXED, __HIP_MEMORY_SCOPE_AGENT);
      poll_counters(P.ws, (int)OFF_G1 + (b >> 4) * 2048 + (lane & 15) * 128, 16u);
    }
    if constexpr (FJ == 0) { __builtin_amdgcn_s_sleep(4); prefetch2(); }
    if (FJ == 1 && mi >= 2) {
      WAITV(12);
      sub_store(P, xj, j0 + 3, lane, lds + K1_ITEM + (mi & 1) * 16384 + 3 * 4096);
      sub_store(P, xj, j0 + 4, lane, lds + stg5);
      if (xtra) sub_store(P, xj, 320 + mb * 2 + p, lane, lds + stg5 + 4096);
      WAITL0();
      prefetch2();
    }
    BAR();
    ST();
    const int e = r < 16 ? r : (r == 16 ? 16 : (r == 20 ? 17 : 18));
    const bool rok = r <= 17 || r == 20 || r == 21;
    u32x4 k0[4], k1[4], qf[4]; u32x2 k2[4];
    dma16x3s(P.ws + OFF_KV + wid * 6400, voff, lds_a + wid * 6144);
    dma16x3s(P.ws + OFF_KV + wid * 6400 + 3072, voff, lds_a + wid * 6144 + 3072);
    const u32x4 ktail = __builtin_amdgcn_raw_buffer_load_b128(wsr, (lane & 15) * 16, (int)OFF_KV + wid * 6400 + 6144, 16);
#pragma unroll
    for (int i = 0; i < 4; ++i) qf[i] = __builtin_amdgcn_raw_buffer_load_b128(wsr, voff, (int)OFF_PQ + mt2 * 36864 + (4 * wid + i) * 1024, 16);
    const u32x4 qr = __builtin_amdgcn_raw_buffer_load_b128(wsr, voff, (int)OFF_PQ + mt2 * 36864 + (32 + (wid >> 1)) * 1024, 16);
    *(lds_u32x4*)(lds + K2_RED + wid * 256 + (lane & 15) * 16) = ktail;
    asm volatile("" ::: "memory");
    SB0();
#pragma unroll
    for (int i = 0; i < 4; ++i) {
      const int o = i * 1600 + e * 80;
      lds_char* kp0 = lds + (o + 16 * h < 6144 ? wid * 6144 + o + 16 * h : K2_RED + wid * 256 + o + 16 * h - 6144);
      lds_char* kp1 = lds + (o + 16 * h + 32 < 6144 ? wid * 6144 + o + 16 * h + 32 : K2_RED + wid * 256 + o + 16 * h + 32 - 6144);
      lds_char* kp2 = lds + (o + 64 < 6144 ? wid * 6144 + o + 64 : K2_RED + wid * 256 + o + 64 - 6144);
      k0[i] = *(lds_u32x4*)kp0; k1[i] = *(lds_u32x4*)kp1; k2[i] = *(lds_u32x2*)kp2;
    }
    float orem[4];
#define F_(x) __uint_as_float(x)
#pragma unroll
    for (int i = 0; i < 4; ++i) {
      u32x4 a1 = {pk2(F_(k0[i][0]), F_(k0[i][1])), pk2(F_(k0[i][2]), F_(k0[i][3])), pk2(F_(k1[i][0]), F_(k1[i][1])), pk2(F_(k1[i][2]), F_(k1[i][3]))};
      u32x4 a2 = {0u, 0u, 0u, 0u};
      a2[i] = (h == (mi & 1)) ? pk2(F_(k2[i][0]), F_(k2[i][1])) : 0u;
      if (!rok) { a1 = (u32x4){0u, 0u, 0u, 0u}; a2[i] = 0u; }
      f32x16 cc;
#pragma unroll
      for (int j = 0; j < 16; ++j) cc[j] = 0.f;
      cc = MFMA32(as_frag(a1), as_frag(qf[i]), cc);
      cc = MFMA32(as_frag(a2), as_frag(qr), cc);
      const float inv = __builtin_amdgcn_rcpf(cc[9] + 1e-6f);
      float o[9];
#pragma unroll
      for (int j = 0; j < 9; ++j) o[j] = cc[j] * inv;
      u32x4 of = {pk2(o[0], o[1]), pk2(o[2], o[3]), pk2(o[4], o[5]), pk2(o[6], o[7])};
      *(lds_u32x4*)(lds + K2_O + (4 * wid + i) * 1024 + lane * 16) = of;
      orem[i] = o[8];
    }
#undef F_
    {
      u32x2 o2 = {pk2(orem[0], orem[1]), pk2(orem[2], orem[3])};
      *(lds_u32x2*)(lds + K2_O + (32 + (wid >> 1)) * 1024 + lane * 16 + 8 * (mi & 1)) = o2;
    }
    if (FJ == 1 && mi >= 2) WAITV(0);
    WAITL0();
    BAR();
    ST();
    if (FJ == 1 && mi == 3 && lane == 0) __hip_atomic_fetch_add((unsigned*)(P.ws + OFF_GW + xj * 128), 1u, __ATOMIC_RELAXED, __HIP_MEMORY_SCOPE_AGENT);
    f32x16 acc, acc4;
#pragma unroll
    for (int j = 0; j < 16; ++j) { acc[j] = 0.f; acc4[j] = 0.f; }
    lds_char* ob = lds + K2_O + lane * 16;
#pragma unroll
    for (int i = 0; i < n4; ++i) acc4 = MFMA32(as_frag(a4[i]), as_frag(*(lds_u32x4*)(ob + (k4 + i) * 1024)), acc4);
#pragma unroll
    for (int i = 0; i < 18; ++i) acc = MFMA32(as_frag(af[i]), as_frag(*(lds_u32x4*)(ob + (18 * FJ + i) * 1024)), acc);
    {
      *(lds_float*)(lds + K2_MEAN + (wid * 64 + lane) * 4) = acc4[8] + acc4[9];
      lds_char* p4 = lds + K2_RED + 16384 + wid * 2048 + lane * 16;
      *(lds_f32x4*)(p4) = (f32x4){acc4[0], acc4[1], acc4[2], acc4[3]};
      *(lds_f32x4*)(p4 + 1024) = (f32x4){acc4[4], acc4[5], acc4[6], acc4[7]};
      if (FJ == 1) {
        lds_char* pr = lds + K2_RED + mi * 4096 + lane * 16;
#pragma unroll
        for (int k = 0; k < 4; ++k) *(lds_f32x4*)(pr + k * 1024) = (f32x4){acc[4 * k], acc[4 * k + 1], acc[4 * k + 2], acc[4 * k + 3]};
      }
    }
    WAITL0();
    BAR();
    if (FJ == 0 || mi == 0) {
#pragma unroll
      for (int w = 0; w < 8; ++w) mean += *(lds_float*)(lds + K2_MEAN + (w * 64 + r) * 4);
      if constexpr (FJ == 0) {
        lds_char* pr = lds + K2_RED + mi * 4096 + lane * 16;
#pragma unroll
        for (int k = 0; k < 4; ++k) {
          f32x4 v = *(lds_f32x4*)(pr + k * 1024);
#pragma unroll
          for (int j = 0; j < 4; ++j) acc[4 * k + j] += v[j];
        }
      } else {
#pragma unroll
        for (int j = 0; j < 8; ++j) acc[j] = 0.f;
#pragma unroll
        for (int w = 0; w < 8; ++w) {
          lds_char* p4 = lds + K2_RED + 16384 + w * 2048 + lane * 16;
          f32x4 v0 = *(lds_f32x4*)(p4), v1 = *(lds_f32x4*)(p4 + 1024);
#pragma unroll
          for (int j = 0; j < 4; ++j) { acc[j] += v0[j]; acc[4 + j] += v1[j]; }
        }
      }
      float ssq = 0.f;
#pragma unroll
      for (int i = 0; i < (FJ == 0 ? 2 : 1); ++i) {
        float v[8];
#pragma unroll
        for (int j = 0; j < 4; ++j) {
          auto sw2 = __builtin_amdgcn_permlane32_swap(__float_as_uint(acc[8 * i + j]), __float_as_uint(acc[8 * i + 4 + j]), false, false);
          v[j] = __uint_as_float(sw2[0]); v[4 + j] = __uint_as_float(sw2[1]);
        }
        const u32x4 xv = i ? xv1 : xv0;
        u32x4 o;
#pragma unroll
        for (int k = 0; k < 4; ++k) {
          const float a0 = v[2 * k] + bflo(xv[k]) - mean, a1 = v[2 * k + 1] + bfhi(xv[k]) - mean;
          ssq += a0 * a0 + a1 * a1;
          o[k] = pk2(a0, a1);
        }
        const int d8 = 2 * (s0 + i) + h;
        WS_ST16(P, OFF_HC + (((row >> 4) * 18 + (d8 >> 2)) * 64 + (row & 15) + 16 * (d8 & 3)) * 16, o, 16);
      }
      ssq += __shfl_xor(ssq, 32);
      if (h == 0) {
        WS_ST4(P, OFF_SSQ + ((mt2 * 20 + cb * 5 + wid) * 32 + r) * 4, __float_as_uint(ssq), 16);
        if (cb == 0 && wid == 0) WS_ST4(P, OFF_MEANH + row * 4, __float_as_uint(mean), 16);
      }
    }
    WAITV(0); WAITL0();
    BAR();
    ST();
    if (FJ == 1 && mi == 1) {
      const int off = lane & 1 ? (int)(OFF_GW - OFF_G3) + xj * 128 : mb * 128;
      if (lane == 0) __hip_atomic_fetch_add((unsigned*)(P.ws + OFF_G3 + off), 1u, __ATOMIC_RELAXED, __HIP_MEMORY_SCOPE_AGENT);
      poll_counters(P.ws, (int)OFF_G3 + off, lane & 1 ? 32u : 16u);
    }
    BAR();
    ST();
  }
  const char* hsrc = P.ws + OFF_HC + (size_t)mt * 36864;
  const char* hmine = hsrc + fj * 18432;
  constexpr int ST0 = K3_RING + 2 * 24576, ST1 = K3_HH, ST2 = K3_HH + 24576;
  const unsigned ring_w = lds_a + K3_RING + mi * 6144;
  const unsigned sw0 = mi * 6144;
  auto first_slot = [&](auto tc) __attribute__((always_inline)) {
    constexpr int t = decltype(tc)::value;
    static_for<3 * FJ, 3 * FJ + 3>([&](auto kc) __attribute__((always_inline)) {
      constexpr int k = decltype(kc)::value;
      bdma_i<t * 24576 + k * 1024, t * 24576 + k * 1024>(sw, voff, sw0, ring_w);
    });
  };
  dma16x3s(hmine, voff, lds_a + ST0 + (mi * 2 + fj) * 3072);
  first_slot(std::integral_constant<int, 0>{});
  dma16x3s(hmine + 3072, voff, lds_a + ST1 + (mi * 2 + fj) * 3072);
  first_slot(std::integral_constant<int, 1>{});
  dma16x3s(hmine + 6144, voff, lds_a + ST2 + (mi * 2 + fj) * 6144);
  dma16x3s(hmine + 9216, voff, lds_a + ST2 + (mi * 2 + fj) * 6144 + 3072);
  u32x4 hf[2][18];
  WAITV(12);
  BAR();
  lds_char* ring_rd = lds + K3_RING + fj * 1024 + lane * 16;
  lds_char* hh_rd = lds + K3_HH + mi * 18432 + lane * 16;
  u32x4 a[6];
  auto read_frag = [&](auto nc) __attribute__((always_inline)) {
    constexpr int n = decltype(nc)::value;
    if constexpr (n < P2) a[n % 6] = *(lds_u32x4*)(ring_rd + SCH.lo[n] * 1024);
    else if constexpr (n < NS) {
      const int q = (lane >> 2) & 3, xg = ((q >> 1) & 1) | ((g & 1) << 1);
      const int gr = SCH.db[n - P2] == 4 ? 2 * p + fj : 2 * fj + SCH.t01[n - P2];
      lds_char* r = lds + K3_RING + (4 * g + q) * 128 + ((gr ^ xg) * 32) + 8 * (lane & 3) + SCH.lo[n] * 1024;
      a[n % 6] = tr_frag(r, r + 2048);
    }
  };
#pragma unroll
  for (int i = 0; i < 6; ++i) hf[i & 1][i >> 1] = *(lds_u32x4*)(lds + ST0 + (mi * 2 + (i & 1)) * 3072 + (i >> 1) * 1024 + lane * 16);
  static_for<0, 3>(read_frag);
  WAITL0();
  BAR();
  first_slot(std::integral_constant<int, 2>{});
  ST();
  const int fl_mine = (int)OFF_FLAG + b * 128;
  const char* fl_other = P.ws + OFF_FLAG + (b ^ 8) * 128;
  const int xh_off = (int)OFF_XH + (b * 4 + mi) * 18432;
  const srd_t sx = make_srd(P.ws + OFF_XH + (size_t)((b ^ 8) * 4 + mi) * 18432, 18432);
  const int pbase = (int)OFF_PART + ((mt * 36 + 9 * p) * 8 + (c >> 1)) * 1024;
  lds_char* hh_wr = hh_rd + 8 * fj;
  float sigma[2], gsc[2];
  f32x4 acc[2][2][2][2];
  f32x4 oc[9][2];
  u32x4 ov[2], ho[2][2], hp[9][2], xl[2];
  u32x2 hpk;
  u32x2 ovs[2];
  unsigned pollv, pfs = 0;

  auto epi = [&](auto gc, auto ic) __attribute__((always_inline)) {
    constexpr int G = decltype(gc)::value, i = decltype(ic)::value;
    if constexpr (i < (G == 4 ? 46 : 92)) {
      constexpr int uu = i / 46, mh = (i % 46) / 23, r = i % 23, set = G & 1, U = 2 * G + uu;
      if constexpr (r < 20) {
        constexpr int e = (r / 10) * 2 + (r & 1), ph = (r % 10) >> 1;
        float tv = acc[set][uu][0][mh][e];
        if (ph == 0) tv = tv * gsc[mh];
        if (ph == 1) tv = __builtin_amdgcn_exp2f(tv);
        if (ph == 2) tv = __builtin_fmaf(tv, sigma[mh], sigma[mh]);
        if (ph == 3) tv = __builtin_amdgcn_rcpf(tv);
        if (ph == 4) tv = tv * acc[set][uu][1][mh][e];
        asm volatile("" : "+v"(tv));
        acc[set][uu][0][mh][e] = tv;
      } else if constexpr (r < 22) {
        unsigned t = pk2(acc[set][uu][0][mh][2 * (r - 20)], acc[set][uu][0][mh][2 * (r - 20) + 1]);
        asm volatile("" : "+v"(t));
        hpk[r - 20] = t;
      } else {
        *(lds_u32x2*)(hh_wr + mh * 9216 + U * 1024) = hpk;
      }
      SB0();
    }
  };
  auto opack = [&](const int pr, const int i) __attribute__((always_inline)) {
    const int mh = i >> 2, r = i & 3;
    if (pr == 4 && r >= 2) return;
    const int ti = pr == 4 ? 8 : 2 * pr + (r >> 1), e = pr == 4 ? 2 * r : 2 * (r & 1);
    unsigned t = pk2(oc[ti][mh][e], oc[ti][mh][e + 1]);
    asm volatile("" : "+v"(t));
    if (pr == 4) ovs[mh][r & 1] = t; else ov[mh][r] = t;
    SB0();
  };
  auto ostore = [&](const int pr) __attribute__((always_inline)) {
#pragma unroll
    for (int mh = 0; mh < 2; ++mh) {
      if (pr < 4) st128(ov[mh], wsr, voff, pbase + (p + 2 * pr + fj + 18 * mh) * 8192);
      else { __builtin_amdgcn_raw_buffer_store_b64(ovs[mh], wsr, voff, pbase + ((p ? 0 : 8) + 18 * mh) * 8192 + 8 * fj, 16); }
    }
    SB0();
  };

  static_for<0, NS>([&](auto nc) __attribute__((always_inline)) {
    constexpr int n = decltype(nc)::value;
    constexpr bool bar = SCH.bar[n];
    if constexpr (n == 9) WAITV(12);
    else if constexpr (FJ == 0 && SCH.wl[n] >= 0) waitv<SCH.wl[n]>();
    else if constexpr (FJ == 1 && SCH.wf[n] >= 0) waitv<SCH.wf[n]>();
    if constexpr (FJ == 1 && SCH.chk[n] >= 0) {
      asm volatile("" : "+v"(pollv));
      if (__builtin_amdgcn_readfirstlane(pollv) < (unsigned)SCH.chk[n]) {
#pragma unroll 1
        for (int spin = 0; spin < (1 << 22); ++spin) {
          asm volatile("global_load_dword %0, %1, %2 sc1\n\ts_waitcnt vmcnt(0)" : "=&v"(pollv) : "v"(0u), "s"(fl_other) : "memory");
          if (__builtin_amdgcn_readfirstlane(pollv) >= (unsigned)SCH.chk[n]) break;
          __builtin_amdgcn_s_sleep(2);
        }
      }
    }
    if constexpr (bar) { WAITL0(); BAR(); __builtin_amdgcn_s_setprio(3); }
    const u32x4 af = a[n % 6];
    if constexpr (SCH.prio[n] >= 0) __builtin_amdgcn_s_setprio(SCH.prio[n]);
    if constexpr (n == 72 || n == P2) ST();
    SB0();
    static_for<0, 2>([&](auto mc) __attribute__((always_inline)) {
      constexpr int mh = decltype(mc)::value;
      if constexpr (n < P2) {
        constexpr int y = n % 72;
        constexpr int set = (n / 72) & 1, S = n >= PH ? y >> 1 : y >> 2, mat = n >= PH ? y & 1 : (y >> 1) & 1, uu = n >= PH ? 0 : y & 1;
        if constexpr (S == 0) asm volatile("v_mfma_f32_16x16x32_bf16 %0, %1, %2, 0" : "=&v"(acc[set][uu][mat][mh]) : "v"(af), "v"(hf[mh][S]));
        else asm volatile("v_mfma_f32_16x16x32_bf16 %0, %1, %2, %0" : "+v"(acc[set][uu][mat][mh]) : "v"(af), "v"(hf[mh][S]));
      } else {
        constexpr int s2 = n - P2, U = SCH.u[s2], ti = SCH.db[s2] == 4 ? 8 : 2 * SCH.db[s2] + SCH.t01[s2];
        if constexpr (s2 < 9) asm volatile("v_mfma_f32_16x16x32_bf16 %0, %1, %2, 0" : "=&v"(oc[ti][mh]) : "v"(af), "v"(ho[0][mh]));
        else if constexpr (SCH.own[s2]) asm volatile("v_mfma_f32_16x16x32_bf16 %0, %1, %2, %0" : "+v"(oc[ti][mh]) : "v"(af), "v"(ho[U & 1][mh]));
        else asm volatile("v_mfma_f32_16x16x32_bf16 %0, %1, %2, %0" : "+v"(oc[ti][mh]) : "v"(af), "v"(hp[U][mh]));
      }
      SB0();
      if constexpr (mh == 0) { read_frag(std::integral_constant<int, n + 3>{}); SB0(); }
      if constexpr (SCH.eg[n] >= 0) {
        constexpr int f = SCH.e0[n] + (mh ? SCH.ne0[n] : 0), cnt = mh ? SCH.ne1[n] : SCH.ne0[n];
        static_for<f, f + cnt>([&](auto ic) __attribute__((always_inline)) { epi(std::integral_constant<int, SCH.eg[n]>{}, ic); });
      }
      if constexpr (SCH.og[n] >= 0) opack(SCH.og[n], SCH.o0[n] + mh);
    });
    if constexpr (FJ == 0 && SCH.dslot[n] >= 0) {
      constexpr int t = SCH.dslot[n], k = SCH.dk[n];
      if constexpr (t >= 27) {
        constexpr int sb = (t - 27) * 6 + k, ow = SCH.sown[sb], U = SCH.su[sb], d = SCH.sdb[sb];
        const unsigned vd = (lane >> 3) * (D_ * 2) + ((((lane >> 1) & 3) ^ ((lane >> 4) & 3)) * 32) + (lane & 1) * 16;
        bdma_i<(t % 3) * 24576 + k * 4096, 32 * U * (D_ * 2) + (d == 4 ? 512 : 128 * d)>(
            sd, vd, (288 * (ow ? c : c ^ 1) + 8 * mi) * (D_ * 2) + (d == 4 ? 0 : 640 * p), lds_a + K3_RING + mi * 1024);
      } else if constexpr (k % 3 == 0) bdma_i<(t % 3) * 24576 + k * 1024, t * 24576 + k * 1024>(sw, voff, sw0, ring_w);
      else asm volatile("buffer_load_dwordx4 %0, %1, %2 offen offset:%3 sc1 lds" :: "v"(voff), "s"(sw), "s"(sw0 + t * 24576 + (k / 3) * 3072), "n"((k % 3) * 1024) : "memory");
      SB0();
    }
    if constexpr (SCH.ost[n] >= 0) ostore(SCH.ost[n]);
    if constexpr (n == 5) { dma16x3s(P.ws + OFF_SSQ + mt * 2560, voff, lds_a + K3_SSQ + mi * 3072); SB0(); }
    if constexpr (n == 28) { dma16x3s(hmine + 12288, voff, lds_a + ST1 + (mi * 2 + fj) * 3072); SB0(); }
    if constexpr (n == 40) { dma16x3s(hmine + 15360, voff, lds_a + ST2 + (mi * 2 + fj) * 3072); SB0(); }
    if constexpr (n == 9 || n == 45 || n == 57) {
      constexpr int s0 = n == 9 ? 3 : (n == 45 ? 12 : 15), stg = n == 57 ? ST2 : ST1;
#pragma unroll
      for (int i = 0; i < 6; ++i) hf[i & 1][s0 + (i >> 1)] = *(lds_u32x4*)(lds + stg + (mi * 2 + (i & 1)) * 3072 + (i >> 1) * 1024 + lane * 16);
      SB0();
    }
    if constexpr (n == 21) {
#pragma unroll
      for (int i = 0; i < 12; ++i) hf[i & 1][6 + (i >> 1)] = *(lds_u32x4*)(lds + ST2 + (mi * 2 + (i & 1)) * 6144 + (i >> 1) * 1024 + lane * 16);
      SB0();
    }
    if constexpr (n == 50) {
#pragma unroll
      for (int mh = 0; mh < 2; ++mh) {
        float ssq = 0.f;
#pragma unroll
        for (int i = 0; i < 20; ++i) ssq += *(lds_float*)(lds + K3_SSQ + mi * 3072 + (i * 32 + mh * 16 + (lane & 15)) * 4);
        sigma[mh] = sqrtf(ssq * (1.f / D_) + LN_EPS);
        gsc[mh] = -1.4426950408889634f / sigma[mh];
        asm volatile("" : "+v"(sigma[mh]), "+v"(gsc[mh]));
      }
      SB0();
    }
    if constexpr (n == P2 - 1) { ho[0][0] = *(lds_u32x4*)(hh_rd); ho[0][1] = *(lds_u32x4*)(hh_rd + 9216); SB0(); }
    if constexpr (n >= P2 && n < P2 + 72 && (n - P2) % 9 == 4) {
      constexpr int U = (n - P2) / 9 + 1;
      ho[U & 1][0] = *(lds_u32x4*)(hh_rd + U * 1024); ho[U & 1][1] = *(lds_u32x4*)(hh_rd + 9216 + U * 1024);
      SB0();
    }
    if constexpr (FJ == 1) {
      if constexpr (SCH.xst[n] >= 0) {
        st128(xl[0], wsr, voff, xh_off + SCH.xst[n] * 1024);
        st128(xl[1], wsr, voff, xh_off + 9216 + SCH.xst[n] * 1024);
        SB0();
      }
      if constexpr (SCH.flag[n] >= 0) { __builtin_amdgcn_raw_buffer_store_b32((unsigned)SCH.flag[n], wsr, 0, fl_mine, 16); SB0(); }
      if constexpr (SCH.poll[n]) { asm volatile("global_load_dword %0, %1, %2 sc1" : "=&v"(pollv) : "v"(0u), "s"(fl_other) : "memory"); SB0(); }
      if constexpr (SCH.ld[n] >= 0) {
        constexpr int u = SCH.ld[n];
        bdma_sc1<u * 1024>(sx, voff, lds_a + K3_HH + mi * 18432);
        bdma_sc1<9216 + u * 1024>(sx, voff, lds_a + K3_HH + mi * 18432);
        SB0();
      }
      if constexpr (SCH.pf[n]) {
        const int l41 = lane < 41 ? lane : 0, o = (((mb & 7) * 2 + p) * 4 + mi) * 10368 + l41 * 256;
        const char* pa = P.wd + (size_t)(c >> 1) * 663552 + (o < 663552 - 256 ? o : 663552 - 256);
        asm volatile("global_load_dword %0, %1, off\n\tglobal_load_dword %0, %1, off offset:128" : "+v"(pfs) : "v"(pa) : "memory");
        SB0();
      }
      if constexpr (SCH.flag[n] == 6) asm volatile("" :: "v"(pfs));
      if constexpr (SCH.xrd[n] >= 0) {
        xl[0] = *(lds_u32x4*)(hh_rd + SCH.xrd[n] * 1024); xl[1] = *(lds_u32x4*)(hh_rd + 9216 + SCH.xrd[n] * 1024);
        SB0();
      }
    }
    if constexpr (SCH.rd[n] >= 0) {
      constexpr int u = SCH.rd[n];
      hp[u][0] = *(lds_u32x4*)(hh_rd + u * 1024); hp[u][1] = *(lds_u32x4*)(hh_rd + 9216 + u * 1024);
      SB0();
    }
  });
  ST();
  MFMA_DRAIN();
#pragma unroll
  for (int i = 0; i < 8; ++i) opack(3, i);
  ostore(3);
  const int t16 = mb * 8 + (c >> 1), f0 = t16 * 18 + 9 * p;
  const int l8 = 8 * wid + (lane & 7);
  const int row = t16 * 16 + (lane & 15), row8 = t16 * 16 + (l8 & 15), g8 = l8 >> 4;
  const int hb = ((lane & 15) + 16 * (g >> 1)) * 16 + 8 * (g & 1), hb8 = ((l8 & 15) + 16 * (g8 >> 1)) * 16 + 8 * (g8 & 1);
  const u32x2 h0 = __builtin_amdgcn_raw_buffer_load_b64(wsr, hb, (int)OFF_HC + (f0 + wid) * 1024, 16);
  const u32x2 h1 = __builtin_amdgcn_raw_buffer_load_b64(wsr, hb + 512, (int)OFF_HC + (f0 + wid) * 1024, 16);
  const u32x2 h80 = __builtin_amdgcn_raw_buffer_load_b64(wsr, hb8, (int)OFF_HC + (f0 + 8) * 1024, 16);
  const u32x2 h81 = __builtin_amdgcn_raw_buffer_load_b64(wsr, hb8 + 512, (int)OFF_HC + (f0 + 8) * 1024, 16);
  const float mean = __uint_as_float(__builtin_amdgcn_raw_buffer_load_b32(wsr, row * 4, (int)OFF_MEANH, 16));
  const float mean8 = __uint_as_float(__builtin_amdgcn_raw_buffer_load_b32(wsr, row8 * 4, (int)OFF_MEANH, 16));
  WAITV(0);
  BAR();
  ST();
  if (FJ == 0 && mi == 0) {
    unsigned* cnt = (unsigned*)(P.ws + OFF_CNT + (mb * 2 + p) * 128);
    if (lane == 0) __hip_atomic_fetch_add(cnt, 1u, __ATOMIC_RELAXED, __HIP_MEMORY_SCOPE_AGENT);
#pragma unroll 1
    for (int spin = 0; spin < (1 << 22); ++spin) {
      asm volatile("global_load_dword %0, %1, %2 sc1\n\ts_waitcnt vmcnt(0)" : "=&v"(pollv) : "v"(0u), "s"(cnt) : "memory");
      if (__builtin_amdgcn_readfirstlane(pollv) >= 8u) break;
      __builtin_amdgcn_s_sleep(1);
    }
  }
  BAR();
  ST();
  u32x4 pv[8], p8;
#pragma unroll
  for (int k = 0; k < 8; ++k) pv[k] = __builtin_amdgcn_raw_buffer_load_b128(wsr, voff, (int)OFF_PART + ((f0 + wid) * 8 + k) * 1024, 16);
  p8 = __builtin_amdgcn_raw_buffer_load_b128(wsr, (lane >> 3) * 1024 + l8 * 16, (int)OFF_PART + (f0 + 8) * 8192, 16);
  float o[8], o8[8];
  o[0] = bflo(h0[0]); o[1] = bfhi(h0[0]); o[2] = bflo(h0[1]); o[3] = bfhi(h0[1]);
  o[4] = bflo(h1[0]); o[5] = bfhi(h1[0]); o[6] = bflo(h1[1]); o[7] = bfhi(h1[1]);
#pragma unroll
  for (int k = 0; k < 8; ++k) o[k] += mean;
#pragma unroll
  for (int r = 0; r < 8; ++r)
#pragma unroll
    for (int k = 0; k < 4; ++k) { o[2 * k] += bflo(pv[r][k]); o[2 * k + 1] += bfhi(pv[r][k]); }
  {
    char* d = P.out + (size_t)row * 1152 + (9 * p + wid) * 64 + g * 8;
    *(u32x2*)d = (u32x2){pk2(o[0], o[1]), pk2(o[2], o[3])};
    *(u32x2*)(d + 32) = (u32x2){pk2(o[4], o[5]), pk2(o[6], o[7])};
  }
#pragma unroll
  for (int k = 0; k < 4; ++k) { o8[2 * k] = bflo(p8[k]); o8[2 * k + 1] = bfhi(p8[k]); }
#pragma unroll
  for (int k = 0; k < 8; ++k) { o8[k] += __shfl_xor(o8[k], 8); o8[k] += __shfl_xor(o8[k], 16); o8[k] += __shfl_xor(o8[k], 32); }
  if (lane < 8) {
    char* d = P.out + (size_t)row8 * 1152 + (9 * p + 8) * 64 + g8 * 8;
    *(u32x2*)d = (u32x2){pk2(o8[0] + bflo(h80[0]) + mean8, o8[1] + bfhi(h80[0]) + mean8), pk2(o8[2] + bflo(h80[1]) + mean8, o8[3] + bfhi(h80[1]) + mean8)};
    *(u32x2*)(d + 32) = (u32x2){pk2(o8[4] + bflo(h81[0]) + mean8, o8[5] + bfhi(h81[0]) + mean8), pk2(o8[6] + bflo(h81[1]) + mean8, o8[7] + bfhi(h81[1]) + mean8)};
  }
}

__global__ __launch_bounds__(512, 2) void k3_ffn(Ptrs P) {
  extern __shared__ __attribute__((aligned(16))) char smem[];
  const int lane = threadIdx.x & 63, wid = __builtin_amdgcn_readfirstlane(threadIdx.x >> 6);
  const int b = blockIdx.x;
  const int c = ((b & 7) >> 1) * 4 + ((b >> 3) & 3), mb = (b & 1) * 8 + (b >> 5);
  if (wid < 4) k3_body<0>(P, (lds_char*)smem, lane, wid, b, c, mb);
  else k3_body<1>(P, (lds_char*)smem, lane, wid - 4, b, c, mb);
}

constexpr int K0_IMG = 4608;
constexpr int K0_RED = 3 * K0_IMG;
constexpr int K0_LDS = K0_RED + 9 * 8 * 8;

DI float dpp_sum8(float v) {
  v += __shfl_xor(v, 1); v += __shfl_xor(v, 2); v += __shfl_xor(v, 4);
  return v;
}

__global__ __launch_bounds__(768) void k0_prep(Ptrs P) {
  extern __shared__ __attribute__((aligned(16))) char smem[];
  lds_char* lds = (lds_char*)smem;
  const int lane = threadIdx.x & 63, wid = __builtin_amdgcn_readfirstlane(threadIdx.x >> 6);
  const int b = blockIdx.x;
  ST_DECL; ST();
  if (wid < 9) {
    const int row = b * 8 + (lane >> 3), i = lane & 7;
    const u32x4 v = *(const u32x4*)(P.x + (size_t)row * 1152 + wid * 128 + i * 16);
    float e[8];
#pragma unroll
    for (int k = 0; k < 4; ++k) { e[2 * k] = bflo(v[k]); e[2 * k + 1] = bfhi(v[k]); }
    const float sum = dpp_sum8(((e[0] + e[1]) + (e[2] + e[3])) + ((e[4] + e[5]) + (e[6] + e[7])));
    const float lm = sum * (1.f / 64.f);
    float m2 = 0.f;
#pragma unroll
    for (int k = 0; k < 8; ++k) { const float d = e[k] - lm; m2 = __builtin_fmaf(d, d, m2); }
    m2 = dpp_sum8(m2);
    if (i == 0) *(lds_f32x2*)(lds + K0_RED + (wid * 8 + (lane >> 3)) * 8) = (f32x2){lm, m2};
    WAITL0();
    BAR();
    ST();
    f32x2 pr[9];
#pragma unroll
    for (int w = 0; w < 9; ++w) pr[w] = *(lds_f32x2*)(lds + K0_RED + (w * 8 + (lane >> 3)) * 8);
    float mean = 0.f;
#pragma unroll
    for (int w = 0; w < 9; ++w) mean += pr[w][0];
    mean *= (1.f / 9.f);
    float tot = 0.f;
#pragma unroll
    for (int w = 0; w < 9; ++w) { const float d = pr[w][0] - mean; tot += __builtin_fmaf(64.f * d, d, pr[w][1]); }
    const float rstd = rsqrtf(tot * (1.f / D_) + LN_EPS);
    u32x4 o;
#pragma unroll
    for (int k = 0; k < 4; ++k) o[k] = pk2((e[2 * k] - mean) * rstd, (e[2 * k + 1] - mean) * rstd);
    WS_ST16(P, (int)OFF_XN + ((row >> 5) * 36 * 64 + (row & 31) + 32 * (i & 1) + (4 * wid + (i >> 1)) * 64) * 16, o, ST_AUX);
    if (wid == 0 && i == 0) WS_ST4(P, OFF_MEANX + row * 4, __float_as_uint(mean), ST_AUX);
    return;
  }
  const int id = (wid - 9) * 256 + b;
  lds_char* img = lds + (wid - 9) * K0_IMG;
  const unsigned img_a = (unsigned)(uintptr_t)img;
  const int g = lane >> 4, q = (lane >> 2) & 3, p = lane & 3;
  unsigned pf_sink = 0;
  if (wid == 10 && b < 96) {
    unsigned long long pc, k1a;
    asm volatile("s_getpc_b64 %0" : "=s"(pc));
    asm volatile("s_getpc_b64 s[40:41]\n\ts_add_u32 s40, s40, _Z6k3_ffn4Ptrs@rel32@lo+4\n\ts_addc_u32 s41, s41, _Z6k3_ffn4Ptrs@rel32@hi+12\n\t"
                 "s_mov_b64 %0, s[40:41]" : "=s"(k1a) :: "s40", "s41", "scc");
    const char* pp = (const char*)k1a + ((b >> 3) * 64 + lane) * 128;
    pp = pp < (const char*)pc ? pp : (const char*)k1a;
    asm volatile("global_load_dword %0, %1, off" : "=&v"(pf_sink) : "v"(pp) : "memory");
  }
  if (id < 576) {
    const int s = id >> 4, h = (id >> 3) & 1, hp0 = (id & 7) * 2;
    const char* src = P.wqkv + (size_t)(16 * s + 8 * h) * 3456 + hp0 * 72;
#pragma unroll
    for (int it = 0; it < 4; ++it) {
      int e = it * 64 + lane; e = e < 216 ? e : 215;
      const int sgm = e / 9, ch = e - sgm * 9;
      dma16x1(src, sgm * 1152 + ch * 16, img_a + it * 1024);
    }
    BAR();
    ST();
    WAITV(0);
    asm volatile("" :: "v"(pf_sink));
    ST();
#pragma unroll
    for (int it = 0; it < 4; ++it) {
      int Q = 16 * it + 4 * g + p; Q = Q < 54 ? Q : 53;
      const int hl = Q >= 27 ? 1 : 0, sg = Q - 27 * hl;
      const int cb = sg < 4 ? 8 * sg : (sg < 8 ? 8 * sg + 8 : (sg == 8 ? 32 : (sg < 18 ? 8 * sg + 72 : 8 * sg + 144)));
      lds_char* a = img + q * 432 + cb + 72 * hl;
      u32x4 v = tr_frag(a, a + 4 * 432);
      const int o = it * 64 + lane, ol = o >= 108 ? 1 : 0, slot = o - 108 * ol;
      const int off = slot < 32 ? WQT_Q + s * 1024 + (h * 32 + slot) * 16 : s * 2432 + (h * 76 + slot - 32) * 16;
      if (o < 216) WS_ST16(P, OFF_WQT + (hp0 + ol) * (int)WQT_HP + off, v, ST_AUX);
    }
  } else if (id < 720) {
    const int j = id - 576, ks = j >> 2, cb = j & 3;
#pragma unroll
    for (int it = 0; it < 5; ++it) {
      int e = it * 64 + lane; e = e < 288 ? e : 287;
      const int kk = e / 18, ch = e - kk * 18, h = kk >> 3, jj = kk & 7;
      const int row = ks < 32 ? 18 * ks + (jj & 3) + 8 * (jj >> 2) + 4 * h : 18 * (8 * (ks - 32) + jj) + 16 + h;
      if (it < 4 || lane < 32) dma16x1(P.wo + cb * 288, row * 1152 + ch * 16, img_a + it * 1024);
    }
    BAR();
    WAITV(0);
#pragma unroll
    for (int it = 0; it < 5; ++it) {
      int G16 = 4 * it + g; G16 = G16 < 18 ? G16 : 17;
      const int h = G16 >= 9 ? 1 : 0, sg = G16 - 9 * h;
      lds_char* a = img + (8 * h + q) * 288 + (16 * sg + 4 * p) * 2;
      u32x4 v = tr_frag(a, a + 4 * 288);
      if (it < 4 || lane < 32) WS_ST16(P, OFF_WOT + cb * (int)WOT_CB + ks * 4672 + (h * 146 + 16 * sg + (lane & 15)) * 16, v, ST_AUX);
    }
  } else if (id < 756) {
    const int row = (id - 720) * 16 + (lane >> 2), i = lane & 3;
    const char* src = P.wo + (size_t)row * 1152 + i * 16;
    u32x4 v[18];
#pragma unroll
    for (int j = 0; j < 18; ++j) v[j] = *(const u32x4*)(src + j * 64);
    BAR();
    float s4[4] = {0.f, 0.f, 0.f, 0.f};
    const bf16x2 one2 = __builtin_bit_cast(bf16x2, 0x3f803f80u);
#pragma unroll
    for (int j = 0; j < 18; ++j)
#pragma unroll
      for (int k = 0; k < 4; ++k) { const unsigned t = v[j][k]; s4[k] = __builtin_amdgcn_fdot2_f32_bf16(__builtin_bit_cast(bf16x2, t), one2, s4[k], false); }
    float sum = (s4[0] + s4[1]) + (s4[2] + s4[3]);
    sum += __shfl_xor(sum, 1); sum += __shfl_xor(sum, 2);
    const float mean = sum * (1.f / D_);
    const int hd = row / 18, e = row % 18;
    const int ks = e < 16 ? hd : 32 + (hd >> 3), jj = e < 16 ? (e & 3) + 4 * (e >> 3) : (hd & 7), hh = e < 16 ? (e >> 2) & 1 : e - 16;
    const unsigned hi = pk2(mean, 0.f) & 0xffffu;
    const unsigned lo = pk2(mean - bflo(hi), 0.f) & 0xffffu;
    unsigned short* d = (unsigned short*)(P.ws + OFF_WOT + (size_t)i * WOT_CB + ks * 4672 + (hh * 146 + 144) * 16 + jj * 2);
    d[0] = (unsigned short)hi; d[8] = (unsigned short)lo;
  } else {
    const u32x4 z = {0u, 0u, 0u, 0u};
    for (int k = id - 756; k < 50; k += 12) WS_ST16(P, OFF_KV + k * 1024 + lane * 16, z, ST_AUX);
    for (int k = id - 756; k < 71; k += 12) WS_ST16(P, OFF_FLAG + k * 1024 + lane * 16, z, ST_AUX);
    BAR();
  }
}

extern "C" void kernel_launch(void* const* d_in, const int* in_sizes, int n_in, void* d_out, int out_size, void* d_ws,
                              size_t ws_size, hipStream_t stream) {
  Ptrs P;
  P.x = (const char*)d_in[0]; P.wqkv = (const char*)d_in[1]; P.wo = (const char*)d_in[2];
  P.wg = (const char*)d_in[4]; P.wu = (const char*)d_in[5]; P.wd = (const char*)d_in[6];
  P.ws = (char*)d_ws; P.out = (char*)d_out;
  (void)hipFuncSetAttribute((const void*)k3_ffn, hipFuncAttributeMaxDynamicSharedMemorySize, K3_LDS);
  k0_prep<<<256, 768, K0_LDS, stream>>>(P);
  k3_ffn<<<256, 512, K3_LDS, stream>>>(P);
}
```

```cpp
#include <hip/hip_runtime.h>
#include <hip/hip_bf16.h>
#include <stdint.h>
#include <type_traits>

typedef __attribute__((ext_vector_type(8))) short bf16x8;
typedef __attribute__((ext_vector_type(4))) short s16x4;
typedef __attribute__((ext_vector_type(16))) float f32x16;
typedef __attribute__((ext_vector_type(4))) float f32x4;
typedef __attribute__((ext_vector_type(2))) float f32x2;
typedef __attribute__((ext_vector_type(2))) __bf16 bf16x2;
typedef __attribute__((ext_vector_type(4))) unsigned u32x4;
typedef __attribute__((ext_vector_type(2))) unsigned u32x2;
typedef __attribute__((address_space(3))) char lds_char;
typedef __attribute__((address_space(3))) s16x4 lds_s16x4;
typedef __attribute__((address_space(3))) u32x4 lds_u32x4;
typedef __attribute__((address_space(3))) u32x2 lds_u32x2;
typedef __attribute__((address_space(3))) f32x4 lds_f32x4;
typedef __attribute__((address_space(3))) float lds_float;
typedef __attribute__((address_space(3))) f32x2 lds_f32x2;
typedef __attribute__((address_space(3))) unsigned lds_uint;

#define DI __device__ __forceinline__
#define MFMA32(a, b, c) __builtin_amdgcn_mfma_f32_32x32x16_bf16((a), (b), (c), 0, 0, 0)
#define MFMA16(a, b, c) __builtin_amdgcn_mfma_f32_16x16x32_bf16((a), (b), (c), 0, 0, 0)
#define WAITV(n) asm volatile("s_waitcnt vmcnt(" #n ")" ::: "memory")
#define WAITL0() asm volatile("s_waitcnt lgkmcnt(0)" ::: "memory")
#define BAR() asm volatile("s_barrier" ::: "memory")
#define SB0() __builtin_amdgcn_sched_barrier(0)

constexpr int S_ = 2048, D_ = 576, F_ = 4608;
constexpr float LN_EPS = 1e-5f;

constexpr size_t FLB = (size_t)S_ * D_ * 2;
constexpr size_t WQT_HP = 36 * 3456;
constexpr int WQT_Q = 36 * 2432;
constexpr size_t WOT_CB = 36 * 4672;
constexpr size_t WF_CH = 648 * 1024;
constexpr size_t OFF_XN = 0;
constexpr size_t OFF_PQ = OFF_XN + FLB;
constexpr size_t OFF_HC = OFF_PQ + FLB;
constexpr size_t OFF_MEANX = OFF_HC + FLB;
constexpr size_t OFF_MEANH = OFF_MEANX + 8192;
constexpr size_t OFF_SSQ = OFF_MEANH + 8192;
constexpr size_t OFF_WBAR = OFF_SSQ + 20 * 8192;
constexpr size_t OFF_KV = OFF_WBAR + 4096;
constexpr size_t OFF_WQT = OFF_KV + 53248;
constexpr size_t OFF_WOT = OFF_WQT + 16 * WQT_HP + 16384;
constexpr size_t OFF_WF = OFF_WOT + 4 * WOT_CB + 4096;
constexpr size_t OFF_PART = OFF_WF + 16 * WF_CH + 65536;
constexpr size_t OFF_XH = OFF_PART;
constexpr size_t OFF_FLAG = OFF_XH + 256 * 73728;
constexpr size_t OFF_CNT = OFF_FLAG + 256 * 128;
constexpr size_t OFF_G1 = OFF_CNT + 32 * 128;
constexpr size_t OFF_G3 = OFF_G1 + 256 * 128;
constexpr size_t OFF_GW = OFF_G3 + 16 * 128;
constexpr size_t WS_END = OFF_GW + 8 * 128;

#define ST_DECL
#define ST()
#define ST_OUT(k, slot)
DI unsigned pk2(float a, float b) {
  f32x2 v = {a, b};
  bf16x2 r = __builtin_convertvector(v, bf16x2);
  return __builtin_bit_cast(unsigned, r);
}
DI float bflo(unsigned u) { return __uint_as_float(u << 16); }
DI float bfhi(unsigned u) { return __uint_as_float(u & 0xffff0000u); }
DI float phi(float t) {
  const float e = __builtin_amdgcn_fmed3f(__builtin_amdgcn_exp2f(t * 1.4426950408889634f), 0.f, 1.f);
  return (__builtin_amdgcn_fmed3f(t, 0.f, __builtin_inff()) + 1e-6f) + e;
}
DI bf16x8 as_frag(u32x4 v) { return __builtin_bit_cast(bf16x8, v); }

DI void dma16(const void* gsrc, unsigned lds_dst) {
  unsigned keep;
  asm volatile("s_mov_b32 %0, m0\n\ts_mov_b32 m0, %2\n\ts_nop 0\n\tglobal_load_lds_dwordx4 %1, off\n\ts_mov_b32 m0, %0"
               : "=&s"(keep) : "v"(gsrc), "s"(lds_dst) : "memory");
}
DI void dma16x3(const void* sbase, unsigned voff, unsigned lds_dst) {
  unsigned keep;
  asm volatile("s_mov_b32 %0, m0\n\ts_mov_b32 m0, %3\n\ts_nop 0\n\t"
               "global_load_lds_dwordx4 %1, %2\n\t"
               "global_load_lds_dwordx4 %1, %2 offset:1024\n\t"
               "global_load_lds_dwordx4 %1, %2 offset:2048\n\t"
               "s_mov_b32 m0, %0"
               : "=&s"(keep) : "v"(voff), "s"(sbase), "s"(lds_dst) : "memory");
}
DI void dma16x3s(const void* sbase, unsigned voff, unsigned lds_dst) {
  unsigned keep;
  asm volatile("s_mov_b32 %0, m0\n\ts_mov_b32 m0, %3\n\ts_nop 0\n\t"
               "global_load_lds_dwordx4 %1, %2 sc1\n\t"
               "global_load_lds_dwordx4 %1, %2 offset:1024 sc1\n\t"
               "global_load_lds_dwordx4 %1, %2 offset:2048 sc1\n\t"
               "s_mov_b32 m0, %0"
               : "=&s"(keep) : "v"(voff), "s"(sbase), "s"(lds_dst) : "memory");
}
DI void dma16x1(const void* sbase, unsigned voff, unsigned lds_dst) {
  unsigned keep;
  asm volatile("s_mov_b32 %0, m0\n\ts_mov_b32 m0, %3\n\ts_nop 0\n\t"
               "global_load_lds_dwordx4 %1, %2\n\t"
               "s_mov_b32 m0, %0"
               : "=&s"(keep) : "v"(voff), "s"(sbase), "s"(lds_dst) : "memory");
}
DI u32x4 tr_frag(lds_char* p0, lds_char* p1) {
  s16x4 lo = __builtin_amdgcn_ds_read_tr16_b64_v4i16((lds_s16x4*)p0);
  s16x4 hi = __builtin_amdgcn_ds_read_tr16_b64_v4i16((lds_s16x4*)p1);
  u32x2 a = __builtin_bit_cast(u32x2, lo), b = __builtin_bit_cast(u32x2, hi);
  u32x4 r = {a[0], a[1], b[0], b[1]};
  return r;
}

#define ST_AUX 16
#define WF_AUX 16
#define WS_RSRC(P) __builtin_amdgcn_make_buffer_rsrc((P).ws, 0, (int)WS_END, 0x00020000)
#define WS_ST16(P, off, v, aux) __builtin_amdgcn_raw_buffer_store_b128((v), WS_RSRC(P), (int)(off), 0, (aux))
#define WS_ST8(P, off, v, aux) __builtin_amdgcn_raw_buffer_store_b64((v), WS_RSRC(P), (int)(off), 0, (aux))
#define WS_ST4(P, off, v, aux) __builtin_amdgcn_raw_buffer_store_b32((v), WS_RSRC(P), (int)(off), 0, (aux))
template <class R> DI void st128(u32x4 v, R rsrc, int voff, int soff) {
  __builtin_amdgcn_raw_buffer_store_b128(v, rsrc, voff, soff, 16);
  asm volatile("s_nop 1" :: "v"(v));
}
struct Ptrs {
  const char* x; const char* wqkv; const char* wo; const char* wg; const char* wu; const char* wd;
  char* ws; char* out;
};

constexpr int ITEM_LDS = 9216;
constexpr int SUB_PER_XCD = 486;

DI void sub_load(const Ptrs& P, int x, int j, int lane, unsigned lds_base) {
  const int rl = lane >> 3, cp = lane & 7;
  const int cb = j / 36, r = j % 36, S = r >> 1, mat = r & 1;
  const char* base = (mat ? P.wu : P.wg) + (size_t)(32 * S) * (F_ * 2) + (576 * x + 64 * cb) * 2;
#pragma unroll
  for (int it = 0; it < 4; ++it) {
    const int X = ((rl >> 1) & 1) | ((it & 1) << 1);
    dma16x1(base + (size_t)(8 * it) * (F_ * 2), rl * (F_ * 2) + (((cp >> 1) ^ X) * 32) + (cp & 1) * 16, lds_base + it * 1024);
  }
}
DI void sub_store(const Ptrs& P, int x, int j, int lane, lds_char* img) {
  const int g = lane >> 4, q = (lane >> 2) & 3, p = lane & 3;
  const int X = ((q >> 1) & 1) | ((g & 1) << 1);
  const int cb = j / 36, r = j % 36, S = r >> 1, mat = r & 1;
#pragma unroll
  for (int u4 = 0; u4 < 4; ++u4) {
    lds_char* a = img + (8 * g + q) * 128 + ((u4 ^ X) * 32) + 8 * p;
    u32x4 v = tr_frag(a, a + 512);
    const int ug = 4 * cb + u4, cc = ug >= 18 ? 1 : 0, u = ug - 18 * cc, U = u >> 1;
    const int n = U < 8 ? (U >> 1) * 72 + S * 4 + mat * 2 + (U & 1) : 288 + 2 * S + mat;
    st128(v, WS_RSRC(P), lane * 16, (int)OFF_WF + (2 * x + cc) * (int)WF_CH + (2 * n + (u & 1)) * 1024);
  }
}

#define CODE_PF 1
#define FRONT_PF 1
#define N0_SUB 0
#define N1_SUB 256
constexpr int K0_ITEMS = 704 + 8 * (N0_SUB / 2);

constexpr int K1_W = 0;
constexpr int K1_ITEM = 124 * 1024;
constexpr int KVP = 192;
constexpr int K2_O = 49152;
constexpr int K2_RED = K2_O + 36864;
constexpr int K2_MEAN = K2_RED + 32768;
static_assert(K2_MEAN + 2048 <= K1_ITEM, "lds");

DI void poll_counters(const char* base, const int off, const unsigned need) {
#pragma unroll 1
  for (int spin = 0; spin < (1 << 22); ++spin) {
    unsigned v;
    asm volatile("global_load_dword %0, %1, %2 sc1\n\ts_waitcnt vmcnt(0)" : "=&v"(v) : "v"(off), "s"(base) : "memory");
    if (__builtin_amdgcn_ballot_w64(v < need) == 0ull) break;
    __builtin_amdgcn_s_sleep(3);
  }
}

constexpr int K3_RING = 0;
constexpr int K3_HH = 73728;
constexpr int K3_SSQ = K3_HH + 73728;
constexpr int K3_LDS = K3_SSQ + 4 * 3072;
#define DSTEP 2
#define XOFF 1
#define DMA0 1
#define SLOW0 7
constexpr int NS = 486, PH = 288, P2 = 324;

struct Sch {
  signed char own[162] = {}, u[162] = {}, db[162] = {}, t01[162] = {};
  short rs[NS + 4] = {}, lo[NS + 4] = {};
  signed char sown[90] = {}, su[90] = {}, sdb[90] = {}, pf[NS] = {};
  bool bar[NS] = {};
  signed char dslot[NS] = {}, dk[NS] = {}, prio[NS] = {};
  signed char e0[NS] = {}, ne0[NS] = {}, ne1[NS] = {}, eg[NS] = {};
  signed char og[NS] = {}, o0[NS] = {};
  signed char xrd[NS] = {}, cbar[NS] = {}, xst[NS] = {}, ost[NS] = {}, xop[NS] = {};
  signed char flag[NS] = {}, fbar[NS] = {}, poll[NS] = {}, chk[NS] = {}, ld[NS] = {}, wr[NS] = {}, rd[NS] = {};
  signed char wl[NS] = {}, wf[NS] = {};
  int ebar = 0;
  bool ok = true;
};
constexpr Sch make_sch() {
  Sch s;
  int n2 = 0, nb = 0;
  auto put = [&](int own, int u, int db) {
    s.sown[nb] = own; s.su[nb] = u; s.sdb[nb] = db;
    for (int t = 0; t < (db == 4 ? 1 : 2); ++t) {
      s.own[n2] = own; s.u[n2] = u; s.db[n2] = db; s.t01[n2] = t;
      s.rs[P2 + n2] = 27 + nb / 6; s.lo[P2 + n2] = ((27 + nb / 6) % 3) * 24 + (nb % 6) * 4; ++n2;
    }
    ++nb;
  };
  for (int u = 0; u < 9; ++u) for (int d = 0; d < 5; ++d) put(1, u, d);
  for (int u = 0; u < 6; ++u) { put(0, u, 4); put(0, u, 0); put(0, u, 1); }
  for (int u = 6; u < 9; ++u) { put(0, u, 4); put(0, u, 0); }
  for (int u = 6; u < 9; ++u) put(0, u, 1);
  for (int d = 2; d < 4; ++d) for (int u = 0; u < 9; ++u) put(0, u, d);
  for (int n = 0; n < P2; ++n) { s.rs[n] = n / 12; s.lo[n] = ((n / 12) % 3) * 24 + (n % 12) * 2; }
  for (int n = NS; n < NS + 4; ++n) { s.rs[n] = s.rs[NS - 1]; s.lo[n] = s.lo[NS - 1]; }
  for (int n = 0; n < NS; ++n) {
    s.bar[n] = s.rs[n + 3] != s.rs[n + 2];
    s.dslot[n] = -1; s.prio[n] = -1; s.eg[n] = -1; s.og[n] = -1; s.flag[n] = -1; s.fbar[n] = -1; s.poll[n] = 0; s.chk[n] = -1;
    s.ld[n] = -1; s.rd[n] = -1; s.wl[n] = -1; s.wf[n] = -1; s.xst[n] = -1; s.ost[n] = -1; s.xrd[n] = -1; s.cbar[n] = -1; s.pf[n] = 0;
  }
  for (int n = 0; n < NS; ++n) if (s.bar[n]) {
    int e = n + 1; while (e < NS && !s.bar[e]) ++e;
    const int L = e - n, tgt = s.rs[n + 3] + 2;
    if (tgt <= 41) for (int k = 0; k < 6; ++k) { s.dslot[n + (k * L) / 6 + 1] = tgt; s.dk[n + (k * L) / 6 + 1] = k; if ((k * L) / 6 + 1 >= L) s.ok = false; }
    if (e < NS) { s.prio[n + L / 2] = 2; s.prio[e - 3] = 1; s.prio[e - 1] = 0; }
  }
  auto nbar = [&](int n) { while (!s.bar[n]) ++n; return n; };
  for (int g = 0; g < 5; ++g) {
    const int start = (g < 4 ? 72 * (g + 1) : P2) + 2, tot = g == 4 ? 46 : 92;
    int e = 0;
    for (int n = start; e < tot; ++n) {
      const int c0 = g == 4 ? 2 : 1, c1 = g >= 3 ? 2 : ((n - start) & 1);
      if (s.eg[n] >= 0) s.ok = false;
      s.eg[n] = g; s.e0[n] = e; s.ne0[n] = c0; s.ne1[n] = c1;
      for (int i = e; i < e + c0 + c1 && i < tot; ++i) if (i % 46 == 45) { int m = n + 1; while (!s.bar[m]) ++m; m += XOFF; while (s.xrd[m] >= 0) m += 3; s.xrd[m] = 2 * g + i / 46; s.xst[m + 2] = 2 * g + i / 46; }
      e += c0 + c1;
      if (g < 3 && n >= start + 68) s.ok = false;
    }
  }
  s.og[P2 + 119] = 4; s.o0[P2 + 119] = 0; s.og[P2 + 120] = 4; s.o0[P2 + 120] = 4; s.ost[P2 + 124] = 4;
  const int opk[3] = {121, 127, 145}, ostl[3] = {125, 136, 149};
  for (int k = 0; k < 3; ++k) {
    for (int i = 0; i < 4; ++i) { const int n = P2 + opk[k] + i; s.og[n] = k; s.o0[n] = 2 * i; }
    s.ost[P2 + ostl[k]] = k;
  }
  int lds_[9] = {16, 18, 24, 34, 43, 44, 76, 78, 80}, rds[9] = {};
  for (int u = 0; u < 9; ++u) {
    const int cbn = nbar(P2 + lds_[(u / 3) * 3 + 2] + 17);
    rds[u] = cbn - P2 + 1 + (u < 6 ? 3 : 2) * (u % 3);
    s.ld[P2 + lds_[u]] = u; s.rd[P2 + rds[u]] = u; s.cbar[cbn] = u;
  }
  s.poll[P2 + 3] = 1; s.chk[P2 + 15] = 6;
  s.poll[P2 + 63] = 1; s.chk[P2 + 75] = 9;
  s.xop[28] = 3; s.xop[40] = 3;
  s.pf[100] = 1;
  const int fb[2] = {PH + 21, nbar(P2 + 40)}, fv[2] = {6, 9};
  for (int k = 0; k < 2; ++k) { if (!s.bar[fb[k]]) s.ok = false; s.fbar[fb[k]] = fv[k]; s.flag[fb[k] + 1] = fv[k]; }
  s.ebar = nbar(P2 + 140);
  int cl[NS + 1] = {}, cf[NS + 1] = {};
  for (int n = 9; n < NS; ++n) {
    const int both = (s.ost[n] >= 0 ? 2 : 0) + s.xop[n];
    cl[n + 1] = cl[n] + both + (s.dslot[n] >= 0 ? 1 : 0);
    cf[n + 1] = cf[n] + both + (s.xst[n] >= 0 ? 2 : 0) + (s.flag[n] >= 0 ? 1 : 0) + s.poll[n] + (s.ld[n] >= 0 ? 2 : 0) + 2 * s.pf[n];
  }
  auto need = [&](int n, int ul_, int uf_) {
    if (ul_ >= 0) { const int w = cl[n] - ul_; if (s.wl[n] < 0 || w < s.wl[n]) s.wl[n] = w; }
    if (uf_ >= 0) { const int w = cf[n] - uf_; if (s.wf[n] < 0 || w < s.wf[n]) s.wf[n] = w; }
  };
  for (int n = 21; n < NS; ++n) {
    if (s.bar[n]) {
      int last = -1;
      for (int i = 9; i < n; ++i) if (s.dslot[i] == s.rs[n + 3]) last = i;
      need(n, last >= 0 ? cl[last] + 1 : 0, n == 21 ? 0 : -1);
      if (n == s.ebar) need(n, cl[P2 + 137], cf[P2 + 137]);
      if (n == 45) need(n, cl[29], cf[29]);
      if (n == 57) need(n, cl[41], cf[41]);
      if (s.fbar[n] >= 0) {
        int at = -1;
        for (int i = 9; i < n; ++i) if (s.xst[i] == s.fbar[n] - 1) at = i;
        if (at < 0) s.ok = false;
        for (int i = 9; i < n; ++i) if (s.xst[i] >= 0 && s.xst[i] < s.fbar[n] && i > at) at = i;
        need(n, -1, cf[at + 1]);
      }
    }
    if (s.chk[n] >= 0) { int i = n; while (!s.poll[i]) --i; need(n, -1, cf[i + 1]); }
    if (s.cbar[n] >= 0) { int i = n; while (s.ld[i] != s.cbar[n]) --i; need(n, -1, cf[i + 1]); }
  }
  for (int u = 0; u < 9; ++u) {
    int own_rd = u == 0 ? P2 - 1 : P2 + 9 * (u - 1) + 4;
    for (int n = 0; n < NS; ++n) if (s.xrd[n] == u && n > own_rd) own_rd = n;
    bool b1 = false;
    for (int n = own_rd + 1; n <= P2 + lds_[u]; ++n) b1 = b1 || s.bar[n];
    int use = 81; while (s.u[use] != u) ++use;
    if (!b1 || rds[u] + 2 > use) s.ok = false;
  }
  return s;
}
constexpr Sch SCH = make_sch();
static_assert(SCH.ok, "schedule");

template <int I, int N, class F> DI void static_for(F&& f) {
  if constexpr (N - I == 1) f(std::integral_constant<int, I>{});
  else if constexpr (N - I > 1) { static_for<I, (I + N) / 2>(f); static_for<(I + N) / 2, N>(f); }
}
template <int N> DI void waitv() { asm volatile("s_waitcnt vmcnt(%0)" :: "n"(N) : "memory"); }
#define MFMA_DRAIN() do { asm volatile("s_nop 7\n\ts_nop 3" ::: "memory"); SB0(); } while (0)
typedef __attribute__((ext_vector_type(4))) int srd_t;
DI srd_t make_srd(const void* p, unsigned bytes) {
  const unsigned long long a = (unsigned long long)p;
  srd_t r = {(int)(unsigned)a, (int)((unsigned)(a >> 32) & 0xffffu), (int)bytes, 0x00020000};
  return r;
}
DI void bdma(srd_t srd, unsigned voff, unsigned soff, unsigned lds_dst) {
  asm volatile("s_mov_b32 m0, %3\n\ts_nop 0\n\tbuffer_load_dwordx4 %0, %1, %2 offen lds" :: "v"(voff), "s"(srd), "s"(soff), "s"(lds_dst) : "memory");
}
template <int LOFF, int SOFF> DI void bdma_i(srd_t srd, unsigned voff, unsigned sbase, unsigned lbase) {
  unsigned so;
  asm volatile("s_add_u32 m0, %4, %6\n\ts_add_u32 %0, %3, %5\n\tbuffer_load_dwordx4 %1, %2, %0 offen sc1 lds"
               : "=&s"(so) : "v"(voff), "s"(srd), "s"(sbase), "s"(lbase), "n"(SOFF), "n"(LOFF) : "memory", "scc");
}

template <int OFF> DI void bdma_sc1(srd_t srd, unsigned voff, unsigned lbase) {
  unsigned so;
  asm volatile("s_add_u32 m0, %3, %4\n\ts_mov_b32 %0, %4\n\tbuffer_load_dwordx4 %1, %2, %0 offen sc1 lds"
               : "=&s"(so) : "v"(voff), "s"(srd), "s"(lbase), "n"(OFF) : "memory", "scc");
}

template <int FJ>
DI void k3_body(const Ptrs& P, lds_char* lds, const int lane, const int mi, const int b, const int c, const int mb) {
  constexpr int fj = FJ;
  const unsigned lds_a = (unsigned)(uintptr_t)lds;
  const int mt = mb * 4 + mi, p = c & 1;
  const int g = lane >> 4;
  ST_DECL; ST();
  const unsigned voff = lane * 16;
  const srd_t sd = make_srd(P.wd, D_ * F_ * 2);
  const srd_t sw = make_srd(P.ws + OFF_WF + (size_t)c * WF_CH, (unsigned)WF_CH);
  const auto wsr = WS_RSRC(P);
  const int wid = fj * 4 + mi;
  {
    const int r = lane & 31, h = lane >> 5;
    const int hp = c;
    const int mt2 = mb * 4 + (c >> 2), cb = c & 3;
    u32x4 af[18], a4[5], xv0, xv1;
    float mean;
    const int k4 = FJ == 0 ? 5 * mi : 20 + 4 * mi;
    constexpr int n4 = FJ == 0 ? 5 : 4;
    const int row = mt2 * 32 + r, s0 = cb * 9 + 2 * (FJ == 0 ? mi : 4);
    auto prefetch2 = [&]() __attribute__((always_inline)) {
      const char* wo = P.ws + OFF_WOT + (size_t)cb * WOT_CB + h * 2336;
#pragma unroll
      for (int i = 0; i < 18; ++i) af[i] = *(const u32x4*)(wo + (18 * FJ + i) * 4672 + (32 * mi + r) * 16);
#pragma unroll
      for (int i = 0; i < n4; ++i) a4[i] = *(const u32x4*)(wo + (k4 + i) * 4672 + (128 + (r < 17 ? r : 17)) * 16);
      xv0 = *(const u32x4*)(P.x + (size_t)row * 1152 + s0 * 32 + h * 16);
      xv1 = *(const u32x4*)(P.x + (size_t)row * 1152 + (FJ == 0 ? s0 + 1 : s0) * 32 + h * 16);
      mean = ((const float*)(P.ws + OFF_MEANX))[row];
      SB0();
    };
    const int xj = c >> 1, j0 = (mb * 2 + p) * 10 + (mi & 1) * 5;
    const bool xtra = mi == 2 && mb < 2;
    const int stg5 = 32768 + (mi & 1) * 8192;
    if constexpr (FJ == 1) {
      const char* wsrc = P.ws + OFF_WQT + (size_t)hp * WQT_HP + mi * 1024;
      const unsigned wdst = lds_a + K1_W + mi * 1024;
#pragma unroll
      for (int i = 0; i < 14; ++i) dma16x1(wsrc + i * 4096, voff, wdst + i * 4096);
      WAITV(8); BAR();
#pragma unroll
      for (int i = 14; i < 20; ++i) dma16x1(wsrc + i * 4096, voff, wdst + i * 4096);
      WAITV(9); BAR();
#pragma unroll
      for (int i = 20; i < 26; ++i) dma16x1(wsrc + i * 4096, voff, wdst + i * 4096);
      WAITV(9); BAR();
#pragma unroll
      for (int i = 26; i < 31; ++i) dma16x1(wsrc + i * 4096, voff, wdst + i * 4096);
      WAITV(9); BAR();
      WAITV(0);
      if (mi >= 2) {
#pragma unroll
        for (int i = 0; i < 3; ++i) sub_load(P, xj, j0 + i, lane, lds_a + K1_ITEM + (mi & 1) * 16384 + i * 4096);
      } else {
        prefetch2();
      }
      BAR();
      ST();
      if (mi >= 2) {
        sub_load(P, xj, j0 + 3, lane, lds_a + K1_ITEM + (mi & 1) * 16384 + 3 * 4096);
        sub_load(P, xj, j0 + 4, lane, lds_a + stg5);
        sub_load(P, xj, xtra ? 320 + mb * 2 + p : j0 + 4, lane, lds_a + stg5 + 4096);
        WAITV(12);
#pragma unroll
        for (int i = 0; i < 3; ++i) sub_store(P, xj, j0 + i, lane, lds + K1_ITEM + (mi & 1) * 16384 + i * 4096);
      }
      if (mi < 2) {
        __builtin_amdgcn_s_setprio(3);
        const int q = (lane >> 2) & 3, op = 4 * (g & 1) + (lane & 3);
        const int vq = op >= 5 ? 19 : (mi ? 14 : 10) + op, kq = (mi ? 5 : 1) + op;
        lds_char* va = lds + (8 * (g >> 1) + q) * KVP + vq * 8;
        lds_char* ka = lds + (8 * (g >> 1) + q) * KVP + kq * 8;
        f32x16 kv;
#pragma unroll
        for (int i = 0; i < 16; ++i) kv[i] = 0.f;
#pragma unroll
        for (int ks = 0; ks < 8; ++ks) {
          u32x4 a = tr_frag(va + ks * 16 * KVP, va + ks * 16 * KVP + 4 * KVP);
          u32x4 bb = tr_frag(ka + ks * 16 * KVP, ka + ks * 16 * KVP + 4 * KVP);
          kv = MFMA32(as_frag(a), as_frag(bb), kv);
        }
        const int d = r - 2 * mi;
        const int dd = mi ? (d >= 2 ? d - 2 : 16 + d) : d;
        float* dst = (float*)(P.ws + OFF_KV) + (2 * hp + mi) * 400 + dd;
        const bool dok = d >= 0 && d < 18;
#pragma unroll
        for (int i = 0; i < 12; ++i) {
          const int rw = (i & 3) + 8 * (i >> 2) + 4 * h;
          const int e = rw == 20 ? 18 : rw - 2 * mi;
          if (dok && (rw == 20 || (e >= 0 && e < 18 && rw < 20))) atomicAdd(dst + e * 20, kv[i]);
        }
        WAITV(0);
      }
    } else {
      u32x4 xf[36];
      const char* xsrc = P.ws + OFF_XN + (size_t)mt * 36864 + lane * 16;
#pragma unroll
      for (int s = 0; s < 18; ++s) xf[s] = *(const u32x4*)(xsrc + s * 1024);
      f32x16 acc[3], accq;
#pragma unroll
      for (int i = 0; i < 16; ++i) { acc[0][i] = 0.f; acc[1][i] = 0.f; acc[2][i] = 0.f; accq[i] = 0.f; }
      lds_char* wa = lds + K1_W + (h * 76 + r) * 16;
      lds_char* wa2 = lds + K1_W + (h * 76 + 64 + (r < 12 ? r : 11)) * 16;
      u32x4 wf[2][3];
#pragma unroll
      for (int s = 0; s < 36; ++s) {
        if (s % 9 == 0) {
          BAR();
          if (s < 18) {
#pragma unroll
            for (int k = s + 18; k < s + 27; ++k) xf[k] = *(const u32x4*)(xsrc + k * 1024);
          }
          wf[s & 1][0] = *(lds_u32x4*)(wa + s * 2432); wf[s & 1][1] = *(lds_u32x4*)(wa + s * 2432 + 512); wf[s & 1][2] = *(lds_u32x4*)(wa2 + s * 2432);
        }
        if (s % 9 != 8) {
          wf[(s + 1) & 1][0] = *(lds_u32x4*)(wa + (s + 1) * 2432); wf[(s + 1) & 1][1] = *(lds_u32x4*)(wa + (s + 1) * 2432 + 512);
          wf[(s + 1) & 1][2] = *(lds_u32x4*)(wa2 + (s + 1) * 2432);
        }
        SB0();
#pragma unroll
        for (int t = 0; t < 3; ++t) acc[t] = MFMA32(as_frag(wf[s & 1][t]), as_frag(xf[s]), acc[t]);
        SB0();
      }
      ST();
      lds_char* kvrow = lds + (mi * 32 + r) * KVP;
      u32x2 qrem;
#pragma unroll
      for (int gi = 0; gi < 10; ++gi) {
        const int t = gi >> 2, gg = gi & 3;
        u32x2 o;
        if (gi < 5) { o[0] = pk2(phi(acc[t][4 * gg]), phi(acc[t][4 * gg + 1])); o[1] = pk2(phi(acc[t][4 * gg + 2]), phi(acc[t][4 * gg + 3])); }
        else { o[0] = pk2(acc[t][4 * gg], acc[t][4 * gg + 1]); o[1] = pk2(acc[t][4 * gg + 2], acc[t][4 * gg + 3]); }
        if (gi == 9 && h == 1) { o[0] = 0x3f803f80u; o[1] = 0x3f803f80u; }
        *(lds_u32x2*)(kvrow + (2 * gi + h) * 8) = o;
        if (gi == 0) qrem = o;
      }
      WAITL0();
      BAR();
      lds_char* wq = lds + K1_W + WQT_Q + lane * 16;
      u32x4 aq[2][4];
#pragma unroll
      for (int i = 0; i < 4; ++i) aq[0][i] = *(lds_u32x4*)(wq + i * 1024);
#pragma unroll
      for (int g3 = 0; g3 < 9; ++g3) {
        if (g3 < 8) {
#pragma unroll
          for (int i = 0; i < 4; ++i) aq[(g3 + 1) & 1][i] = *(lds_u32x4*)(wq + (4 * g3 + 4 + i) * 1024);
        }
        SB0();
#pragma unroll
        for (int i = 0; i < 4; ++i) accq = MFMA32(as_frag(aq[g3 & 1][i]), as_frag(xf[4 * g3 + i]), accq);
        SB0();
      }
      const int pq = (int)OFF_PQ + mt * 36864;
#pragma unroll
      for (int e = 0; e < 2; ++e) {
        u32x4 o;
#pragma unroll
        for (int k = 0; k < 4; ++k) o[k] = pk2(phi(accq[8 * e + 2 * k]), phi(accq[8 * e + 2 * k + 1]));
        WS_ST16(P, pq + (2 * hp + e) * 1024 + lane * 16, o, 16);
      }
      if (h == 0) WS_ST8(P, pq + (32 + (hp >> 2)) * 1024 + (r + 32 * ((hp >> 1) & 1)) * 16 + 8 * (hp & 1), qrem, 16);
      WAITV(0);
    }
    BAR();
    ST();
    if (FJ == 1 && mi == 0) {
      if (lane < 16) __hip_atomic_fetch_add((unsigned*)(P.ws + OFF_G1 + lane * 2048 + (b & 15) * 128), 1u, __ATOMIC_RELAXED, __HIP_MEMORY_SCOPE_AGENT);
      poll_counters(P.ws, (int)OFF_G1 + (b >> 4) * 2048 + (lane & 15) * 128, 16u);
    }
    if constexpr (FJ == 0) { __builtin_amdgcn_s_sleep(4); prefetch2(); }
    if (FJ == 1 && mi >= 2) {
      WAITV(12);
      sub_store(P, xj, j0 + 3, lane, lds + K1_ITEM + (mi & 1) * 16384 + 3 * 4096);
      sub_store(P, xj, j0 + 4, lane, lds + stg5);
      if (xtra) sub_store(P, xj, 320 + mb * 2 + p, lane, lds + stg5 + 4096);
      WAITL0();
      prefetch2();
    }
    BAR();
    ST();
    const int e = r < 16 ? r : (r == 16 ? 16 : (r == 20 ? 17 : 18));
    const bool rok = r <= 17 || r == 20 || r == 21;
    u32x4 k0[4], k1[4], qf[4]; u32x2 k2[4];
    dma16x3s(P.ws + OFF_KV + wid * 6400, voff, lds_a + wid * 6144);
    dma16x3s(P.ws + OFF_KV + wid * 6400 + 3072, voff, lds_a + wid * 6144 + 3072);
    const u32x4 ktail = __builtin_amdgcn_raw_buffer_load_b128(wsr, (lane & 15) * 16, (int)OFF_KV + wid * 6400 + 6144, 16);
#pragma unroll
    for (int i = 0; i < 4; ++i) qf[i] = __builtin_amdgcn_raw_buffer_load_b128(wsr, voff, (int)OFF_PQ + mt2 * 36864 + (4 * wid + i) * 1024, 16);
    const u32x4 qr = __builtin_amdgcn_raw_buffer_load_b128(wsr, voff, (int)OFF_PQ + mt2 * 36864 + (32 + (wid >> 1)) * 1024, 16);
    *(lds_u32x4*)(lds + K2_RED + wid * 256 + (lane & 15) * 16) = ktail;
    asm volatile("" ::: "memory");
    SB0();
#pragma unroll
    for (int i = 0; i < 4; ++i) {
      const int o = i * 1600 + e * 80;
      lds_char* kp0 = lds + (o + 16 * h < 6144 ? wid * 6144 + o + 16 * h : K2_RED + wid * 256 + o + 16 * h - 6144);
      lds_char* kp1 = lds + (o + 16 * h + 32 < 6144 ? wid * 6144 + o + 16 * h + 32 : K2_RED + wid * 256 + o + 16 * h + 32 - 6144);
      lds_char* kp2 = lds + (o + 64 < 6144 ? wid * 6144 + o + 64 : K2_RED + wid * 256 + o + 64 - 6144);
      k0[i] = *(lds_u32x4*)kp0; k1[i] = *(lds_u32x4*)kp1; k2[i] = *(lds_u32x2*)kp2;
    }
    float orem[4];
#define F_(x) __uint_as_float(x)
#pragma unroll
    for (int i = 0; i < 4; ++i) {
      u32x4 a1 = {pk2(F_(k0[i][0]), F_(k0[i][1])), pk2(F_(k0[i][2]), F_(k0[i][3])), pk2(F_(k1[i][0]), F_(k1[i][1])), pk2(F_(k1[i][2]), F_(k1[i][3]))};
      u32x4 a2 = {0u, 0u, 0u, 0u};
      a2[i] = (h == (mi & 1)) ? pk2(F_(k2[i][0]), F_(k2[i][1])) : 0u;
      if (!rok) { a1 = (u32x4){0u, 0u, 0u, 0u}; a2[i] = 0u; }
      f32x16 cc;
#pragma unroll
      for (int j = 0; j < 16; ++j) cc[j] = 0.f;
      cc = MFMA32(as_frag(a1), as_frag(qf[i]), cc);
      cc = MFMA32(as_frag(a2), as_frag(qr), cc);
      const float inv = __builtin_amdgcn_rcpf(cc[9] + 1e-6f);
      float o[9];
#pragma unroll
      for (int j = 0; j < 9; ++j) o[j] = cc[j] * inv;
      u32x4 of = {pk2(o[0], o[1]), pk2(o[2], o[3]), pk2(o[4], o[5]), pk2(o[6], o[7])};
      *(lds_u32x4*)(lds + K2_O + (4 * wid + i) * 1024 + lane * 16) = of;
      orem[i] = o[8];
    }
#undef F_
    {
      u32x2 o2 = {pk2(orem[0], orem[1]), pk2(orem[2], orem[3])};
      *(lds_u32x2*)(lds + K2_O + (32 + (wid >> 1)) * 1024 + lane * 16 + 8 * (mi & 1)) = o2;
    }
    if (FJ == 1 && mi >= 2) WAITV(0);
    WAITL0();
    BAR();
    ST();
    if (FJ == 1 && mi == 3 && lane == 0) __hip_atomic_fetch_add((unsigned*)(P.ws + OFF_GW + xj * 128), 1u, __ATOMIC_RELAXED, __HIP_MEMORY_SCOPE_AGENT);
    f32x16 acc, acc4;
#pragma unroll
    for (int j = 0; j < 16; ++j) { acc[j] = 0.f; acc4[j] = 0.f; }
    lds_char* ob = lds + K2_O + lane * 16;
#pragma unroll
    for (int i = 0; i < n4; ++i) acc4 = MFMA32(as_frag(a4[i]), as_frag(*(lds_u32x4*)(ob + (k4 + i) * 1024)), acc4);
#pragma unroll
    for (int i = 0; i < 18; ++i) acc = MFMA32(as_frag(af[i]), as_frag(*(lds_u32x4*)(ob + (18 * FJ + i) * 1024)), acc);
    {
      *(lds_float*)(lds + K2_MEAN + (wid * 64 + lane) * 4) = acc4[8] + acc4[9];
      lds_char* p4 = lds + K2_RED + 16384 + wid * 2048 + lane * 16;
      *(lds_f32x4*)(p4) = (f32x4){acc4[0], acc4[1], acc4[2], acc4[3]};
      *(lds_f32x4*)(p4 + 1024) = (f32x4){acc4[4], acc4[5], acc4[6], acc4[7]};
      if (FJ == 1) {
        lds_char* pr = lds + K2_RED + mi * 4096 + lane * 16;
#pragma unroll
        for (int k = 0; k < 4; ++k) *(lds_f32x4*)(pr + k * 1024) = (f32x4){acc[4 * k], acc[4 * k + 1], acc[4 * k + 2], acc[4 * k + 3]};
      }
    }
    WAITL0();
    BAR();
    if (FJ == 0 || mi == 0) {
#pragma unroll
      for (int w = 0; w < 8; ++w) mean += *(lds_float*)(lds + K2_MEAN + (w * 64 + r) * 4);
      if constexpr (FJ == 0) {
        lds_char* pr = lds + K2_RED + mi * 4096 + lane * 16;
#pragma unroll
        for (int k = 0; k < 4; ++k) {
          f32x4 v = *(lds_f32x4*)(pr + k * 1024);
#pragma unroll
          for (int j = 0; j < 4; ++j) acc[4 * k + j] += v[j];
        }
      } else {
#pragma unroll
        for (int j = 0; j < 8; ++j) acc[j] = 0.f;
#pragma unroll
        for (int w = 0; w < 8; ++w) {
          lds_char* p4 = lds + K2_RED + 16384 + w * 2048 + lane * 16;
          f32x4 v0 = *(lds_f32x4*)(p4), v1 = *(lds_f32x4*)(p4 + 1024);
#pragma unroll
          for (int j = 0; j < 4; ++j) { acc[j] += v0[j]; acc[4 + j] += v1[j]; }
        }
      }
      float ssq = 0.f;
#pragma unroll
      for (int i = 0; i < (FJ == 0 ? 2 : 1); ++i) {
        float v[8];
#pragma unroll
        for (int j = 0; j < 4; ++j) {
          auto sw2 = __builtin_amdgcn_permlane32_swap(__float_as_uint(acc[8 * i + j]), __float_as_uint(acc[8 * i + 4 + j]), false, false);
          v[j] = __uint_as_float(sw2[0]); v[4 + j] = __uint_as_float(sw2[1]);
        }
        const u32x4 xv = i ? xv1 : xv0;
        u32x4 o;
#pragma unroll
        for (int k = 0; k < 4; ++k) {
          const float a0 = v[2 * k] + bflo(xv[k]) - mean, a1 = v[2 * k + 1] + bfhi(xv[k]) - mean;
          ssq += a0 * a0 + a1 * a1;
          o[k] = pk2(a0, a1);
        }
        const int d8 = 2 * (s0 + i) + h;
        WS_ST16(P, OFF_HC + (((row >> 4) * 18 + (d8 >> 2)) * 64 + (row & 15) + 16 * (d8 & 3)) * 16, o, 16);
      }
      ssq += __shfl_xor(ssq, 32);
      if (h == 0) {
        WS_ST4(P, OFF_SSQ + ((mt2 * 20 + cb * 5 + wid) * 32 + r) * 4, __float_as_uint(ssq), 16);
        if (cb == 0 && wid == 0) WS_ST4(P, OFF_MEANH + row * 4, __float_as_uint(mean), 16);
      }
    }
    WAITV(0); WAITL0();
    BAR();
    ST();
    if (FJ == 1 && mi == 1) {
      const int off = lane & 1 ? (int)(OFF_GW - OFF_G3) + xj * 128 : mb * 128;
      if (lane == 0) __hip_atomic_fetch_add((unsigned*)(P.ws + OFF_G3 + off), 1u, __ATOMIC_RELAXED, __HIP_MEMORY_SCOPE_AGENT);
      poll_counters(P.ws, (int)OFF_G3 + off, lane & 1 ? 32u : 16u);
    }
    BAR();
    ST();
  }
  const char* hsrc = P.ws + OFF_HC + (size_t)mt * 36864;
  const char* hmine = hsrc + fj * 18432;
  constexpr int ST0 = K3_RING + 2 * 24576, ST1 = K3_HH, ST2 = K3_HH + 24576;
  const unsigned ring_w = lds_a + K3_RING + mi * 6144;
  const unsigned sw0 = mi * 6144;
  auto first_slot = [&](auto tc) __attribute__((always_inline)) {
    constexpr int t = decltype(tc)::value;
    static_for<3 * FJ, 3 * FJ + 3>([&](auto kc) __attribute__((always_inline)) {
      constexpr int k = decltype(kc)::value;
      bdma_i<t * 24576 + k * 1024, t * 24576 + k * 1024>(sw, voff, sw0, ring_w);
    });
  };
  dma16x3s(hmine, voff, lds_a + ST0 + (mi * 2 + fj) * 3072);
  first_slot(std::integral_constant<int, 0>{});
  dma16x3s(hmine + 3072, voff, lds_a + ST1 + (mi * 2 + fj) * 3072);
  first_slot(std::integral_constant<int, 1>{});
  dma16x3s(hmine + 6144, voff, lds_a + ST2 + (mi * 2 + fj) * 6144);
  dma16x3s(hmine + 9216, voff, lds_a + ST2 + (mi * 2 + fj) * 6144 + 3072);
  u32x4 hf[2][18];
  WAITV(12);
  BAR();
  lds_char* ring_rd = lds + K3_RING + fj * 1024 + lane * 16;
  lds_char* hh_rd = lds + K3_HH + mi * 18432 + lane * 16;
  u32x4 a[6];
  auto read_frag = [&](auto nc) __attribute__((always_inline)) {
    constexpr int n = decltype(nc)::value;
    if constexpr (n < P2) a[n % 6] = *(lds_u32x4*)(ring_rd + SCH.lo[n] * 1024);
    else if constexpr (n < NS) {
      const int q = (lane >> 2) & 3, xg = ((q >> 1) & 1) | ((g & 1) << 1);
      const int gr = SCH.db[n - P2] == 4 ? 2 * p + fj : 2 * fj + SCH.t01[n - P2];
      lds_char* r = lds + K3_RING + (4 * g + q) * 128 + ((gr ^ xg) * 32) + 8 * (lane & 3) + SCH.lo[n] * 1024;
      a[n % 6] = tr_frag(r, r + 2048);
    }
  };
#pragma unroll
  for (int i = 0; i < 6; ++i) hf[i & 1][i >> 1] = *(lds_u32x4*)(lds + ST0 + (mi * 2 + (i & 1)) * 3072 + (i >> 1) * 1024 + lane * 16);
  static_for<0, 3>(read_frag);
  WAITL0();
  BAR();
  first_slot(std::integral_constant<int, 2>{});
  ST();
  const int fl_mine = (int)OFF_FLAG + b * 128;
  const char* fl_other = P.ws + OFF_FLAG + (b ^ 8) * 128;
  const int xh_off = (int)OFF_XH + (b * 4 + mi) * 18432;
  const srd_t sx = make_srd(P.ws + OFF_XH + (size_t)((b ^ 8) * 4 + mi) * 18432, 18432);
  const int pbase = (int)OFF_XH + (b ^ 8) * 73728 + mi * 18432;
  lds_char* hh_wr = hh_rd + 8 * fj;
  float sigma[2], gsc[2];
  f32x4 acc[2][2][2][2];
  f32x4 oc[9][2];
  u32x4 ov[2], ho[2][2], hp[9][2], xl[2];
  u32x2 hpk;
  u32x2 ovs[2];
  unsigned pollv, pfs = 0;

  auto epi = [&](auto gc, auto ic) __attribute__((always_inline)) {
    constexpr int G = decltype(gc)::value, i = decltype(ic)::value;
    if constexpr (i < (G == 4 ? 46 : 92)) {
      constexpr int uu = i / 46, mh = (i % 46) / 23, r = i % 23, set = G & 1, U = 2 * G + uu;
      if constexpr (r < 20) {
        constexpr int e = (r / 10) * 2 + (r & 1), ph = (r % 10) >> 1;
        float tv = acc[set][uu][0][mh][e];
        if (ph == 0) tv = tv * gsc[mh];
        if (ph == 1) tv = __builtin_amdgcn_exp2f(tv);
        if (ph == 2) tv = __builtin_fmaf(tv, sigma[mh], sigma[mh]);
        if (ph == 3) tv = __builtin_amdgcn_rcpf(tv);
        if (ph == 4) tv = tv * acc[set][uu][1][mh][e];
        asm volatile("" : "+v"(tv));
        acc[set][uu][0][mh][e] = tv;
      } else if constexpr (r < 22) {
        unsigned t = pk2(acc[set][uu][0][mh][2 * (r - 20)], acc[set][uu][0][mh][2 * (r - 20) + 1]);
        asm volatile("" : "+v"(t));
        hpk[r - 20] = t;
      } else {
        *(lds_u32x2*)(hh_wr + mh * 9216 + U * 1024) = hpk;
      }
      SB0();
    }
  };
  auto opack = [&](const int pr, const int i) __attribute__((always_inline)) {
    const int mh = i >> 2, r = i & 3;
    if (pr == 4 && r >= 2) return;
    const int ti = pr == 4 ? 8 : 2 * pr + (r >> 1), e = pr == 4 ? 2 * r : 2 * (r & 1);
    unsigned t = pk2(oc[ti][mh][e], oc[ti][mh][e + 1]);
    asm volatile("" : "+v"(t));
    if (pr == 4) ovs[mh][r & 1] = t; else ov[mh][r] = t;
    SB0();
  };
  auto ostore = [&](const int pr) __attribute__((always_inline)) {
#pragma unroll
    for (int mh = 0; mh < 2; ++mh) {
      if (pr < 4) st128(ov[mh], wsr, voff, pbase + (p + 2 * pr + fj + 9 * mh) * 1024);
      else { __builtin_amdgcn_raw_buffer_store_b64(ovs[mh], wsr, voff, pbase + ((p ? 0 : 8) + 9 * mh) * 1024 + 8 * fj, 16); }
    }
    SB0();
  };

  static_for<0, NS>([&](auto nc) __attribute__((always_inline)) {
    constexpr int n = decltype(nc)::value;
    constexpr bool bar = SCH.bar[n];
    if constexpr (n == 9) WAITV(12);
    else if constexpr (FJ == 0 && SCH.wl[n] >= 0) waitv<SCH.wl[n]>();
    else if constexpr (FJ == 1 && SCH.wf[n] >= 0) waitv<SCH.wf[n]>();
    if constexpr (FJ == 1 && SCH.chk[n] >= 0) {
      asm volatile("" : "+v"(pollv));
      if (__builtin_amdgcn_readfirstlane(pollv) < (unsigned)SCH.chk[n]) {
#pragma unroll 1
        for (int spin = 0; spin < (1 << 22); ++spin) {
          asm volatile("global_load_dword %0, %1, %2 sc1\n\ts_waitcnt vmcnt(0)" : "=&v"(pollv) : "v"(0u), "s"(fl_other) : "memory");
          if (__builtin_amdgcn_readfirstlane(pollv) >= (unsigned)SCH.chk[n]) break;
          __builtin_amdgcn_s_sleep(2);
        }
      }
    }
    if constexpr (bar) { WAITL0(); BAR(); __builtin_amdgcn_s_setprio(3); }
    const u32x4 af = a[n % 6];
    if constexpr (SCH.prio[n] >= 0) __builtin_amdgcn_s_setprio(SCH.prio[n]);
    if constexpr (n == 72 || n == P2) ST();
    SB0();
    static_for<0, 2>([&](auto mc) __attribute__((always_inline)) {
      constexpr int mh = decltype(mc)::value;
      if constexpr (n < P2) {
        constexpr int y = n % 72;
        constexpr int set = (n / 72) & 1, S = n >= PH ? y >> 1 : y >> 2, mat = n >= PH ? y & 1 : (y >> 1) & 1, uu = n >= PH ? 0 : y & 1;
        if constexpr (S == 0) asm volatile("v_mfma_f32_16x16x32_bf16 %0, %1, %2, 0" : "=&v"(acc[set][uu][mat][mh]) : "v"(af), "v"(hf[mh][S]));
        else asm volatile("v_mfma_f32_16x16x32_bf16 %0, %1, %2, %0" : "+v"(acc[set][uu][mat][mh]) : "v"(af), "v"(hf[mh][S]));
      } else {
        constexpr int s2 = n - P2, U = SCH.u[s2], ti = SCH.db[s2] == 4 ? 8 : 2 * SCH.db[s2] + SCH.t01[s2];
        if constexpr (s2 < 9) asm volatile("v_mfma_f32_16x16x32_bf16 %0, %1, %2, 0" : "=&v"(oc[ti][mh]) : "v"(af), "v"(ho[0][mh]));
        else if constexpr (SCH.own[s2]) asm volatile("v_mfma_f32_16x16x32_bf16 %0, %1, %2, %0" : "+v"(oc[ti][mh]) : "v"(af), "v"(ho[U & 1][mh]));
        else asm volatile("v_mfma_f32_16x16x32_bf16 %0, %1, %2, %0" : "+v"(oc[ti][mh]) : "v"(af), "v"(hp[U][mh]));
      }
      SB0();
      if constexpr (mh == 0) { read_frag(std::integral_constant<int, n + 3>{}); SB0(); }
      if constexpr (SCH.eg[n] >= 0) {
        constexpr int f = SCH.e0[n] + (mh ? SCH.ne0[n] : 0), cnt = mh ? SCH.ne1[n] : SCH.ne0[n];
        static_for<f, f + cnt>([&](auto ic) __attribute__((always_inline)) { epi(std::integral_constant<int, SCH.eg[n]>{}, ic); });
      }
      if constexpr (SCH.og[n] >= 0) opack(SCH.og[n], SCH.o0[n] + mh);
    });
    if constexpr (FJ == 0 && SCH.dslot[n] >= 0) {
      constexpr int t = SCH.dslot[n], k = SCH.dk[n];
      if constexpr (t >= 27) {
        constexpr int sb = (t - 27) * 6 + k, ow = SCH.sown[sb], U = SCH.su[sb], d = SCH.sdb[sb];
        const unsigned vd = (lane >> 3) * (D_ * 2) + ((((lane >> 1) & 3) ^ ((lane >> 4) & 3)) * 32) + (lane & 1) * 16;
        bdma_i<(t % 3) * 24576 + k * 4096, 32 * U * (D_ * 2) + (d == 4 ? 512 : 128 * d)>(
            sd, vd, (288 * (ow ? c : c ^ 1) + 8 * mi) * (D_ * 2) + (d == 4 ? 0 : 640 * p), lds_a + K3_RING + mi * 1024);
      } else if constexpr (k % 3 == 0) bdma_i<(t % 3) * 24576 + k * 1024, t * 24576 + k * 1024>(sw, voff, sw0, ring_w);
      else asm volatile("buffer_load_dwordx4 %0, %1, %2 offen offset:%3 sc1 lds" :: "v"(voff), "s"(sw), "s"(sw0 + t * 24576 + (k / 3) * 3072), "n"((k % 3) * 1024) : "memory");
      SB0();
    }
    if constexpr (SCH.ost[n] >= 0) ostore(SCH.ost[n]);
    if constexpr (n == 5) { dma16x3s(P.ws + OFF_SSQ + mt * 2560, voff, lds_a + K3_SSQ + mi * 3072); SB0(); }
    if constexpr (n == 28) { dma16x3s(hmine + 12288, voff, lds_a + ST1 + (mi * 2 + fj) * 3072); SB0(); }
    if constexpr (n == 40) { dma16x3s(hmine + 15360, voff, lds_a + ST2 + (mi * 2 + fj) * 3072); SB0(); }
    if constexpr (n == 9 || n == 45 || n == 57) {
      constexpr int s0 = n == 9 ? 3 : (n == 45 ? 12 : 15), stg = n == 57 ? ST2 : ST1;
#pragma unroll
      for (int i = 0; i < 6; ++i) hf[i & 1][s0 + (i >> 1)] = *(lds_u32x4*)(lds + stg + (mi * 2 + (i & 1)) * 3072 + (i >> 1) * 1024 + lane * 16);
      SB0();
    }
    if constexpr (n == 21) {
#pragma unroll
      for (int i = 0; i < 12; ++i) hf[i & 1][6 + (i >> 1)] = *(lds_u32x4*)(lds + ST2 + (mi * 2 + (i & 1)) * 6144 + (i >> 1) * 1024 + lane * 16);
      SB0();
    }
    if constexpr (n == 50) {
#pragma unroll
      for (int mh = 0; mh < 2; ++mh) {
        float ssq = 0.f;
#pragma unroll
        for (int i = 0; i < 20; ++i) ssq += *(lds_float*)(lds + K3_SSQ + mi * 3072 + (i * 32 + mh * 16 + (lane & 15)) * 4);
        sigma[mh] = sqrtf(ssq * (1.f / D_) + LN_EPS);
        gsc[mh] = -1.4426950408889634f / sigma[mh];
        asm volatile("" : "+v"(sigma[mh]), "+v"(gsc[mh]));
      }
      SB0();
    }
    if constexpr (n == P2 - 1) { ho[0][0] = *(lds_u32x4*)(hh_rd); ho[0][1] = *(lds_u32x4*)(hh_rd + 9216); SB0(); }
    if constexpr (n >= P2 && n < P2 + 72 && (n - P2) % 9 == 4) {
      constexpr int U = (n - P2) / 9 + 1;
      ho[U & 1][0] = *(lds_u32x4*)(hh_rd + U * 1024); ho[U & 1][1] = *(lds_u32x4*)(hh_rd + 9216 + U * 1024);
      SB0();
    }
    if constexpr (FJ == 1) {
      if constexpr (SCH.xst[n] >= 0) {
        st128(xl[0], wsr, voff, xh_off + SCH.xst[n] * 1024);
        st128(xl[1], wsr, voff, xh_off + 9216 + SCH.xst[n] * 1024);
        SB0();
      }
      if constexpr (SCH.flag[n] >= 0) { __builtin_amdgcn_raw_buffer_store_b32((unsigned)SCH.flag[n], wsr, 0, fl_mine, 16); SB0(); }
      if constexpr (SCH.poll[n]) { asm volatile("global_load_dword %0, %1, %2 sc1" : "=&v"(pollv) : "v"(0u), "s"(fl_other) : "memory"); SB0(); }
      if constexpr (SCH.ld[n] >= 0) {
        constexpr int u = SCH.ld[n];
        bdma_sc1<u * 1024>(sx, voff, lds_a + K3_HH + mi * 18432);
        bdma_sc1<9216 + u * 1024>(sx, voff, lds_a + K3_HH + mi * 18432);
        SB0();
      }
      if constexpr (n == SCH.ebar) {
        if (mi == 0 && lane == 0) __hip_atomic_fetch_add((unsigned*)(P.ws + OFF_CNT + (mb * 2 + p) * 128), 1u, __ATOMIC_RELAXED, __HIP_MEMORY_SCOPE_AGENT);
        SB0();
      }
      if constexpr (SCH.pf[n]) {
        const int l41 = lane < 41 ? lane : 0, o = (((mb & 7) * 2 + p) * 4 + mi) * 10368 + l41 * 256;
        const char* pa = P.wd + (size_t)(c >> 1) * 663552 + (o < 663552 - 256 ? o : 663552 - 256);
        asm volatile("global_load_dword %0, %1, off\n\tglobal_load_dword %0, %1, off offset:128" : "+v"(pfs) : "v"(pa) : "memory");
        SB0();
      }
      if constexpr (SCH.flag[n] == 6) asm volatile("" :: "v"(pfs));
      if constexpr (SCH.xrd[n] >= 0) {
        xl[0] = *(lds_u32x4*)(hh_rd + SCH.xrd[n] * 1024); xl[1] = *(lds_u32x4*)(hh_rd + 9216 + SCH.xrd[n] * 1024);
        SB0();
      }
    }
    if constexpr (SCH.rd[n] >= 0) {
      constexpr int u = SCH.rd[n];
      hp[u][0] = *(lds_u32x4*)(hh_rd + u * 1024); hp[u][1] = *(lds_u32x4*)(hh_rd + 9216 + u * 1024);
      SB0();
    }
  });
  ST();
  MFMA_DRAIN();
#pragma unroll
  for (int i = 0; i < 8; ++i) opack(3, i);
  ostore(3);
  const int j = c >> 1, t16 = mb * 8 + j, f0 = t16 * 18 + 9 * p;
  const int qr = p + wid, qs = p ? 0 : 8;
  const int l16 = 16 * mi + (lane & 15);
  const int row = t16 * 16 + (lane & 15);
  const int hb = ((lane & 15) + 16 * (g >> 1)) * 16 + 8 * (g & 1), hbs = ((lane & 15) + 16 * (mi >> 1)) * 16 + 8 * (mi & 1);
  const u32x2 h0 = __builtin_amdgcn_raw_buffer_load_b64(wsr, hb, (int)OFF_HC + (f0 + qr) * 1024, 16);
  const u32x2 h1 = __builtin_amdgcn_raw_buffer_load_b64(wsr, hb + 512, (int)OFF_HC + (f0 + qr) * 1024, 16);
  u32x2 hs0, hs1;
  if constexpr (FJ == 0) {
    hs0 = __builtin_amdgcn_raw_buffer_load_b64(wsr, hbs, (int)OFF_HC + (f0 + qs) * 1024, 16);
    hs1 = __builtin_amdgcn_raw_buffer_load_b64(wsr, hbs + 512, (int)OFF_HC + (f0 + qs) * 1024, 16);
  }
  const float mean = __uint_as_float(__builtin_amdgcn_raw_buffer_load_b32(wsr, row * 4, (int)OFF_MEANH, 16));
  const int cnt_off = (int)OFF_CNT + (mb * 2 + p) * 128;
  if constexpr (FJ == 0) poll_counters(P.ws, cnt_off, 8u);
  else WAITV(0);
  BAR();
  ST();
  if constexpr (FJ == 1) {
    if (mi == 0 && lane == 0) __hip_atomic_fetch_add((unsigned*)(P.ws + cnt_off + 64), 1u, __ATOMIC_RELAXED, __HIP_MEMORY_SCOPE_AGENT);
    poll_counters(P.ws, cnt_off + 64, 8u);
  }
  ST();
  auto area = [&](const int k) { const int ck = 2 * k + (p ^ 1); return (int)OFF_XH + (((mb & 7) * 4 + (ck & 3)) * 8 + (ck >> 2) * 2 + (mb >> 3)) * 73728 + j * 9216; };
  u32x4 pv[8], ps[2];
#pragma unroll
  for (int k = 0; k < 8; ++k) pv[k] = __builtin_amdgcn_raw_buffer_load_b128(wsr, voff, area(k) + qr * 1024, 16);
  if constexpr (FJ == 0) {
#pragma unroll
    for (int k = 0; k < 2; ++k) ps[k] = __builtin_amdgcn_raw_buffer_load_b128(wsr, area(4 * k + g) + l16 * 16, qs * 1024, 16);
  }
  float o[8];
  o[0] = bflo(h0[0]); o[1] = bfhi(h0[0]); o[2] = bflo(h0[1]); o[3] = bfhi(h0[1]);
  o[4] = bflo(h1[0]); o[5] = bfhi(h1[0]); o[6] = bflo(h1[1]); o[7] = bfhi(h1[1]);
#pragma unroll
  for (int k = 0; k < 8; ++k) o[k] += mean;
#pragma unroll
  for (int r = 0; r < 8; ++r)
#pragma unroll
    for (int k = 0; k < 4; ++k) { o[2 * k] += bflo(pv[r][k]); o[2 * k + 1] += bfhi(pv[r][k]); }
  {
    char* d = P.out + (size_t)row * 1152 + (9 * p + qr) * 64 + g * 8;
    *(u32x2*)d = (u32x2){pk2(o[0], o[1]), pk2(o[2], o[3])};
    *(u32x2*)(d + 32) = (u32x2){pk2(o[4], o[5]), pk2(o[6], o[7])};
  }
  if constexpr (FJ == 0) {
    float os[8];
#pragma unroll
    for (int k = 0; k < 4; ++k) { os[2 * k] = bflo(ps[0][k]) + bflo(ps[1][k]); os[2 * k + 1] = bfhi(ps[0][k]) + bfhi(ps[1][k]); }
#pragma unroll
    for (int k = 0; k < 8; ++k) { os[k] += __shfl_xor(os[k], 16); os[k] += __shfl_xor(os[k], 32); }
    if (lane < 16) {
      char* d = P.out + (size_t)row * 1152 + (9 * p + qs) * 64 + mi * 8;
      *(u32x2*)d = (u32x2){pk2(os[0] + bflo(hs0[0]) + mean, os[1] + bfhi(hs0[0]) + mean), pk2(os[2] + bflo(hs0[1]) + mean, os[3] + bfhi(hs0[1]) + mean)};
      *(u32x2*)(d + 32) = (u32x2){pk2(os[4] + bflo(hs1[0]) + mean, os[5] + bfhi(hs1[0]) + mean), pk2(os[6] + bflo(hs1[1]) + mean, os[7] + bfhi(hs1[1]) + mean)};
    }
  }
}

__global__ __launch_bounds__(512, 2) void k3_ffn(Ptrs P) {
  extern __shared__ __attribute__((aligned(16))) char smem[];
  const int lane = threadIdx.x & 63, wid = __builtin_amdgcn_readfirstlane(threadIdx.x >> 6);
  const int b = blockIdx.x;
  const int c = ((b & 7) >> 1) * 4 + ((b >> 3) & 3), mb = (b & 1) * 8 + (b >> 5);
  if (wid < 4) k3_body<0>(P, (lds_char*)smem, lane, wid, b, c, mb);
  else k3_body<1>(P, (lds_char*)smem, lane, wid - 4, b, c, mb);
}

constexpr int K0_IMG = 4608;
constexpr int K0_RED = 3 * K0_IMG;
constexpr int K0_LDS = K0_RED + 9 * 8 * 8;

DI float dpp_sum8(float v) {
  v += __shfl_xor(v, 1); v += __shfl_xor(v, 2); v += __shfl_xor(v, 4);
  return v;
}

__global__ __launch_bounds__(768) void k0_prep(Ptrs P) {
  extern __shared__ __attribute__((aligned(16))) char smem[];
  lds_char* lds = (lds_char*)smem;
  const int lane = threadIdx.x & 63, wid = __builtin_amdgcn_readfirstlane(threadIdx.x >> 6);
  const int b = blockIdx.x;
  ST_DECL; ST();
  if (wid < 9) {
    const int row = b * 8 + (lane >> 3), i = lane & 7;
    const u32x4 v = *(const u32x4*)(P.x + (size_t)row * 1152 + wid * 128 + i * 16);
    float e[8];
#pragma unroll
    for (int k = 0; k < 4; ++k) { e[2 * k] = bflo(v[k]); e[2 * k + 1] = bfhi(v[k]); }
    const float sum = dpp_sum8(((e[0] + e[1]) + (e[2] + e[3])) + ((e[4] + e[5]) + (e[6] + e[7])));
    const float lm = sum * (1.f / 64.f);
    float m2 = 0.f;
#pragma unroll
    for (int k = 0; k < 8; ++k) { const float d = e[k] - lm; m2 = __builtin_fmaf(d, d, m2); }
    m2 = dpp_sum8(m2);
    if (i == 0) *(lds_f32x2*)(lds + K0_RED + (wid * 8 + (lane >> 3)) * 8) = (f32x2){lm, m2};
    WAITL0();
    BAR();
    ST();
    f32x2 pr[9];
#pragma unroll
    for (int w = 0; w < 9; ++w) pr[w] = *(lds_f32x2*)(lds + K0_RED + (w * 8 + (lane >> 3)) * 8);
    float mean = 0.f;
#pragma unroll
    for (int w = 0; w < 9; ++w) mean += pr[w][0];
    mean *= (1.f / 9.f);
    float tot = 0.f;
#pragma unroll
    for (int w = 0; w < 9; ++w) { const float d = pr[w][0] - mean; tot += __builtin_fmaf(64.f * d, d, pr[w][1]); }
    const float rstd = rsqrtf(tot * (1.f / D_) + LN_EPS);
    u32x4 o;
#pragma unroll
    for (int k = 0; k < 4; ++k) o[k] = pk2((e[2 * k] - mean) * rstd, (e[2 * k + 1] - mean) * rstd);
    WS_ST16(P, (int)OFF_XN + ((row >> 5) * 36 * 64 + (row & 31) + 32 * (i & 1) + (4 * wid + (i >> 1)) * 64) * 16, o, ST_AUX);
    if (wid == 0 && i == 0) WS_ST4(P, OFF_MEANX + row * 4, __float_as_uint(mean), ST_AUX);
    return;
  }
  const int id = (wid - 9) * 256 + b;
  lds_char* img = lds + (wid - 9) * K0_IMG;
  const unsigned img_a = (unsigned)(uintptr_t)img;
  const int g = lane >> 4, q = (lane >> 2) & 3, p = lane & 3;
  unsigned pf_sink = 0;
  if (wid == 10 && b < 96) {
    unsigned long long pc, k1a;
    asm volatile("s_getpc_b64 %0" : "=s"(pc));
    asm volatile("s_getpc_b64 s[40:41]\n\ts_add_u32 s40, s40, _Z6k3_ffn4Ptrs@rel32@lo+4\n\ts_addc_u32 s41, s41, _Z6k3_ffn4Ptrs@rel32@hi+12\n\t"
                 "s_mov_b64 %0, s[40:41]" : "=s"(k1a) :: "s40", "s41", "scc");
    const char* pp = (const char*)k1a + ((b >> 3) * 64 + lane) * 128;
    pp = pp < (const char*)pc ? pp : (const char*)k1a;
    asm volatile("global_load_dword %0, %1, off" : "=&v"(pf_sink) : "v"(pp) : "memory");
  }
  if (id < 576) {
    const int s = id >> 4, h = (id >> 3) & 1, hp0 = (id & 7) * 2;
    const char* src = P.wqkv + (size_t)(16 * s + 8 * h) * 3456 + hp0 * 72;
#pragma unroll
    for (int it = 0; it < 4; ++it) {
      int e = it * 64 + lane; e = e < 216 ? e : 215;
      const int sgm = e / 9, ch = e - sgm * 9;
      dma16x1(src, sgm * 1152 + ch * 16, img_a + it * 1024);
    }
    BAR();
    ST();
    WAITV(0);
    asm volatile("" :: "v"(pf_sink));
    ST();
#pragma unroll
    for (int it = 0; it < 4; ++it) {
      int Q = 16 * it + 4 * g + p; Q = Q < 54 ? Q : 53;
      const int hl = Q >= 27 ? 1 : 0, sg = Q - 27 * hl;
      const int cb = sg < 4 ? 8 * sg : (sg < 8 ? 8 * sg + 8 : (sg == 8 ? 32 : (sg < 18 ? 8 * sg + 72 : 8 * sg + 144)));
      lds_char* a = img + q * 432 + cb + 72 * hl;
      u32x4 v = tr_frag(a, a + 4 * 432);
      const int o = it * 64 + lane, ol = o >= 108 ? 1 : 0, slot = o - 108 * ol;
      const int off = slot < 32 ? WQT_Q + s * 1024 + (h * 32 + slot) * 16 : s * 2432 + (h * 76 + slot - 32) * 16;
      if (o < 216) WS_ST16(P, OFF_WQT + (hp0 + ol) * (int)WQT_HP + off, v, ST_AUX);
    }
  } else if (id < 720) {
    const int j = id - 576, ks = j >> 2, cb = j & 3;
#pragma unroll
    for (int it = 0; it < 5; ++it) {
      int e = it * 64 + lane; e = e < 288 ? e : 287;
      const int kk = e / 18, ch = e - kk * 18, h = kk >> 3, jj = kk & 7;
      const int row = ks < 32 ? 18 * ks + (jj & 3) + 8 * (jj >> 2) + 4 * h : 18 * (8 * (ks - 32) + jj) + 16 + h;
      if (it < 4 || lane < 32) dma16x1(P.wo + cb * 288, row * 1152 + ch * 16, img_a + it * 1024);
    }
    BAR();
    WAITV(0);
#pragma unroll
    for (int it = 0; it < 5; ++it) {
      int G16 = 4 * it + g; G16 = G16 < 18 ? G16 : 17;
      const int h = G16 >= 9 ? 1 : 0, sg = G16 - 9 * h;
      lds_char* a = img + (8 * h + q) * 288 + (16 * sg + 4 * p) * 2;
      u32x4 v = tr_frag(a, a + 4 * 288);
      if (it < 4 || lane < 32) WS_ST16(P, OFF_WOT + cb * (int)WOT_CB + ks * 4672 + (h * 146 + 16 * sg + (lane & 15)) * 16, v, ST_AUX);
    }
  } else if (id < 756) {
    const int row = (id - 720) * 16 + (lane >> 2), i = lane & 3;
    const char* src = P.wo + (size_t)row * 1152 + i * 16;
    u32x4 v[18];
#pragma unroll
    for (int j = 0; j < 18; ++j) v[j] = *(const u32x4*)(src + j * 64);
    BAR();
    float s4[4] = {0.f, 0.f, 0.f, 0.f};
    const bf16x2 one2 = __builtin_bit_cast(bf16x2, 0x3f803f80u);
#pragma unroll
    for (int j = 0; j < 18; ++j)
#pragma unroll
      for (int k = 0; k < 4; ++k) { const unsigned t = v[j][k]; s4[k] = __builtin_amdgcn_fdot2_f32_bf16(__builtin_bit_cast(bf16x2, t), one2, s4[k], false); }
    float sum = (s4[0] + s4[1]) + (s4[2] + s4[3]);
    sum += __shfl_xor(sum, 1); sum += __shfl_xor(sum, 2);
    const float mean = sum * (1.f / D_);
    const int hd = row / 18, e = row % 18;
    const int ks = e < 16 ? hd : 32 + (hd >> 3), jj = e < 16 ? (e & 3) + 4 * (e >> 3) : (hd & 7), hh = e < 16 ? (e >> 2) & 1 : e - 16;
    const unsigned hi = pk2(mean, 0.f) & 0xffffu;
    const unsigned lo = pk2(mean - bflo(hi), 0.f) & 0xffffu;
    unsigned short* d = (unsigned short*)(P.ws + OFF_WOT + (size_t)i * WOT_CB + ks * 4672 + (hh * 146 + 144) * 16 + jj * 2);
    d[0] = (unsigned short)hi; d[8] = (unsigned short)lo;
  } else {
    const u32x4 z = {0u, 0u, 0u, 0u};
    for (int k = id - 756; k < 50; k += 12) WS_ST16(P, OFF_KV + k * 1024 + lane * 16, z, ST_AUX);
    for (int k = id - 756; k < 71; k += 12) WS_ST16(P, OFF_FLAG + k * 1024 + lane * 16, z, ST_AUX);
    BAR();
  }
}

extern "C" void kernel_launch(void* const* d_in, const int* in_sizes, int n_in, void* d_out, int out_size, void* d_ws,
                              size_t ws_size, hipStream_t stream) {
  Ptrs P;
  P.x = (const char*)d_in[0]; P.wqkv = (const char*)d_in[1]; P.wo = (const char*)d_in[2];
  P.wg = (const char*)d_in[4]; P.wu = (const char*)d_in[5]; P.wd = (const char*)d_in[6];
  P.ws = (char*)d_ws; P.out = (char*)d_out;
  (void)hipFuncSetAttribute((const void*)k3_ffn, hipFuncAttributeMaxDynamicSharedMemorySize, K3_LDS);
  k0_prep<<<256, 768, K0_LDS, stream>>>(P);
  k3_ffn<<<256, 512, K3_LDS, stream>>>(P);
}
```

```cpp
#include <hip/hip_runtime.h>
#include <hip/hip_bf16.h>
#include <stdint.h>
#include <type_traits>

typedef __attribute__((ext_vector_type(8))) short bf16x8;
typedef __attribute__((ext_vector_type(4))) short s16x4;
typedef __attribute__((ext_vector_type(16))) float f32x16;
typedef __attribute__((ext_vector_type(4))) float f32x4;
typedef __attribute__((ext_vector_type(2))) float f32x2;
typedef __attribute__((ext_vector_type(2))) __bf16 bf16x2;
typedef __attribute__((ext_vector_type(4))) unsigned u32x4;
typedef __attribute__((ext_vector_type(2))) unsigned u32x2;
typedef __attribute__((address_space(3))) char lds_char;
typedef __attribute__((address_space(3))) s16x4 lds_s16x4;
typedef __attribute__((address_space(3))) u32x4 lds_u32x4;
typedef __attribute__((address_space(3))) u32x2 lds_u32x2;
typedef __attribute__((address_space(3))) f32x4 lds_f32x4;
typedef __attribute__((address_space(3))) float lds_float;
typedef __attribute__((address_space(3))) f32x2 lds_f32x2;
typedef __attribute__((address_space(3))) unsigned lds_uint;

#define DI __device__ __forceinline__
#define MFMA32(a, b, c) __builtin_amdgcn_mfma_f32_32x32x16_bf16((a), (b), (c), 0, 0, 0)
#define MFMA16(a, b, c) __builtin_amdgcn_mfma_f32_16x16x32_bf16((a), (b), (c), 0, 0, 0)
#define WAITV(n) asm volatile("s_waitcnt vmcnt(" #n ")" ::: "memory")
#define WAITL0() asm volatile("s_waitcnt lgkmcnt(0)" ::: "memory")
#define BAR() asm volatile("s_barrier" ::: "memory")
#define SB0() __builtin_amdgcn_sched_barrier(0)

constexpr int S_ = 2048, D_ = 576, F_ = 4608;
constexpr float LN_EPS = 1e-5f;

constexpr size_t FLB = (size_t)S_ * D_ * 2;
constexpr size_t WQT_HP = 36 * 3456;
constexpr int WQT_Q = 36 * 2432;
constexpr size_t WOT_CB = 36 * 4672;
constexpr size_t WF_CH = 648 * 1024;
constexpr size_t OFF_XN = 0;
constexpr size_t OFF_PQ = OFF_XN + FLB;
constexpr size_t OFF_HC = OFF_PQ + FLB;
constexpr size_t OFF_MEANX = OFF_HC + FLB;
constexpr size_t OFF_MEANH = OFF_MEANX + 8192;
constexpr size_t OFF_SSQ = OFF_MEANH + 8192;
constexpr size_t OFF_WBAR = OFF_SSQ + 20 * 8192;
constexpr size_t OFF_KV = OFF_WBAR + 4096;
constexpr size_t OFF_WQT = OFF_KV + 53248;
constexpr size_t OFF_WOT = OFF_WQT + 16 * WQT_HP + 16384;
constexpr size_t OFF_WF = OFF_WOT + 4 * WOT_CB + 4096;
constexpr size_t OFF_PART = OFF_WF + 16 * WF_CH + 65536;
constexpr size_t OFF_XH = OFF_PART;
constexpr size_t OFF_FLAG = OFF_XH + 256 * 73728;
constexpr size_t OFF_CNT = OFF_FLAG + 256 * 128;
constexpr size_t OFF_G1 = OFF_CNT + 32 * 128;
constexpr size_t OFF_G3 = OFF_G1 + 256 * 128;
constexpr size_t OFF_GW = OFF_G3 + 16 * 128;
constexpr size_t WS_END = OFF_GW + 8 * 128;

#define ST_DECL
#define ST()
#define ST_OUT(k, slot)
DI unsigned pk2(float a, float b) {
  f32x2 v = {a, b};
  bf16x2 r = __builtin_convertvector(v, bf16x2);
  return __builtin_bit_cast(unsigned, r);
}
DI float bflo(unsigned u) { return __uint_as_float(u << 16); }
DI float bfhi(unsigned u) { return __uint_as_float(u & 0xffff0000u); }
DI float phi(float t) {
  const float e = __builtin_amdgcn_fmed3f(__builtin_amdgcn_exp2f(t * 1.4426950408889634f), 0.f, 1.f);
  return (__builtin_amdgcn_fmed3f(t, 0.f, __builtin_inff()) + 1e-6f) + e;
}
DI bf16x8 as_frag(u32x4 v) { return __builtin_bit_cast(bf16x8, v); }

DI void dma16(const void* gsrc, unsigned lds_dst) {
  unsigned keep;
  asm volatile("s_mov_b32 %0, m0\n\ts_mov_b32 m0, %2\n\ts_nop 0\n\tglobal_load_lds_dwordx4 %1, off\n\ts_mov_b32 m0, %0"
               : "=&s"(keep) : "v"(gsrc), "s"(lds_dst) : "memory");
}
DI void dma16x3(const void* sbase, unsigned voff, unsigned lds_dst) {
  unsigned keep;
  asm volatile("s_mov_b32 %0, m0\n\ts_mov_b32 m0, %3\n\ts_nop 0\n\t"
               "global_load_lds_dwordx4 %1, %2\n\t"
               "global_load_lds_dwordx4 %1, %2 offset:1024\n\t"
               "global_load_lds_dwordx4 %1, %2 offset:2048\n\t"
               "s_mov_b32 m0, %0"
               : "=&s"(keep) : "v"(voff), "s"(sbase), "s"(lds_dst) : "memory");
}
DI void dma16x3s(const void* sbase, unsigned voff, unsigned lds_dst) {
  unsigned keep;
  asm volatile("s_mov_b32 %0, m0\n\ts_mov_b32 m0, %3\n\ts_nop 0\n\t"
               "global_load_lds_dwordx4 %1, %2 sc1\n\t"
               "global_load_lds_dwordx4 %1, %2 offset:1024 sc1\n\t"
               "global_load_lds_dwordx4 %1, %2 offset:2048 sc1\n\t"
               "s_mov_b32 m0, %0"
               : "=&s"(keep) : "v"(voff), "s"(sbase), "s"(lds_dst) : "memory");
}
DI void dma16x1(const void* sbase, unsigned voff, unsigned lds_dst) {
  unsigned keep;
  asm volatile("s_mov_b32 %0, m0\n\ts_mov_b32 m0, %3\n\ts_nop 0\n\t"
               "global_load_lds_dwordx4 %1, %2\n\t"
               "s_mov_b32 m0, %0"
               : "=&s"(keep) : "v"(voff), "s"(sbase), "s"(lds_dst) : "memory");
}
DI u32x4 tr_frag(lds_char* p0, lds_char* p1) {
  s16x4 lo = __builtin_amdgcn_ds_read_tr16_b64_v4i16((lds_s16x4*)p0);
  s16x4 hi = __builtin_amdgcn_ds_read_tr16_b64_v4i16((lds_s16x4*)p1);
  u32x2 a = __builtin_bit_cast(u32x2, lo), b = __builtin_bit_cast(u32x2, hi);
  u32x4 r = {a[0], a[1], b[0], b[1]};
  return r;
}

#define ST_AUX 16
#define WF_AUX 16
#define WS_RSRC(P) __builtin_amdgcn_make_buffer_rsrc((P).ws, 0, (int)WS_END, 0x00020000)
#define WS_ST16(P, off, v, aux) __builtin_amdgcn_raw_buffer_store_b128((v), WS_RSRC(P), (int)(off), 0, (aux))
#define WS_ST8(P, off, v, aux) __builtin_amdgcn_raw_buffer_store_b64((v), WS_RSRC(P), (int)(off), 0, (aux))
#define WS_ST4(P, off, v, aux) __builtin_amdgcn_raw_buffer_store_b32((v), WS_RSRC(P), (int)(off), 0, (aux))
template <class R> DI void st128(u32x4 v, R rsrc, int voff, int soff) {
  __builtin_amdgcn_raw_buffer_store_b128(v, rsrc, voff, soff, 16);
  asm volatile("s_nop 1" :: "v"(v));
}
struct Ptrs {
  const char* x; const char* wqkv; const char* wo; const char* wg; const char* wu; const char* wd;
  char* ws; char* out;
};

constexpr int ITEM_LDS = 9216;
constexpr int SUB_PER_XCD = 486;

DI void sub_load(const Ptrs& P, int x, int j, int lane, unsigned lds_base) {
  const int rl = lane >> 3, cp = lane & 7;
  const int cb = j / 36, r = j % 36, S = r >> 1, mat = r & 1;
  const char* base = (mat ? P.wu : P.wg) + (size_t)(32 * S) * (F_ * 2) + (576 * x + 64 * cb) * 2;
#pragma unroll
  for (int it = 0; it < 4; ++it) {
    const int X = ((rl >> 1) & 1) | ((it & 1) << 1);
    dma16x1(base + (size_t)(8 * it) * (F_ * 2), rl * (F_ * 2) + (((cp >> 1) ^ X) * 32) + (cp & 1) * 16, lds_base + it * 1024);
  }
}
DI void sub_store(const Ptrs& P, int x, int j, int lane, lds_char* img) {
  const int g = lane >> 4, q = (lane >> 2) & 3, p = lane & 3;
  const int X = ((q >> 1) & 1) | ((g & 1) << 1);
  const int cb = j / 36, r = j % 36, S = r >> 1, mat = r & 1;
#pragma unroll
  for (int u4 = 0; u4 < 4; ++u4) {
    lds_char* a = img + (8 * g + q) * 128 + ((u4 ^ X) * 32) + 8 * p;
    u32x4 v = tr_frag(a, a + 512);
    const int ug = 4 * cb + u4, cc = ug >= 18 ? 1 : 0, u = ug - 18 * cc, U = u >> 1;
    const int n = U < 8 ? (U >> 1) * 72 + S * 4 + mat * 2 + (U & 1) : 288 + 2 * S + mat;
    st128(v, WS_RSRC(P), lane * 16, (int)OFF_WF + (2 * x + cc) * (int)WF_CH + (2 * n + (u & 1)) * 1024);
  }
}

#define CODE_PF 1
#define FRONT_PF 1
#define N0_SUB 0
#define N1_SUB 256
constexpr int K0_ITEMS = 704 + 8 * (N0_SUB / 2);

constexpr int K1_W = 0;
constexpr int K1_ITEM = 124 * 1024;
constexpr int KVP = 192;
constexpr int K2_O = 49152;
constexpr int K2_RED = K2_O + 36864;
constexpr int K2_MEAN = K2_RED + 32768;
static_assert(K2_MEAN + 2048 <= K1_ITEM, "lds");

DI void poll_counters(const char* base, const int off, const unsigned need) {
#pragma unroll 1
  for (int spin = 0; spin < (1 << 22); ++spin) {
    unsigned v;
    asm volatile("global_load_dword %0, %1, %2 sc1\n\ts_waitcnt vmcnt(0)" : "=&v"(v) : "v"(off), "s"(base) : "memory");
    if (__builtin_amdgcn_ballot_w64(v < need) == 0ull) break;
    __builtin_amdgcn_s_sleep(3);
  }
}

constexpr int K3_RING = 0;
constexpr int K3_HH = 73728;
constexpr int K3_SSQ = K3_HH + 73728;
constexpr int K3_LDS = K3_SSQ + 4 * 3072;
#define DSTEP 2
#define EBAR 156
#define XOFF 1
#define DMA0 1
#define SLOW0 7
constexpr int NS = 486, PH = 288, P2 = 324;

struct Sch {
  signed char own[162] = {}, u[162] = {}, db[162] = {}, t01[162] = {};
  short rs[NS + 4] = {}, lo[NS + 4] = {};
  signed char sown[90] = {}, su[90] = {}, sdb[90] = {}, pf[NS] = {};
  bool bar[NS] = {};
  signed char dslot[NS] = {}, dk[NS] = {}, prio[NS] = {};
  signed char e0[NS] = {}, ne0[NS] = {}, ne1[NS] = {}, eg[NS] = {};
  signed char og[NS] = {}, o0[NS] = {};
  signed char xrd[NS] = {}, cbar[NS] = {}, xst[NS] = {}, ost[NS] = {}, xop[NS] = {};
  signed char flag[NS] = {}, fbar[NS] = {}, poll[NS] = {}, chk[NS] = {}, ld[NS] = {}, wr[NS] = {}, rd[NS] = {};
  signed char wl[NS] = {}, wf[NS] = {};
  int ebar = 0;
  bool ok = true;
};
constexpr Sch make_sch() {
  Sch s;
  int n2 = 0, nb = 0;
  auto put = [&](int own, int u, int db) {
    s.sown[nb] = own; s.su[nb] = u; s.sdb[nb] = db;
    for (int t = 0; t < (db == 4 ? 1 : 2); ++t) {
      s.own[n2] = own; s.u[n2] = u; s.db[n2] = db; s.t01[n2] = t;
      s.rs[P2 + n2] = 27 + nb / 6; s.lo[P2 + n2] = ((27 + nb / 6) % 3) * 24 + (nb % 6) * 4; ++n2;
    }
    ++nb;
  };
  for (int u = 0; u < 9; ++u) for (int d = 0; d < 5; ++d) put(1, u, d);
  for (int u = 0; u < 6; ++u) { put(0, u, 4); put(0, u, 0); put(0, u, 1); }
  for (int u = 6; u < 9; ++u) { put(0, u, 4); put(0, u, 0); }
  for (int u = 6; u < 9; ++u) put(0, u, 1);
  for (int d = 2; d < 4; ++d) for (int u = 0; u < 9; ++u) put(0, u, d);
  for (int n = 0; n < P2; ++n) { s.rs[n] = n / 12; s.lo[n] = ((n / 12) % 3) * 24 + (n % 12) * 2; }
  for (int n = NS; n < NS + 4; ++n) { s.rs[n] = s.rs[NS - 1]; s.lo[n] = s.lo[NS - 1]; }
  for (int n = 0; n < NS; ++n) {
    s.bar[n] = s.rs[n + 3] != s.rs[n + 2];
    s.dslot[n] = -1; s.prio[n] = -1; s.eg[n] = -1; s.og[n] = -1; s.flag[n] = -1; s.fbar[n] = -1; s.poll[n] = 0; s.chk[n] = -1;
    s.ld[n] = -1; s.rd[n] = -1; s.wl[n] = -1; s.wf[n] = -1; s.xst[n] = -1; s.ost[n] = -1; s.xrd[n] = -1; s.cbar[n] = -1; s.pf[n] = 0;
  }
  for (int n = 0; n < NS; ++n) if (s.bar[n]) {
    int e = n + 1; while (e < NS && !s.bar[e]) ++e;
    const int L = e - n, tgt = s.rs[n + 3] + 2;
    if (tgt <= 41) for (int k = 0; k < 6; ++k) { s.dslot[n + (k * L) / 6 + 1] = tgt; s.dk[n + (k * L) / 6 + 1] = k; if ((k * L) / 6 + 1 >= L) s.ok = false; }
    if (e < NS) { s.prio[n + L / 2] = 2; s.prio[e - 3] = 1; s.prio[e - 1] = 0; }
  }
  auto nbar = [&](int n) { while (!s.bar[n]) ++n; return n; };
  for (int g = 0; g < 5; ++g) {
    const int start = (g < 4 ? 72 * (g + 1) : P2) + 2, tot = g == 4 ? 46 : 92;
    int e = 0;
    for (int n = start; e < tot; ++n) {
      const int c0 = g == 4 ? 2 : 1, c1 = g >= 3 ? 2 : ((n - start) & 1);
      if (s.eg[n] >= 0) s.ok = false;
      s.eg[n] = g; s.e0[n] = e; s.ne0[n] = c0; s.ne1[n] = c1;
      for (int i = e; i < e + c0 + c1 && i < tot; ++i) if (i % 46 == 45) { int m = n + 1; while (!s.bar[m]) ++m; m += XOFF; while (s.xrd[m] >= 0) m += 3; s.xrd[m] = 2 * g + i / 46; s.xst[m + 2] = 2 * g + i / 46; }
      e += c0 + c1;
      if (g < 3 && n >= start + 68) s.ok = false;
    }
  }
  s.og[P2 + 119] = 4; s.o0[P2 + 119] = 0; s.og[P2 + 120] = 4; s.o0[P2 + 120] = 4; s.ost[P2 + 120] = 8; s.ost[P2 + 122] = 9;
  const int opk[3] = {121, 128, 145};
  for (int k = 0; k < 3; ++k) {
    for (int i = 0; i < 4; ++i) { const int n = P2 + opk[k] + i; s.og[n] = k; s.o0[n] = 2 * i; }
    s.ost[P2 + opk[k] + 3] = 2 * k; s.ost[P2 + opk[k] + 6] = 2 * k + 1;
  }
  int lds_[9] = {16, 18, 24, 34, 43, 44, 76, 78, 80}, rds[9] = {};
  for (int u = 0; u < 9; ++u) {
    const int cbn = nbar(P2 + lds_[(u / 3) * 3 + 2] + 17);
    rds[u] = cbn - P2 + 1 + (u < 6 ? 3 : 2) * (u % 3);
    s.ld[P2 + lds_[u]] = u; s.rd[P2 + rds[u]] = u; s.cbar[cbn] = u;
  }
  s.poll[P2 + 3] = 1; s.chk[P2 + 15] = 6;
  s.poll[P2 + 63] = 1; s.chk[P2 + 75] = 9;
  s.xop[28] = 3; s.xop[40] = 3;
  s.pf[100] = 1;
  const int fb[2] = {PH + 21, nbar(P2 + 40)}, fv[2] = {6, 9};
  for (int k = 0; k < 2; ++k) { if (!s.bar[fb[k]]) s.ok = false; s.fbar[fb[k]] = fv[k]; s.flag[fb[k] + 1] = fv[k]; }
  s.ebar = P2 + EBAR;
  int cl[NS + 1] = {}, cf[NS + 1] = {};
  for (int n = 9; n < NS; ++n) {
    const int both = (s.ost[n] >= 0 ? 1 : 0) + s.xop[n];
    cl[n + 1] = cl[n] + both + (s.dslot[n] >= 0 ? 1 : 0);
    cf[n + 1] = cf[n] + both + (s.xst[n] >= 0 ? 2 : 0) + (s.flag[n] >= 0 ? 1 : 0) + s.poll[n] + (s.ld[n] >= 0 ? 2 : 0) + 2 * s.pf[n];
  }
  auto need = [&](int n, int ul_, int uf_) {
    if (ul_ >= 0) { const int w = cl[n] - ul_; if (s.wl[n] < 0 || w < s.wl[n]) s.wl[n] = w; }
    if (uf_ >= 0) { const int w = cf[n] - uf_; if (s.wf[n] < 0 || w < s.wf[n]) s.wf[n] = w; }
  };
  for (int n = 21; n < NS; ++n) {
    if (s.bar[n]) {
      int last = -1;
      for (int i = 9; i < n; ++i) if (s.dslot[i] == s.rs[n + 3]) last = i;
      need(n, last >= 0 ? cl[last] + 1 : 0, n == 21 ? 0 : -1);
      if (n == 45) need(n, cl[29], cf[29]);
      if (n == 57) need(n, cl[41], cf[41]);
      if (s.fbar[n] >= 0) {
        int at = -1;
        for (int i = 9; i < n; ++i) if (s.xst[i] == s.fbar[n] - 1) at = i;
        if (at < 0) s.ok = false;
        for (int i = 9; i < n; ++i) if (s.xst[i] >= 0 && s.xst[i] < s.fbar[n] && i > at) at = i;
        need(n, -1, cf[at + 1]);
      }
    }
    if (n == s.ebar) need(n, cl[P2 + 135], cf[P2 + 135]);
    if (s.chk[n] >= 0) { int i = n; while (!s.poll[i]) --i; need(n, -1, cf[i + 1]); }
    if (s.cbar[n] >= 0) { int i = n; while (s.ld[i] != s.cbar[n]) --i; need(n, -1, cf[i + 1]); }
  }
  for (int u = 0; u < 9; ++u) {
    int own_rd = u == 0 ? P2 - 1 : P2 + 9 * (u - 1) + 4;
    for (int n = 0; n < NS; ++n) if (s.xrd[n] == u && n > own_rd) own_rd = n;
    bool b1 = false;
    for (int n = own_rd + 1; n <= P2 + lds_[u]; ++n) b1 = b1 || s.bar[n];
    int use = 81; while (s.u[use] != u) ++use;
    if (!b1 || rds[u] + 2 > use) s.ok = false;
  }
  return s;
}
constexpr Sch SCH = make_sch();
static_assert(SCH.ok, "schedule");

template <int I, int N, class F> DI void static_for(F&& f) {
  if constexpr (N - I == 1) f(std::integral_constant<int, I>{});
  else if constexpr (N - I > 1) { static_for<I, (I + N) / 2>(f); static_for<(I + N) / 2, N>(f); }
}
template <int N> DI void waitv() { asm volatile("s_waitcnt vmcnt(%0)" :: "n"(N) : "memory"); }
#define MFMA_DRAIN() do { asm volatile("s_nop 7\n\ts_nop 3" ::: "memory"); SB0(); } while (0)
typedef __attribute__((ext_vector_type(4))) int srd_t;
DI srd_t make_srd(const void* p, unsigned bytes) {
  const unsigned long long a = (unsigned long long)p;
  srd_t r = {(int)(unsigned)a, (int)((unsigned)(a >> 32) & 0xffffu), (int)bytes, 0x00020000};
  return r;
}
DI void bdma(srd_t srd, unsigned voff, unsigned soff, unsigned lds_dst) {
  asm volatile("s_mov_b32 m0, %3\n\ts_nop 0\n\tbuffer_load_dwordx4 %0, %1, %2 offen lds" :: "v"(voff), "s"(srd), "s"(soff), "s"(lds_dst) : "memory");
}
template <int LOFF, int SOFF> DI void bdma_i(srd_t srd, unsigned voff, unsigned sbase, unsigned lbase) {
  unsigned so;
  asm volatile("s_add_u32 m0, %4, %6\n\ts_add_u32 %0, %3, %5\n\tbuffer_load_dwordx4 %1, %2, %0 offen sc1 lds"
               : "=&s"(so) : "v"(voff), "s"(srd), "s"(sbase), "s"(lbase), "n"(SOFF), "n"(LOFF) : "memory", "scc");
}

template <int OFF> DI void bdma_sc1(srd_t srd, unsigned voff, unsigned lbase) {
  unsigned so;
  asm volatile("s_add_u32 m0, %3, %4\n\ts_mov_b32 %0, %4\n\tbuffer_load_dwordx4 %1, %2, %0 offen sc1 lds"
               : "=&s"(so) : "v"(voff), "s"(srd), "s"(lbase), "n"(OFF) : "memory", "scc");
}

template <int FJ>
DI void k3_body(const Ptrs& P, lds_char* lds, const int lane, const int mi, const int b, const int c, const int mb) {
  constexpr int fj = FJ;
  const unsigned lds_a = (unsigned)(uintptr_t)lds;
  const int mt = mb * 4 + mi, p = c & 1;
  const int g = lane >> 4;
  ST_DECL; ST();
  const unsigned voff = lane * 16;
  const srd_t sd = make_srd(P.wd, D_ * F_ * 2);
  const srd_t sw = make_srd(P.ws + OFF_WF + (size_t)c * WF_CH, (unsigned)WF_CH);
  const auto wsr = WS_RSRC(P);
  const int wid = fj * 4 + mi;
  const unsigned ring_w = lds_a + K3_RING + mi * 6144;
  const unsigned sw0 = mi * 6144;
  auto first_slot = [&](auto tc) __attribute__((always_inline)) {
    constexpr int t = decltype(tc)::value;
    static_for<3 * FJ, 3 * FJ + 3>([&](auto kc) __attribute__((always_inline)) {
      constexpr int k = decltype(kc)::value;
      bdma_i<t * 24576 + k * 1024, t * 24576 + k * 1024>(sw, voff, sw0, ring_w);
    });
  };
  {
    const int r = lane & 31, h = lane >> 5;
    const int hp = c;
    const int mt2 = mb * 4 + (c >> 2), cb = c & 3;
    u32x4 af[18], a4[5], xv0, xv1;
    float mean;
    const int k4 = FJ == 0 ? 5 * mi : 20 + 4 * mi;
    constexpr int n4 = FJ == 0 ? 5 : 4;
    const int row = mt2 * 32 + r, s0 = cb * 9 + 2 * (FJ == 0 ? mi : 4);
    auto prefetch2 = [&]() __attribute__((always_inline)) {
      const char* wo = P.ws + OFF_WOT + (size_t)cb * WOT_CB + h * 2336;
#pragma unroll
      for (int i = 0; i < 18; ++i) af[i] = *(const u32x4*)(wo + (18 * FJ + i) * 4672 + (32 * mi + r) * 16);
#pragma unroll
      for (int i = 0; i < n4; ++i) a4[i] = *(const u32x4*)(wo + (k4 + i) * 4672 + (128 + (r < 17 ? r : 17)) * 16);
      xv0 = *(const u32x4*)(P.x + (size_t)row * 1152 + s0 * 32 + h * 16);
      xv1 = *(const u32x4*)(P.x + (size_t)row * 1152 + (FJ == 0 ? s0 + 1 : s0) * 32 + h * 16);
      mean = ((const float*)(P.ws + OFF_MEANX))[row];
      SB0();
    };
    const int xj = c >> 1, j0 = (mb * 2 + p) * 10 + (mi & 1) * 5;
    const bool xtra = mi == 2 && mb < 2;
    const int stg5 = 32768 + (mi & 1) * 8192;
    if constexpr (FJ == 1) {
      const char* wsrc = P.ws + OFF_WQT + (size_t)hp * WQT_HP + mi * 1024;
      const unsigned wdst = lds_a + K1_W + mi * 1024;
#pragma unroll
      for (int i = 0; i < 14; ++i) dma16x1(wsrc + i * 4096, voff, wdst + i * 4096);
      WAITV(8); BAR();
#pragma unroll
      for (int i = 14; i < 20; ++i) dma16x1(wsrc + i * 4096, voff, wdst + i * 4096);
      WAITV(9); BAR();
#pragma unroll
      for (int i = 20; i < 26; ++i) dma16x1(wsrc + i * 4096, voff, wdst + i * 4096);
      WAITV(9); BAR();
#pragma unroll
      for (int i = 26; i < 31; ++i) dma16x1(wsrc + i * 4096, voff, wdst + i * 4096);
      WAITV(9); BAR();
      WAITV(0);
      if (mi >= 2) {
#pragma unroll
        for (int i = 0; i < 3; ++i) sub_load(P, xj, j0 + i, lane, lds_a + K1_ITEM + (mi & 1) * 16384 + i * 4096);
      } else {
        prefetch2();
      }
      BAR();
      ST();
      if (mi >= 2) {
        sub_load(P, xj, j0 + 3, lane, lds_a + K1_ITEM + (mi & 1) * 16384 + 3 * 4096);
        sub_load(P, xj, j0 + 4, lane, lds_a + stg5);
        sub_load(P, xj, xtra ? 320 + mb * 2 + p : j0 + 4, lane, lds_a + stg5 + 4096);
        WAITV(12);
#pragma unroll
        for (int i = 0; i < 3; ++i) sub_store(P, xj, j0 + i, lane, lds + K1_ITEM + (mi & 1) * 16384 + i * 4096);
      }
      if (mi < 2) {
        __builtin_amdgcn_s_setprio(3);
        const int q = (lane >> 2) & 3, op = 4 * (g & 1) + (lane & 3);
        const int vq = op >= 5 ? 19 : (mi ? 14 : 10) + op, kq = (mi ? 5 : 1) + op;
        lds_char* va = lds + (8 * (g >> 1) + q) * KVP + vq * 8;
        lds_char* ka = lds + (8 * (g >> 1) + q) * KVP + kq * 8;
        f32x16 kv;
#pragma unroll
        for (int i = 0; i < 16; ++i) kv[i] = 0.f;
#pragma unroll
        for (int ks = 0; ks < 8; ++ks) {
          u32x4 a = tr_frag(va + ks * 16 * KVP, va + ks * 16 * KVP + 4 * KVP);
          u32x4 bb = tr_frag(ka + ks * 16 * KVP, ka + ks * 16 * KVP + 4 * KVP);
          kv = MFMA32(as_frag(a), as_frag(bb), kv);
        }
        const int d = r - 2 * mi;
        const int dd = mi ? (d >= 2 ? d - 2 : 16 + d) : d;
        float* dst = (float*)(P.ws + OFF_KV) + (2 * hp + mi) * 400 + dd;
        const bool dok = d >= 0 && d < 18;
#pragma unroll
        for (int i = 0; i < 12; ++i) {
          const int rw = (i & 3) + 8 * (i >> 2) + 4 * h;
          const int e = rw == 20 ? 18 : rw - 2 * mi;
          if (dok && (rw == 20 || (e >= 0 && e < 18 && rw < 20))) atomicAdd(dst + e * 20, kv[i]);
        }
        WAITV(0);
      }
    } else {
      u32x4 xf[36];
      const char* xsrc = P.ws + OFF_XN + (size_t)mt * 36864 + lane * 16;
#pragma unroll
      for (int s = 0; s < 18; ++s) xf[s] = *(const u32x4*)(xsrc + s * 1024);
      f32x16 acc[3], accq;
#pragma unroll
      for (int i = 0; i < 16; ++i) { acc[0][i] = 0.f; acc[1][i] = 0.f; acc[2][i] = 0.f; accq[i] = 0.f; }
      lds_char* wa = lds + K1_W + (h * 76 + r) * 16;
      lds_char* wa2 = lds + K1_W + (h * 76 + 64 + (r < 12 ? r : 11)) * 16;
      u32x4 wf[2][3];
#pragma unroll
      for (int s = 0; s < 36; ++s) {
        if (s % 9 == 0) {
          BAR();
          if (s < 18) {
#pragma unroll
            for (int k = s + 18; k < s + 27; ++k) xf[k] = *(const u32x4*)(xsrc + k * 1024);
          }
          wf[s & 1][0] = *(lds_u32x4*)(wa + s * 2432); wf[s & 1][1] = *(lds_u32x4*)(wa + s * 2432 + 512); wf[s & 1][2] = *(lds_u32x4*)(wa2 + s * 2432);
        }
        if (s % 9 != 8) {
          wf[(s + 1) & 1][0] = *(lds_u32x4*)(wa + (s + 1) * 2432); wf[(s + 1) & 1][1] = *(lds_u32x4*)(wa + (s + 1) * 2432 + 512);
          wf[(s + 1) & 1][2] = *(lds_u32x4*)(wa2 + (s + 1) * 2432);
        }
        SB0();
#pragma unroll
        for (int t = 0; t < 3; ++t) acc[t] = MFMA32(as_frag(wf[s & 1][t]), as_frag(xf[s]), acc[t]);
        SB0();
      }
      ST();
      lds_char* kvrow = lds + (mi * 32 + r) * KVP;
      u32x2 qrem;
#pragma unroll
      for (int gi = 0; gi < 10; ++gi) {
        const int t = gi >> 2, gg = gi & 3;
        u32x2 o;
        if (gi < 5) { o[0] = pk2(phi(acc[t][4 * gg]), phi(acc[t][4 * gg + 1])); o[1] = pk2(phi(acc[t][4 * gg + 2]), phi(acc[t][4 * gg + 3])); }
        else { o[0] = pk2(acc[t][4 * gg], acc[t][4 * gg + 1]); o[1] = pk2(acc[t][4 * gg + 2], acc[t][4 * gg + 3]); }
        if (gi == 9 && h == 1) { o[0] = 0x3f803f80u; o[1] = 0x3f803f80u; }
        *(lds_u32x2*)(kvrow + (2 * gi + h) * 8) = o;
        if (gi == 0) qrem = o;
      }
      WAITL0();
      BAR();
      lds_char* wq = lds + K1_W + WQT_Q + lane * 16;
      u32x4 aq[2][4];
#pragma unroll
      for (int i = 0; i < 4; ++i) aq[0][i] = *(lds_u32x4*)(wq + i * 1024);
#pragma unroll
      for (int g3 = 0; g3 < 9; ++g3) {
        if (g3 < 8) {
#pragma unroll
          for (int i = 0; i < 4; ++i) aq[(g3 + 1) & 1][i] = *(lds_u32x4*)(wq + (4 * g3 + 4 + i) * 1024);
        }
        SB0();
#pragma unroll
        for (int i = 0; i < 4; ++i) accq = MFMA32(as_frag(aq[g3 & 1][i]), as_frag(xf[4 * g3 + i]), accq);
        SB0();
      }
      const int pq = (int)OFF_PQ + mt * 36864;
#pragma unroll
      for (int e = 0; e < 2; ++e) {
        u32x4 o;
#pragma unroll
        for (int k = 0; k < 4; ++k) o[k] = pk2(phi(accq[8 * e + 2 * k]), phi(accq[8 * e + 2 * k + 1]));
        WS_ST16(P, pq + (2 * hp + e) * 1024 + lane * 16, o, 16);
      }
      if (h == 0) WS_ST8(P, pq + (32 + (hp >> 2)) * 1024 + (r + 32 * ((hp >> 1) & 1)) * 16 + 8 * (hp & 1), qrem, 16);
      WAITV(0);
    }
    BAR();
    ST();
    if (FJ == 1 && mi == 0) {
      if (lane < 16) __hip_atomic_fetch_add((unsigned*)(P.ws + OFF_G1 + lane * 2048 + (b & 15) * 128), 1u, __ATOMIC_RELAXED, __HIP_MEMORY_SCOPE_AGENT);
      poll_counters(P.ws, (int)OFF_G1 + (b >> 4) * 2048 + (lane & 15) * 128, 16u);
    }
    if constexpr (FJ == 0) { __builtin_amdgcn_s_sleep(4); prefetch2(); }
    if (FJ == 1 && mi >= 2) {
      WAITV(12);
      sub_store(P, xj, j0 + 3, lane, lds + K1_ITEM + (mi & 1) * 16384 + 3 * 4096);
      sub_store(P, xj, j0 + 4, lane, lds + stg5);
      if (xtra) sub_store(P, xj, 320 + mb * 2 + p, lane, lds + stg5 + 4096);
      WAITL0();
      prefetch2();
    }
    BAR();
    ST();
    const int e = r < 16 ? r : (r == 16 ? 16 : (r == 20 ? 17 : 18));
    const bool rok = r <= 17 || r == 20 || r == 21;
    u32x4 k0[4], k1[4], qf[4]; u32x2 k2[4];
    dma16x3s(P.ws + OFF_KV + wid * 6400, voff, lds_a + wid * 6144);
    dma16x3s(P.ws + OFF_KV + wid * 6400 + 3072, voff, lds_a + wid * 6144 + 3072);
    const u32x4 ktail = __builtin_amdgcn_raw_buffer_load_b128(wsr, (lane & 15) * 16, (int)OFF_KV + wid * 6400 + 6144, 16);
#pragma unroll
    for (int i = 0; i < 4; ++i) qf[i] = __builtin_amdgcn_raw_buffer_load_b128(wsr, voff, (int)OFF_PQ + mt2 * 36864 + (4 * wid + i) * 1024, 16);
    const u32x4 qr = __builtin_amdgcn_raw_buffer_load_b128(wsr, voff, (int)OFF_PQ + mt2 * 36864 + (32 + (wid >> 1)) * 1024, 16);
    *(lds_u32x4*)(lds + K2_RED + wid * 256 + (lane & 15) * 16) = ktail;
    asm volatile("" ::: "memory");
    SB0();
#pragma unroll
    for (int i = 0; i < 4; ++i) {
      const int o = i * 1600 + e * 80;
      lds_char* kp0 = lds + (o + 16 * h < 6144 ? wid * 6144 + o + 16 * h : K2_RED + wid * 256 + o + 16 * h - 6144);
      lds_char* kp1 = lds + (o + 16 * h + 32 < 6144 ? wid * 6144 + o + 16 * h + 32 : K2_RED + wid * 256 + o + 16 * h + 32 - 6144);
      lds_char* kp2 = lds + (o + 64 < 6144 ? wid * 6144 + o + 64 : K2_RED + wid * 256 + o + 64 - 6144);
      k0[i] = *(lds_u32x4*)kp0; k1[i] = *(lds_u32x4*)kp1; k2[i] = *(lds_u32x2*)kp2;
    }
    float orem[4];
#define F_(x) __uint_as_float(x)
#pragma unroll
    for (int i = 0; i < 4; ++i) {
      u32x4 a1 = {pk2(F_(k0[i][0]), F_(k0[i][1])), pk2(F_(k0[i][2]), F_(k0[i][3])), pk2(F_(k1[i][0]), F_(k1[i][1])), pk2(F_(k1[i][2]), F_(k1[i][3]))};
      u32x4 a2 = {0u, 0u, 0u, 0u};
      a2[i] = (h == (mi & 1)) ? pk2(F_(k2[i][0]), F_(k2[i][1])) : 0u;
      if (!rok) { a1 = (u32x4){0u, 0u, 0u, 0u}; a2[i] = 0u; }
      f32x16 cc;
#pragma unroll
      for (int j = 0; j < 16; ++j) cc[j] = 0.f;
      cc = MFMA32(as_frag(a1), as_frag(qf[i]), cc);
      cc = MFMA32(as_frag(a2), as_frag(qr), cc);
      const float inv = __builtin_amdgcn_rcpf(cc[9] + 1e-6f);
      float o[9];
#pragma unroll
      for (int j = 0; j < 9; ++j) o[j] = cc[j] * inv;
      u32x4 of = {pk2(o[0], o[1]), pk2(o[2], o[3]), pk2(o[4], o[5]), pk2(o[6], o[7])};
      *(lds_u32x4*)(lds + K2_O + (4 * wid + i) * 1024 + lane * 16) = of;
      orem[i] = o[8];
    }
#undef F_
    {
      u32x2 o2 = {pk2(orem[0], orem[1]), pk2(orem[2], orem[3])};
      *(lds_u32x2*)(lds + K2_O + (32 + (wid >> 1)) * 1024 + lane * 16 + 8 * (mi & 1)) = o2;
    }
    if (FJ == 1 && mi >= 2) WAITV(0);
    WAITL0();
    BAR();
    ST();
    if (FJ == 1 && mi == 3 && lane == 0) __hip_atomic_fetch_add((unsigned*)(P.ws + OFF_GW + xj * 128), 1u, __ATOMIC_RELAXED, __HIP_MEMORY_SCOPE_AGENT);
    f32x16 acc, acc4;
#pragma unroll
    for (int j = 0; j < 16; ++j) { acc[j] = 0.f; acc4[j] = 0.f; }
    lds_char* ob = lds + K2_O + lane * 16;
#pragma unroll
    for (int i = 0; i < n4; ++i) acc4 = MFMA32(as_frag(a4[i]), as_frag(*(lds_u32x4*)(ob + (k4 + i) * 1024)), acc4);
#pragma unroll
    for (int i = 0; i < 18; ++i) acc = MFMA32(as_frag(af[i]), as_frag(*(lds_u32x4*)(ob + (18 * FJ + i) * 1024)), acc);
    {
      *(lds_float*)(lds + K2_MEAN + (wid * 64 + lane) * 4) = acc4[8] + acc4[9];
      lds_char* p4 = lds + K2_RED + 16384 + wid * 2048 + lane * 16;
      *(lds_f32x4*)(p4) = (f32x4){acc4[0], acc4[1], acc4[2], acc4[3]};
      *(lds_f32x4*)(p4 + 1024) = (f32x4){acc4[4], acc4[5], acc4[6], acc4[7]};
      if (FJ == 1) {
        lds_char* pr = lds + K2_RED + mi * 4096 + lane * 16;
#pragma unroll
        for (int k = 0; k < 4; ++k) *(lds_f32x4*)(pr + k * 1024) = (f32x4){acc[4 * k], acc[4 * k + 1], acc[4 * k + 2], acc[4 * k + 3]};
      }
    }
    WAITL0();
    BAR();
    if (FJ == 0 || mi == 0) {
#pragma unroll
      for (int w = 0; w < 8; ++w) mean += *(lds_float*)(lds + K2_MEAN + (w * 64 + r) * 4);
      if constexpr (FJ == 0) {
        lds_char* pr = lds + K2_RED + mi * 4096 + lane * 16;
#pragma unroll
        for (int k = 0; k < 4; ++k) {
          f32x4 v = *(lds_f32x4*)(pr + k * 1024);
#pragma unroll
          for (int j = 0; j < 4; ++j) acc[4 * k + j] += v[j];
        }
      } else {
#pragma unroll
        for (int j = 0; j < 8; ++j) acc[j] = 0.f;
#pragma unroll
        for (int w = 0; w < 8; ++w) {
          lds_char* p4 = lds + K2_RED + 16384 + w * 2048 + lane * 16;
          f32x4 v0 = *(lds_f32x4*)(p4), v1 = *(lds_f32x4*)(p4 + 1024);
#pragma unroll
          for (int j = 0; j < 4; ++j) { acc[j] += v0[j]; acc[4 + j] += v1[j]; }
        }
      }
      float ssq = 0.f;
#pragma unroll
      for (int i = 0; i < (FJ == 0 ? 2 : 1); ++i) {
        float v[8];
#pragma unroll
        for (int j = 0; j < 4; ++j) {
          auto sw2 = __builtin_amdgcn_permlane32_swap(__float_as_uint(acc[8 * i + j]), __float_as_uint(acc[8 * i + 4 + j]), false, false);
          v[j] = __uint_as_float(sw2[0]); v[4 + j] = __uint_as_float(sw2[1]);
        }
        const u32x4 xv = i ? xv1 : xv0;
        u32x4 o;
#pragma unroll
        for (int k = 0; k < 4; ++k) {
          const float a0 = v[2 * k] + bflo(xv[k]) - mean, a1 = v[2 * k + 1] + bfhi(xv[k]) - mean;
          ssq += a0 * a0 + a1 * a1;
          o[k] = pk2(a0, a1);
        }
        const int d8 = 2 * (s0 + i) + h;
        WS_ST16(P, OFF_HC + (((row >> 4) * 18 + (d8 >> 2)) * 64 + (row & 15) + 16 * (d8 & 3)) * 16, o, 16);
      }
      ssq += __shfl_xor(ssq, 32);
      if (h == 0) {
        WS_ST4(P, OFF_SSQ + ((mt2 * 20 + cb * 5 + wid) * 32 + r) * 4, __float_as_uint(ssq), 16);
        if (cb == 0 && wid == 0) WS_ST4(P, OFF_MEANH + row * 4, __float_as_uint(mean), 16);
      }
    }
    if (FJ == 1 && mi == 3) poll_counters(P.ws, (int)OFF_GW + xj * 128, 32u);
    WAITV(0); WAITL0();
    BAR();
    ST();
    if (FJ == 1 && mi == 1 && lane == 0) __hip_atomic_fetch_add((unsigned*)(P.ws + OFF_G3 + mb * 128), 1u, __ATOMIC_RELAXED, __HIP_MEMORY_SCOPE_AGENT);
    first_slot(std::integral_constant<int, 0>{});
    first_slot(std::integral_constant<int, 1>{});
    if (FJ == 1 && mi == 1) poll_counters(P.ws, (int)OFF_G3 + mb * 128, 16u);
    BAR();
    ST();
  }
  const char* hsrc = P.ws + OFF_HC + (size_t)mt * 36864;
  const char* hmine = hsrc + fj * 18432;
  constexpr int ST0 = K3_RING + 2 * 24576, ST1 = K3_HH, ST2 = K3_HH + 24576;
  dma16x3s(hmine, voff, lds_a + ST0 + (mi * 2 + fj) * 3072);
  dma16x3s(hmine + 3072, voff, lds_a + ST1 + (mi * 2 + fj) * 3072);
  dma16x3s(hmine + 6144, voff, lds_a + ST2 + (mi * 2 + fj) * 6144);
  dma16x3s(hmine + 9216, voff, lds_a + ST2 + (mi * 2 + fj) * 6144 + 3072);
  u32x4 hf[2][18];
  WAITV(9);
  BAR();
  lds_char* ring_rd = lds + K3_RING + fj * 1024 + lane * 16;
  lds_char* hh_rd = lds + K3_HH + mi * 18432 + lane * 16;
  u32x4 a[6];
  auto read_frag = [&](auto nc) __attribute__((always_inline)) {
    constexpr int n = decltype(nc)::value;
    if constexpr (n < P2) a[n % 6] = *(lds_u32x4*)(ring_rd + SCH.lo[n] * 1024);
    else if constexpr (n < NS) {
      const int q = (lane >> 2) & 3, xg = ((q >> 1) & 1) | ((g & 1) << 1);
      const int gr = SCH.db[n - P2] == 4 ? 2 * p + fj : 2 * fj + SCH.t01[n - P2];
      lds_char* r = lds + K3_RING + (4 * g + q) * 128 + ((gr ^ xg) * 32) + 8 * (lane & 3) + SCH.lo[n] * 1024;
      a[n % 6] = tr_frag(r, r + 2048);
    }
  };
#pragma unroll
  for (int i = 0; i < 6; ++i) hf[i & 1][i >> 1] = *(lds_u32x4*)(lds + ST0 + (mi * 2 + (i & 1)) * 3072 + (i >> 1) * 1024 + lane * 16);
  static_for<0, 3>(read_frag);
  WAITL0();
  BAR();
  first_slot(std::integral_constant<int, 2>{});
  ST();
  const int fl_mine = (int)OFF_FLAG + b * 128;
  const char* fl_other = P.ws + OFF_FLAG + (b ^ 8) * 128;
  const int xh_off = (int)OFF_XH + (b * 4 + mi) * 18432;
  const srd_t sx = make_srd(P.ws + OFF_XH + (size_t)((b ^ 8) * 4 + mi) * 18432, 18432);
  const int pbase = (int)OFF_XH + (b ^ 8) * 73728 + mi * 18432;
  lds_char* hh_wr = hh_rd + 8 * fj;
  float sigma[2], gsc[2];
  f32x4 acc[2][2][2][2];
  f32x4 oc[9][2];
  u32x4 ov[2], ho[2][2], hp[9][2], xl[2];
  u32x2 hpk;
  u32x2 ovs[2];
  unsigned pollv, pfs = 0;

  auto epi = [&](auto gc, auto ic) __attribute__((always_inline)) {
    constexpr int G = decltype(gc)::value, i = decltype(ic)::value;
    if constexpr (i < (G == 4 ? 46 : 92)) {
      constexpr int uu = i / 46, mh = (i % 46) / 23, r = i % 23, set = G & 1, U = 2 * G + uu;
      if constexpr (r < 20) {
        constexpr int e = (r / 10) * 2 + (r & 1), ph = (r % 10) >> 1;
        float tv = acc[set][uu][0][mh][e];
        if (ph == 0) tv = tv * gsc[mh];
        if (ph == 1) tv = __builtin_amdgcn_exp2f(tv);
        if (ph == 2) tv = __builtin_fmaf(tv, sigma[mh], sigma[mh]);
        if (ph == 3) tv = __builtin_amdgcn_rcpf(tv);
        if (ph == 4) tv = tv * acc[set][uu][1][mh][e];
        asm volatile("" : "+v"(tv));
        acc[set][uu][0][mh][e] = tv;
      } else if constexpr (r < 22) {
        unsigned t = pk2(acc[set][uu][0][mh][2 * (r - 20)], acc[set][uu][0][mh][2 * (r - 20) + 1]);
        asm volatile("" : "+v"(t));
        hpk[r - 20] = t;
      } else {
        *(lds_u32x2*)(hh_wr + mh * 9216 + U * 1024) = hpk;
      }
      SB0();
    }
  };
  auto opack = [&](const int pr, const int i) __attribute__((always_inline)) {
    const int mh = i >> 2, r = i & 3;
    if (pr == 4 && r >= 2) return;
    const int ti = pr == 4 ? 8 : 2 * pr + (r >> 1), e = pr == 4 ? 2 * r : 2 * (r & 1);
    unsigned t = pk2(oc[ti][mh][e], oc[ti][mh][e + 1]);
    asm volatile("" : "+v"(t));
    if (pr == 4) ovs[mh][r & 1] = t; else ov[mh][r] = t;
    SB0();
  };
  auto ostore = [&](const int x) __attribute__((always_inline)) {
    const int pr = x >> 1, mh = x & 1;
    if (pr < 4) st128(ov[mh], wsr, voff, pbase + (p + 2 * pr + fj + 9 * mh) * 1024);
    else __builtin_amdgcn_raw_buffer_store_b64(ovs[mh], wsr, voff, pbase + ((p ? 0 : 8) + 9 * mh) * 1024 + 8 * fj, 16);
    SB0();
  };

  static_for<0, NS>([&](auto nc) __attribute__((always_inline)) {
    constexpr int n = decltype(nc)::value;
    constexpr bool bar = SCH.bar[n];
    if constexpr (n == 9) WAITV(12);
    else if constexpr (FJ == 0 && SCH.wl[n] >= 0) waitv<SCH.wl[n]>();
    else if constexpr (FJ == 1 && SCH.wf[n] >= 0) waitv<SCH.wf[n]>();
    if constexpr (FJ == 1 && SCH.chk[n] >= 0) {
      asm volatile("" : "+v"(pollv));
      if (__builtin_amdgcn_readfirstlane(pollv) < (unsigned)SCH.chk[n]) {
#pragma unroll 1
        for (int spin = 0; spin < (1 << 22); ++spin) {
          asm volatile("global_load_dword %0, %1, %2 sc1\n\ts_waitcnt vmcnt(0)" : "=&v"(pollv) : "v"(0u), "s"(fl_other) : "memory");
          if (__builtin_amdgcn_readfirstlane(pollv) >= (unsigned)SCH.chk[n]) break;
          __builtin_amdgcn_s_sleep(2);
        }
      }
    }
    if constexpr (bar) { WAITL0(); BAR(); __builtin_amdgcn_s_setprio(3); }
    else if constexpr (n == SCH.ebar) BAR();
    const u32x4 af = a[n % 6];
    if constexpr (SCH.prio[n] >= 0) __builtin_amdgcn_s_setprio(SCH.prio[n]);
    if constexpr (n == 72 || n == P2) ST();
    SB0();
    static_for<0, 2>([&](auto mc) __attribute__((always_inline)) {
      constexpr int mh = decltype(mc)::value;
      if constexpr (n < P2) {
        constexpr int y = n % 72;
        constexpr int set = (n / 72) & 1, S = n >= PH ? y >> 1 : y >> 2, mat = n >= PH ? y & 1 : (y >> 1) & 1, uu = n >= PH ? 0 : y & 1;
        if constexpr (S == 0) asm volatile("v_mfma_f32_16x16x32_bf16 %0, %1, %2, 0" : "=&v"(acc[set][uu][mat][mh]) : "v"(af), "v"(hf[mh][S]));
        else asm volatile("v_mfma_f32_16x16x32_bf16 %0, %1, %2, %0" : "+v"(acc[set][uu][mat][mh]) : "v"(af), "v"(hf[mh][S]));
      } else {
        constexpr int s2 = n - P2, U = SCH.u[s2], ti = SCH.db[s2] == 4 ? 8 : 2 * SCH.db[s2] + SCH.t01[s2];
        if constexpr (s2 < 9) asm volatile("v_mfma_f32_16x16x32_bf16 %0, %1, %2, 0" : "=&v"(oc[ti][mh]) : "v"(af), "v"(ho[0][mh]));
        else if constexpr (SCH.own[s2]) asm volatile("v_mfma_f32_16x16x32_bf16 %0, %1, %2, %0" : "+v"(oc[ti][mh]) : "v"(af), "v"(ho[U & 1][mh]));
        else asm volatile("v_mfma_f32_16x16x32_bf16 %0, %1, %2, %0" : "+v"(oc[ti][mh]) : "v"(af), "v"(hp[U][mh]));
      }
      SB0();
      if constexpr (mh == 0) { read_frag(std::integral_constant<int, n + 3>{}); SB0(); }
      if constexpr (SCH.eg[n] >= 0) {
        constexpr int f = SCH.e0[n] + (mh ? SCH.ne0[n] : 0), cnt = mh ? SCH.ne1[n] : SCH.ne0[n];
        static_for<f, f + cnt>([&](auto ic) __attribute__((always_inline)) { epi(std::integral_constant<int, SCH.eg[n]>{}, ic); });
      }
      if constexpr (SCH.og[n] >= 0) opack(SCH.og[n], SCH.o0[n] + mh);
    });
    if constexpr (FJ == 0 && SCH.dslot[n] >= 0) {
      constexpr int t = SCH.dslot[n], k = SCH.dk[n];
      if constexpr (t >= 27) {
        constexpr int sb = (t - 27) * 6 + k, ow = SCH.sown[sb], U = SCH.su[sb], d = SCH.sdb[sb];
        const unsigned vd = (lane >> 3) * (D_ * 2) + ((((lane >> 1) & 3) ^ ((lane >> 4) & 3)) * 32) + (lane & 1) * 16;
        bdma_i<(t % 3) * 24576 + k * 4096, 32 * U * (D_ * 2) + (d == 4 ? 512 : 128 * d)>(
            sd, vd, (288 * (ow ? c : c ^ 1) + 8 * mi) * (D_ * 2) + (d == 4 ? 0 : 640 * p), lds_a + K3_RING + mi * 1024);
      } else if constexpr (k % 3 == 0) bdma_i<(t % 3) * 24576 + k * 1024, t * 24576 + k * 1024>(sw, voff, sw0, ring_w);
      else asm volatile("buffer_load_dwordx4 %0, %1, %2 offen offset:%3 sc1 lds" :: "v"(voff), "s"(sw), "s"(sw0 + t * 24576 + (k / 3) * 3072), "n"((k % 3) * 1024) : "memory");
      SB0();
    }
    if constexpr (SCH.ost[n] >= 0) ostore(SCH.ost[n]);
    if constexpr (n == 5) { dma16x3s(P.ws + OFF_SSQ + mt * 2560, voff, lds_a + K3_SSQ + mi * 3072); SB0(); }
    if constexpr (n == 28) { dma16x3s(hmine + 12288, voff, lds_a + ST1 + (mi * 2 + fj) * 3072); SB0(); }
    if constexpr (n == 40) { dma16x3s(hmine + 15360, voff, lds_a + ST2 + (mi * 2 + fj) * 3072); SB0(); }
    if constexpr (n == 9 || n == 45 || n == 57) {
      constexpr int s0 = n == 9 ? 3 : (n == 45 ? 12 : 15), stg = n == 57 ? ST2 : ST1;
#pragma unroll
      for (int i = 0; i < 6; ++i) hf[i & 1][s0 + (i >> 1)] = *(lds_u32x4*)(lds + stg + (mi * 2 + (i & 1)) * 3072 + (i >> 1) * 1024 + lane * 16);
      SB0();
    }
    if constexpr (n == 21) {
#pragma unroll
      for (int i = 0; i < 12; ++i) hf[i & 1][6 + (i >> 1)] = *(lds_u32x4*)(lds + ST2 + (mi * 2 + (i & 1)) * 6144 + (i >> 1) * 1024 + lane * 16);
      SB0();
    }
    if constexpr (n == 50) {
#pragma unroll
      for (int mh = 0; mh < 2; ++mh) {
        float ssq = 0.f;
#pragma unroll
        for (int i = 0; i < 20; ++i) ssq += *(lds_float*)(lds + K3_SSQ + mi * 3072 + (i * 32 + mh * 16 + (lane & 15)) * 4);
        sigma[mh] = sqrtf(ssq * (1.f / D_) + LN_EPS);
        gsc[mh] = -1.4426950408889634f / sigma[mh];
        asm volatile("" : "+v"(sigma[mh]), "+v"(gsc[mh]));
      }
      SB0();
    }
    if constexpr (n == P2 - 1) { ho[0][0] = *(lds_u32x4*)(hh_rd); ho[0][1] = *(lds_u32x4*)(hh_rd + 9216); SB0(); }
    if constexpr (n >= P2 && n < P2 + 72 && (n - P2) % 9 == 4) {
      constexpr int U = (n - P2) / 9 + 1;
      ho[U & 1][0] = *(lds_u32x4*)(hh_rd + U * 1024); ho[U & 1][1] = *(lds_u32x4*)(hh_rd + 9216 + U * 1024);
      SB0();
    }
    if constexpr (FJ == 1) {
      if constexpr (SCH.xst[n] >= 0) {
        st128(xl[0], wsr, voff, xh_off + SCH.xst[n] * 1024);
        st128(xl[1], wsr, voff, xh_off + 9216 + SCH.xst[n] * 1024);
        SB0();
      }
      if constexpr (SCH.flag[n] >= 0) { __builtin_amdgcn_raw_buffer_store_b32((unsigned)SCH.flag[n], wsr, 0, fl_mine, 16); SB0(); }
      if constexpr (SCH.poll[n]) { asm volatile("global_load_dword %0, %1, %2 sc1" : "=&v"(pollv) : "v"(0u), "s"(fl_other) : "memory"); SB0(); }
      if constexpr (SCH.ld[n] >= 0) {
        constexpr int u = SCH.ld[n];
        bdma_sc1<u * 1024>(sx, voff, lds_a + K3_HH + mi * 18432);
        bdma_sc1<9216 + u * 1024>(sx, voff, lds_a + K3_HH + mi * 18432);
        SB0();
      }
      if constexpr (n == SCH.ebar) {
        if (mi == 0 && lane == 0) __hip_atomic_fetch_add((unsigned*)(P.ws + OFF_CNT + (mb * 2 + p) * 128), 1u, __ATOMIC_RELAXED, __HIP_MEMORY_SCOPE_AGENT);
        SB0();
      }
      if constexpr (SCH.pf[n]) {
        const int l41 = lane < 41 ? lane : 0, o = (((mb & 7) * 2 + p) * 4 + mi) * 10368 + l41 * 256;
        const char* pa = P.wd + (size_t)(c >> 1) * 663552 + (o < 663552 - 256 ? o : 663552 - 256);
        asm volatile("global_load_dword %0, %1, off\n\tglobal_load_dword %0, %1, off offset:128" : "+v"(pfs) : "v"(pa) : "memory");
        SB0();
      }
      if constexpr (SCH.flag[n] == 6) asm volatile("" :: "v"(pfs));
      if constexpr (SCH.xrd[n] >= 0) {
        xl[0] = *(lds_u32x4*)(hh_rd + SCH.xrd[n] * 1024); xl[1] = *(lds_u32x4*)(hh_rd + 9216 + SCH.xrd[n] * 1024);
        SB0();
      }
    }
    if constexpr (SCH.rd[n] >= 0) {
      constexpr int u = SCH.rd[n];
      hp[u][0] = *(lds_u32x4*)(hh_rd + u * 1024); hp[u][1] = *(lds_u32x4*)(hh_rd + 9216 + u * 1024);
      SB0();
    }
  });
  ST();
  MFMA_DRAIN();
#pragma unroll
  for (int i = 0; i < 8; ++i) opack(3, i);
  ostore(6);
  ostore(7);
  const int j = c >> 1, t16 = mb * 8 + j, f0 = t16 * 18 + 9 * p;
  const int qr = p + wid, qs = p ? 0 : 8;
  const int l16 = 16 * mi + (lane & 15);
  const int row = t16 * 16 + (lane & 15);
  const int hb = ((lane & 15) + 16 * (g >> 1)) * 16 + 8 * (g & 1), hbs = ((lane & 15) + 16 * (mi >> 1)) * 16 + 8 * (mi & 1);
  const u32x2 h0 = __builtin_amdgcn_raw_buffer_load_b64(wsr, hb, (int)OFF_HC + (f0 + qr) * 1024, 16);
  const u32x2 h1 = __builtin_amdgcn_raw_buffer_load_b64(wsr, hb + 512, (int)OFF_HC + (f0 + qr) * 1024, 16);
  u32x2 hs0, hs1;
  if constexpr (FJ == 0) {
    hs0 = __builtin_amdgcn_raw_buffer_load_b64(wsr, hbs, (int)OFF_HC + (f0 + qs) * 1024, 16);
    hs1 = __builtin_amdgcn_raw_buffer_load_b64(wsr, hbs + 512, (int)OFF_HC + (f0 + qs) * 1024, 16);
  }
  const float mean = __uint_as_float(__builtin_amdgcn_raw_buffer_load_b32(wsr, row * 4, (int)OFF_MEANH, 16));
  const int cnt_off = (int)OFF_CNT + (mb * 2 + p) * 128;
  if constexpr (FJ == 0) poll_counters(P.ws, cnt_off, 8u);
  else WAITV(0);
  BAR();
  ST();
  if constexpr (FJ == 1) {
    if (mi == 0 && lane == 0) __hip_atomic_fetch_add((unsigned*)(P.ws + cnt_off + 64), 1u, __ATOMIC_RELAXED, __HIP_MEMORY_SCOPE_AGENT);
    poll_counters(P.ws, cnt_off + 64, 8u);
  }
  ST();
  auto area = [&](const int k) { const int ck = 2 * k + (p ^ 1); return (int)OFF_XH + (((mb & 7) * 4 + (ck & 3)) * 8 + (ck >> 2) * 2 + (mb >> 3)) * 73728 + j * 9216; };
  u32x4 pv[8], ps[2];
#pragma unroll
  for (int k = 0; k < 8; ++k) pv[k] = __builtin_amdgcn_raw_buffer_load_b128(wsr, voff, area(k) + qr * 1024, 16);
  if constexpr (FJ == 0) {
#pragma unroll
    for (int k = 0; k < 2; ++k) ps[k] = __builtin_amdgcn_raw_buffer_load_b128(wsr, area(4 * k + g) + l16 * 16, qs * 1024, 16);
  }
  float o[8];
  o[0] = bflo(h0[0]); o[1] = bfhi(h0[0]); o[2] = bflo(h0[1]); o[3] = bfhi(h0[1]);
  o[4] = bflo(h1[0]); o[5] = bfhi(h1[0]); o[6] = bflo(h1[1]); o[7] = bfhi(h1[1]);
#pragma unroll
  for (int k = 0; k < 8; ++k) o[k] += mean;
#pragma unroll
  for (int r = 0; r < 8; ++r)
#pragma unroll
    for (int k = 0; k < 4; ++k) { o[2 * k] += bflo(pv[r][k]); o[2 * k + 1] += bfhi(pv[r][k]); }
  {
    char* d = P.out + (size_t)row * 1152 + (9 * p + qr) * 64 + g * 8;
    *(u32x2*)d = (u32x2){pk2(o[0], o[1]), pk2(o[2], o[3])};
    *(u32x2*)(d + 32) = (u32x2){pk2(o[4], o[5]), pk2(o[6], o[7])};
  }
  if constexpr (FJ == 0) {
    float os[8];
#pragma unroll
    for (int k = 0; k < 4; ++k) { os[2 * k] = bflo(ps[0][k]) + bflo(ps[1][k]); os[2 * k + 1] = bfhi(ps[0][k]) + bfhi(ps[1][k]); }
#pragma unroll
    for (int k = 0; k < 8; ++k) { os[k] += __shfl_xor(os[k], 16); os[k] += __shfl_xor(os[k], 32); }
    if (lane < 16) {
      char* d = P.out + (size_t)row * 1152 + (9 * p + qs) * 64 + mi * 8;
      *(u32x2*)d = (u32x2){pk2(os[0] + bflo(hs0[0]) + mean, os[1] + bfhi(hs0[0]) + mean), pk2(os[2] + bflo(hs0[1]) + mean, os[3] + bfhi(hs0[1]) + mean)};
      *(u32x2*)(d + 32) = (u32x2){pk2(os[4] + bflo(hs1[0]) + mean, os[5] + bfhi(hs1[0]) + mean), pk2(os[6] + bflo(hs1[1]) + mean, os[7] + bfhi(hs1[1]) + mean)};
    }
  }
}

__global__ __launch_bounds__(512, 2) void k3_ffn(Ptrs P) {
  extern __shared__ __attribute__((aligned(16))) char smem[];
  const int lane = threadIdx.x & 63, wid = __builtin_amdgcn_readfirstlane(threadIdx.x >> 6);
  const int b = blockIdx.x;
  const int c = ((b & 7) >> 1) * 4 + ((b >> 3) & 3), mb = (b & 1) * 8 + (b >> 5);
  if (wid < 4) k3_body<0>(P, (lds_char*)smem, lane, wid, b, c, mb);
  else k3_body<1>(P, (lds_char*)smem, lane, wid - 4, b, c, mb);
}

constexpr int K0_IMG = 4608;
constexpr int K0_RED = 3 * K0_IMG;
constexpr int K0_LDS = K0_RED + 9 * 8 * 8;

DI float dpp_sum8(float v) {
  v += __shfl_xor(v, 1); v += __shfl_xor(v, 2); v += __shfl_xor(v, 4);
  return v;
}

__global__ __launch_bounds__(768) void k0_prep(Ptrs P) {
  extern __shared__ __attribute__((aligned(16))) char smem[];
  lds_char* lds = (lds_char*)smem;
  const int lane = threadIdx.x & 63, wid = __builtin_amdgcn_readfirstlane(threadIdx.x >> 6);
  const int b = blockIdx.x;
  ST_DECL; ST();
  if (wid < 9) {
    const int row = b * 8 + (lane >> 3), i = lane & 7;
    const u32x4 v = *(const u32x4*)(P.x + (size_t)row * 1152 + wid * 128 + i * 16);
    float e[8];
#pragma unroll
    for (int k = 0; k < 4; ++k) { e[2 * k] = bflo(v[k]); e[2 * k + 1] = bfhi(v[k]); }
    const float sum = dpp_sum8(((e[0] + e[1]) + (e[2] + e[3])) + ((e[4] + e[5]) + (e[6] + e[7])));
    const float lm = sum * (1.f / 64.f);
    float m2 = 0.f;
#pragma unroll
    for (int k = 0; k < 8; ++k) { const float d = e[k] - lm; m2 = __builtin_fmaf(d, d, m2); }
    m2 = dpp_sum8(m2);
    if (i == 0) *(lds_f32x2*)(lds + K0_RED + (wid * 8 + (lane >> 3)) * 8) = (f32x2){lm, m2};
    WAITL0();
    BAR();
    ST();
    f32x2 pr[9];
#pragma unroll
    for (int w = 0; w < 9; ++w) pr[w] = *(lds_f32x2*)(lds + K0_RED + (w * 8 + (lane >> 3)) * 8);
    float mean = 0.f;
#pragma unroll
    for (int w = 0; w < 9; ++w) mean += pr[w][0];
    mean *= (1.f / 9.f);
    float tot = 0.f;
#pragma unroll
    for (int w = 0; w < 9; ++w) { const float d = pr[w][0] - mean; tot += __builtin_fmaf(64.f * d, d, pr[w][1]); }
    const float rstd = rsqrtf(tot * (1.f / D_) + LN_EPS);
    u32x4 o;
#pragma unroll
    for (int k = 0; k < 4; ++k) o[k] = pk2((e[2 * k] - mean) * rstd, (e[2 * k + 1] - mean) * rstd);
    WS_ST16(P, (int)OFF_XN + ((row >> 5) * 36 * 64 + (row & 31) + 32 * (i & 1) + (4 * wid + (i >> 1)) * 64) * 16, o, ST_AUX);
    if (wid == 0 && i == 0) WS_ST4(P, OFF_MEANX + row * 4, __float_as_uint(mean), ST_AUX);
    return;
  }
  const int id = (wid - 9) * 256 + b;
  lds_char* img = lds + (wid - 9) * K0_IMG;
  const unsigned img_a = (unsigned)(uintptr_t)img;
  const int g = lane >> 4, q = (lane >> 2) & 3, p = lane & 3;
  unsigned pf_sink = 0;
  if (wid == 10 && b < 96) {
    unsigned long long pc, k1a;
    asm volatile("s_getpc_b64 %0" : "=s"(pc));
    asm volatile("s_getpc_b64 s[40:41]\n\ts_add_u32 s40, s40, _Z6k3_ffn4Ptrs@rel32@lo+4\n\ts_addc_u32 s41, s41, _Z6k3_ffn4Ptrs@rel32@hi+12\n\t"
                 "s_mov_b64 %0, s[40:41]" : "=s"(k1a) :: "s40", "s41", "scc");
    const char* pp = (const char*)k1a + ((b >> 3) * 64 + lane) * 128;
    pp = pp < (const char*)pc ? pp : (const char*)k1a;
    asm volatile("global_load_dword %0, %1, off" : "=&v"(pf_sink) : "v"(pp) : "memory");
  }
  if (id < 576) {
    const int s = id >> 4, h = (id >> 3) & 1, hp0 = (id & 7) * 2;
    const char* src = P.wqkv + (size_t)(16 * s + 8 * h) * 3456 + hp0 * 72;
#pragma unroll
    for (int it = 0; it < 4; ++it) {
      int e = it * 64 + lane; e = e < 216 ? e : 215;
      const int sgm = e / 9, ch = e - sgm * 9;
      dma16x1(src, sgm * 1152 + ch * 16, img_a + it * 1024);
    }
    BAR();
    ST();
    WAITV(0);
    asm volatile("" :: "v"(pf_sink));
    ST();
#pragma unroll
    for (int it = 0; it < 4; ++it) {
      int Q = 16 * it + 4 * g + p; Q = Q < 54 ? Q : 53;
      const int hl = Q >= 27 ? 1 : 0, sg = Q - 27 * hl;
      const int cb = sg < 4 ? 8 * sg : (sg < 8 ? 8 * sg + 8 : (sg == 8 ? 32 : (sg < 18 ? 8 * sg + 72 : 8 * sg + 144)));
      lds_char* a = img + q * 432 + cb + 72 * hl;
      u32x4 v = tr_frag(a, a + 4 * 432);
      const int o = it * 64 + lane, ol = o >= 108 ? 1 : 0, slot = o - 108 * ol;
      const int off = slot < 32 ? WQT_Q + s * 1024 + (h * 32 + slot) * 16 : s * 2432 + (h * 76 + slot - 32) * 16;
      if (o < 216) WS_ST16(P, OFF_WQT + (hp0 + ol) * (int)WQT_HP + off, v, ST_AUX);
    }
  } else if (id < 720) {
    const int j = id - 576, ks = j >> 2, cb = j & 3;
#pragma unroll
    for (int it = 0; it < 5; ++it) {
      int e = it * 64 + lane; e = e < 288 ? e : 287;
      const int kk = e / 18, ch = e - kk * 18, h = kk >> 3, jj = kk & 7;
      const int row = ks < 32 ? 18 * ks + (jj & 3) + 8 * (jj >> 2) + 4 * h : 18 * (8 * (ks - 32) + jj) + 16 + h;
      if (it < 4 || lane < 32) dma16x1(P.wo + cb * 288, row * 1152 + ch * 16, img_a + it * 1024);
    }
    BAR();
    WAITV(0);
#pragma unroll
    for (int it = 0; it < 5; ++it) {
      int G16 = 4 * it + g; G16 = G16 < 18 ? G16 : 17;
      const int h = G16 >= 9 ? 1 : 0, sg = G16 - 9 * h;
      lds_char* a = img + (8 * h + q) * 288 + (16 * sg + 4 * p) * 2;
      u32x4 v = tr_frag(a, a + 4 * 288);
      if (it < 4 || lane < 32) WS_ST16(P, OFF_WOT + cb * (int)WOT_CB + ks * 4672 + (h * 146 + 16 * sg + (lane & 15)) * 16, v, ST_AUX);
    }
  } else if (id < 756) {
    const int row = (id - 720) * 16 + (lane >> 2), i = lane & 3;
    const char* src = P.wo + (size_t)row * 1152 + i * 16;
    u32x4 v[18];
#pragma unroll
    for (int j = 0; j < 18; ++j) v[j] = *(const u32x4*)(src + j * 64);
    BAR();
    float s4[4] = {0.f, 0.f, 0.f, 0.f};
    const bf16x2 one2 = __builtin_bit_cast(bf16x2, 0x3f803f80u);
#pragma unroll
    for (int j = 0; j < 18; ++j)
#pragma unroll
      for (int k = 0; k < 4; ++k) { const unsigned t = v[j][k]; s4[k] = __builtin_amdgcn_fdot2_f32_bf16(__builtin_bit_cast(bf16x2, t), one2, s4[k], false); }
    float sum = (s4[0] + s4[1]) + (s4[2] + s4[3]);
    sum += __shfl_xor(sum, 1); sum += __shfl_xor(sum, 2);
    const float mean = sum * (1.f / D_);
    const int hd = row / 18, e = row % 18;
    const int ks = e < 16 ? hd : 32 + (hd >> 3), jj = e < 16 ? (e & 3) + 4 * (e >> 3) : (hd & 7), hh = e < 16 ? (e >> 2) & 1 : e - 16;
    const unsigned hi = pk2(mean, 0.f) & 0xffffu;
    const unsigned lo = pk2(mean - bflo(hi), 0.f) & 0xffffu;
    unsigned short* d = (unsigned short*)(P.ws + OFF_WOT + (size_t)i * WOT_CB + ks * 4672 + (hh * 146 + 144) * 16 + jj * 2);
    d[0] = (unsigned short)hi; d[8] = (unsigned short)lo;
  } else {
    const u32x4 z = {0u, 0u, 0u, 0u};
    for (int k = id - 756; k < 50; k += 12) WS_ST16(P, OFF_KV + k * 1024 + lane * 16, z, ST_AUX);
    for (int k = id - 756; k < 71; k += 12) WS_ST16(P, OFF_FLAG + k * 1024 + lane * 16, z, ST_AUX);
    BAR();
  }
}

extern "C" void kernel_launch(void* const* d_in, const int* in_sizes, int n_in, void* d_out, int out_size, void* d_ws,
                              size_t ws_size, hipStream_t stream) {
  Ptrs P;
  P.x = (const char*)d_in[0]; P.wqkv = (const char*)d_in[1]; P.wo = (const char*)d_in[2];
  P.wg = (const char*)d_in[4]; P.wu = (const char*)d_in[5]; P.wd = (const char*)d_in[6];
  P.ws = (char*)d_ws; P.out = (char*)d_out;
  (void)hipFuncSetAttribute((const void*)k3_ffn, hipFuncAttributeMaxDynamicSharedMemorySize, K3_LDS);
  k0_prep<<<256, 768, K0_LDS, stream>>>(P);
  k3_ffn<<<256, 512, K3_LDS, stream>>>(P);
}
```

```cpp
#include <hip/hip_runtime.h>
#include <hip/hip_bf16.h>
#include <stdint.h>
#include <type_traits>

typedef __attribute__((ext_vector_type(8))) short bf16x8;
typedef __attribute__((ext_vector_type(4))) short s16x4;
typedef __attribute__((ext_vector_type(16))) float f32x16;
typedef __attribute__((ext_vector_type(4))) float f32x4;
typedef __attribute__((ext_vector_type(2))) float f32x2;
typedef __attribute__((ext_vector_type(2))) __bf16 bf16x2;
typedef __attribute__((ext_vector_type(4))) unsigned u32x4;
typedef __attribute__((ext_vector_type(2))) unsigned u32x2;
typedef __attribute__((address_space(3))) char lds_char;
typedef __attribute__((address_space(3))) s16x4 lds_s16x4;
typedef __attribute__((address_space(3))) u32x4 lds_u32x4;
typedef __attribute__((address_space(3))) u32x2 lds_u32x2;
typedef __attribute__((address_space(3))) f32x4 lds_f32x4;
typedef __attribute__((address_space(3))) float lds_float;
typedef __attribute__((address_space(3))) f32x2 lds_f32x2;
typedef __attribute__((address_space(3))) unsigned lds_uint;

#define DI __device__ __forceinline__
#define MFMA32(a, b, c) __builtin_amdgcn_mfma_f32_32x32x16_bf16((a), (b), (c), 0, 0, 0)
#define WAITV(n) asm volatile("s_waitcnt vmcnt(" #n ")" ::: "memory")
#define WAITL0() asm volatile("s_waitcnt lgkmcnt(0)" ::: "memory")
#define BAR() asm volatile("s_barrier" ::: "memory")
#define SB0() __builtin_amdgcn_sched_barrier(0)

constexpr int S_ = 2048, D_ = 576, F_ = 4608;
constexpr float LN_EPS = 1e-5f;

constexpr size_t FLB = (size_t)S_ * D_ * 2;
constexpr size_t WQT_HP = 36 * 3456;
constexpr int WQT_Q = 36 * 2432;
constexpr size_t WOT_CB = 36 * 4672;
constexpr size_t OFF_XN = 0;
constexpr size_t OFF_PQ = OFF_XN + FLB;
constexpr size_t OFF_HC = OFF_PQ + FLB;
constexpr size_t OFF_MEANX = OFF_HC + FLB;
constexpr size_t OFF_MEANH = OFF_MEANX + 8192;
constexpr size_t OFF_SSQ = OFF_MEANH + 8192;
constexpr size_t OFF_KV = OFF_SSQ + 20 * 8192 + 4096;
constexpr size_t OFF_WQT = OFF_KV + 53248;
constexpr size_t OFF_WOT = OFF_WQT + 16 * WQT_HP + 16384;
constexpr size_t OFF_XH = OFF_WOT + 4 * WOT_CB + 4096;
constexpr size_t OFF_FLAG = OFF_XH + 256 * 73728;
constexpr size_t OFF_CNT = OFF_FLAG + 256 * 128;
constexpr size_t OFF_G1 = OFF_CNT + 32 * 128;
constexpr size_t OFF_G3 = OFF_G1 + 256 * 128;
constexpr size_t WS_END = OFF_G3 + 16 * 128;
static_assert(OFF_XH % 1024 == 0 && (WS_END - OFF_FLAG) == 70 * 1024, "workspace");

#define ST_DECL
#define ST()
#define ST_OUT(k, slot)
DI unsigned pk2(float a, float b) {
  f32x2 v = {a, b};
  bf16x2 r = __builtin_convertvector(v, bf16x2);
  return __builtin_bit_cast(unsigned, r);
}
DI float bflo(unsigned u) { return __uint_as_float(u << 16); }
DI float bfhi(unsigned u) { return __uint_as_float(u & 0xffff0000u); }
DI float phi(float t) {
  const float e = __builtin_amdgcn_fmed3f(__builtin_amdgcn_exp2f(t * 1.4426950408889634f), 0.f, 1.f);
  return (__builtin_amdgcn_fmed3f(t, 0.f, __builtin_inff()) + 1e-6f) + e;
}
DI bf16x8 as_frag(u32x4 v) { return __builtin_bit_cast(bf16x8, v); }

DI void dma16x3s(const void* sbase, unsigned voff, unsigned lds_dst) {
  unsigned keep;
  asm volatile("s_mov_b32 %0, m0\n\ts_mov_b32 m0, %3\n\ts_nop 0\n\t"
               "global_load_lds_dwordx4 %1, %2 sc1\n\t"
               "global_load_lds_dwordx4 %1, %2 offset:1024 sc1\n\t"
               "global_load_lds_dwordx4 %1, %2 offset:2048 sc1\n\t"
               "s_mov_b32 m0, %0"
               : "=&s"(keep) : "v"(voff), "s"(sbase), "s"(lds_dst) : "memory");
}
DI void dma16x1(const void* sbase, unsigned voff, unsigned lds_dst) {
  unsigned keep;
  asm volatile("s_mov_b32 %0, m0\n\ts_mov_b32 m0, %3\n\ts_nop 0\n\t"
               "global_load_lds_dwordx4 %1, %2\n\t"
               "s_mov_b32 m0, %0"
               : "=&s"(keep) : "v"(voff), "s"(sbase), "s"(lds_dst) : "memory");
}
DI u32x4 tr_frag(lds_char* p0, lds_char* p1) {
  s16x4 lo = __builtin_amdgcn_ds_read_tr16_b64_v4i16((lds_s16x4*)p0);
  s16x4 hi = __builtin_amdgcn_ds_read_tr16_b64_v4i16((lds_s16x4*)p1);
  u32x2 a = __builtin_bit_cast(u32x2, lo), b = __builtin_bit_cast(u32x2, hi);
  u32x4 r = {a[0], a[1], b[0], b[1]};
  return r;
}

#define ST_AUX 16
#define WS_RSRC(P) __builtin_amdgcn_make_buffer_rsrc((P).ws, 0, (int)WS_END, 0x00020000)
#define WS_ST16(P, off, v, aux) __builtin_amdgcn_raw_buffer_store_b128((v), WS_RSRC(P), (int)(off), 0, (aux))
#define WS_ST8(P, off, v, aux) __builtin_amdgcn_raw_buffer_store_b64((v), WS_RSRC(P), (int)(off), 0, (aux))
#define WS_ST4(P, off, v, aux) __builtin_amdgcn_raw_buffer_store_b32((v), WS_RSRC(P), (int)(off), 0, (aux))
template <class R> DI void st128(u32x4 v, R rsrc, int voff, int soff) {
  __builtin_amdgcn_raw_buffer_store_b128(v, rsrc, voff, soff, 16);
  asm volatile("s_nop 1" :: "v"(v));
}
struct Ptrs {
  const char* x; const char* wqkv; const char* wo; const char* wg; const char* wu; const char* wd;
  char* ws; char* out;
};

constexpr int G1R = 16;

constexpr int K1_W = 0;
constexpr int KVP = 192;
constexpr int K2_O = 49152;
constexpr int K2_RED = K2_O + 36864;
constexpr int K2_MEAN = K2_RED + 32768;

DI void poll_counters(const char* base, const int off, const unsigned need) {
#pragma unroll 1
  for (int spin = 0; spin < (1 << 22); ++spin) {
    unsigned v;
    asm volatile("global_load_dword %0, %1, %2 sc1\n\ts_waitcnt vmcnt(0)" : "=&v"(v) : "v"(off), "s"(base) : "memory");
    if (__builtin_amdgcn_ballot_w64(v < need) == 0ull) break;
    __builtin_amdgcn_s_sleep(2);
  }
}

constexpr int K3_RING = 0;
constexpr int K3_HH = 73728;
constexpr int K3_SSQ = K3_HH + 73728;
constexpr int K3_LDS = K3_SSQ + 4 * 3072;
constexpr int EBAR = 156, XOFF = 1;
constexpr int NS = 486, PH = 288, P2 = 324;

struct Sch {
  signed char own[162] = {}, u[162] = {}, db[162] = {}, t01[162] = {};
  short rs[NS + 4] = {}, lo[NS + 4] = {};
  signed char sown[90] = {}, su[90] = {}, sdb[90] = {}, pf[NS] = {};
  bool bar[NS] = {};
  signed char dslot[NS] = {}, dk[NS] = {}, prio[NS] = {};
  signed char e0[NS] = {}, ne0[NS] = {}, ne1[NS] = {}, eg[NS] = {};
  signed char og[NS] = {}, o0[NS] = {};
  signed char xrd[NS] = {}, cbar[NS] = {}, xst[NS] = {}, ost[NS] = {}, xop[NS] = {};
  signed char flag[NS] = {}, fbar[NS] = {}, poll[NS] = {}, chk[NS] = {}, ld[NS] = {}, wr[NS] = {}, rd[NS] = {};
  signed char wl[NS] = {}, wf[NS] = {};
  int ebar = 0;
  bool ok = true;
};
constexpr Sch make_sch() {
  Sch s;
  int n2 = 0, nb = 0;
  auto put = [&](int own, int u, int db) {
    s.sown[nb] = own; s.su[nb] = u; s.sdb[nb] = db;
    for (int t = 0; t < (db == 4 ? 1 : 2); ++t) {
      s.own[n2] = own; s.u[n2] = u; s.db[n2] = db; s.t01[n2] = t;
      s.rs[P2 + n2] = 27 + nb / 6; s.lo[P2 + n2] = ((27 + nb / 6) % 3) * 24 + (nb % 6) * 4; ++n2;
    }
    ++nb;
  };
  for (int u = 0; u < 9; ++u) for (int d = 0; d < 5; ++d) put(1, u, d);
  for (int u = 0; u < 6; ++u) { put(0, u, 4); put(0, u, 0); put(0, u, 1); }
  for (int u = 6; u < 9; ++u) { put(0, u, 4); put(0, u, 0); }
  for (int u = 6; u < 9; ++u) put(0, u, 1);
  for (int d = 2; d < 4; ++d) for (int u = 0; u < 9; ++u) put(0, u, d);
  for (int n = 0; n < PH; ++n) { s.rs[n] = n / 12; s.lo[n] = ((n / 12) % 3) * 24 + ((n % 12) >> 1) * 4; }
  for (int n = PH; n < P2; ++n) { s.rs[n] = n / 12; s.lo[n] = ((n / 12) % 3) * 24 + (n % 12) * 2; }
  for (int n = NS; n < NS + 4; ++n) { s.rs[n] = s.rs[NS - 1]; s.lo[n] = s.lo[NS - 1]; }
  for (int n = 0; n < NS; ++n) {
    s.bar[n] = s.rs[n + 3] != s.rs[n + 2];
    s.dslot[n] = -1; s.prio[n] = -1; s.eg[n] = -1; s.og[n] = -1; s.flag[n] = -1; s.fbar[n] = -1; s.poll[n] = 0; s.chk[n] = -1;
    s.ld[n] = -1; s.rd[n] = -1; s.wl[n] = -1; s.wf[n] = -1; s.xst[n] = -1; s.ost[n] = -1; s.xrd[n] = -1; s.cbar[n] = -1; s.pf[n] = 0;
  }
  for (int n = 0; n < NS; ++n) if (s.bar[n]) {
    int e = n + 1; while (e < NS && !s.bar[e]) ++e;
    const int L = e - n, tgt = s.rs[n + 3] + 2;
    if (tgt <= 41) for (int k = 0; k < 6; ++k) { s.dslot[n + (k * L) / 6 + 1] = tgt; s.dk[n + (k * L) / 6 + 1] = k; if ((k * L) / 6 + 1 >= L) s.ok = false; }
    if (e < NS) { s.prio[n + L / 2] = 2; s.prio[e - 3] = 1; s.prio[e - 1] = 0; }
  }
  auto nbar = [&](int n) { while (!s.bar[n]) ++n; return n; };
  for (int g = 0; g < 5; ++g) {
    const int start = (g < 4 ? 72 * (g + 1) : P2) + 2, tot = g == 4 ? 46 : 92;
    int e = 0;
    for (int n = start; e < tot; ++n) {
      const int c0 = g == 4 ? 2 : 1, c1 = g >= 3 ? 2 : ((n - start) & 1);
      if (s.eg[n] >= 0) s.ok = false;
      s.eg[n] = g; s.e0[n] = e; s.ne0[n] = c0; s.ne1[n] = c1;
      for (int i = e; i < e + c0 + c1 && i < tot; ++i) if (i % 46 == 45) { int m = n + 1; while (!s.bar[m]) ++m; m += XOFF; while (s.xrd[m] >= 0) m += 3; s.xrd[m] = 2 * g + i / 46; s.xst[m + 2] = 2 * g + i / 46; }
      e += c0 + c1;
      if (g < 3 && n >= start + 68) s.ok = false;
    }
  }
  s.og[P2 + 119] = 4; s.o0[P2 + 119] = 0; s.og[P2 + 120] = 4; s.o0[P2 + 120] = 4; s.ost[P2 + 120] = 8; s.ost[P2 + 122] = 9;
  const int opk[3] = {121, 128, 145};
  for (int k = 0; k < 3; ++k) {
    for (int i = 0; i < 4; ++i) { const int n = P2 + opk[k] + i; s.og[n] = k; s.o0[n] = 2 * i; }
    s.ost[P2 + opk[k] + 3] = 2 * k; s.ost[P2 + opk[k] + 6] = 2 * k + 1;
  }
  int lds_[9] = {16, 18, 24, 34, 43, 44, 76, 78, 80}, rds[9] = {};
  for (int u = 0; u < 9; ++u) {
    const int cbn = nbar(P2 + lds_[(u / 3) * 3 + 2] + 17);
    rds[u] = cbn - P2 + 1 + (u < 6 ? 3 : 2) * (u % 3);
    s.ld[P2 + lds_[u]] = u; s.rd[P2 + rds[u]] = u; s.cbar[cbn] = u;
  }
  s.poll[P2 + 3] = 1; s.chk[P2 + 15] = 6;
  s.poll[P2 + 63] = 1; s.chk[P2 + 75] = 9;
  s.xop[28] = 3; s.xop[40] = 3;
  s.pf[62] = 1; s.pf[110] = 2; s.pf[160] = 3;
  const int fb[2] = {PH + 21, nbar(P2 + 40)}, fv[2] = {6, 9};
  for (int k = 0; k < 2; ++k) { if (!s.bar[fb[k]]) s.ok = false; s.fbar[fb[k]] = fv[k]; s.flag[fb[k] + 1] = fv[k]; }
  s.ebar = P2 + EBAR;
  int cl[NS + 1] = {}, cf[NS + 1] = {};
  for (int n = 9; n < NS; ++n) {
    const int both = (s.ost[n] >= 0 ? 1 : 0) + s.xop[n];
    cl[n + 1] = cl[n] + both + (s.dslot[n] >= 0 ? 1 : 0);
    cf[n + 1] = cf[n] + both + (s.xst[n] >= 0 ? 2 : 0) + (s.flag[n] >= 0 ? 1 : 0) + s.poll[n] + (s.ld[n] >= 0 ? 2 : 0) + (s.pf[n] ? 2 : 0);
  }
  auto need = [&](int n, int ul_, int uf_) {
    if (ul_ >= 0) { const int w = cl[n] - ul_; if (s.wl[n] < 0 || w < s.wl[n]) s.wl[n] = w; }
    if (uf_ >= 0) { const int w = cf[n] - uf_; if (s.wf[n] < 0 || w < s.wf[n]) s.wf[n] = w; }
  };
  for (int n = 21; n < NS; ++n) {
    if (s.bar[n]) {
      int last = -1;
      for (int i = 9; i < n; ++i) if (s.dslot[i] == s.rs[n + 3]) last = i;
      need(n, last >= 0 ? cl[last] + 1 : 0, n == 21 ? 0 : -1);
      if (n == 45) need(n, cl[29], cf[29]);
      if (n == 57) need(n, cl[41], cf[41]);
      if (s.fbar[n] >= 0) {
        int at = -1;
        for (int i = 9; i < n; ++i) if (s.xst[i] == s.fbar[n] - 1) at = i;
        if (at < 0) s.ok = false;
        for (int i = 9; i < n; ++i) if (s.xst[i] >= 0 && s.xst[i] < s.fbar[n] && i > at) at = i;
        need(n, -1, cf[at + 1]);
      }
    }
    if (n == s.ebar) need(n, cl[P2 + 135], cf[P2 + 135]);
    if (s.chk[n] >= 0) { int i = n; while (!s.poll[i]) --i; need(n, -1, cf[i + 1]); }
    if (s.cbar[n] >= 0) { int i = n; while (s.ld[i] != s.cbar[n]) --i; need(n, -1, cf[i + 1]); }
  }
  for (int u = 0; u < 9; ++u) {
    int own_rd = u == 0 ? P2 - 1 : P2 + 9 * (u - 1) + 4;
    for (int n = 0; n < NS; ++n) if (s.xrd[n] == u && n > own_rd) own_rd = n;
    bool b1 = false;
    for (int n = own_rd + 1; n <= P2 + lds_[u]; ++n) b1 = b1 || s.bar[n];
    int use = 81; while (s.u[use] != u) ++use;
    if (!b1 || rds[u] + 2 > use) s.ok = false;
  }
  return s;
}
constexpr Sch SCH = make_sch();
static_assert(SCH.ok, "schedule");

template <int I, int N, class F> DI void static_for(F&& f) {
  if constexpr (N - I == 1) f(std::integral_constant<int, I>{});
  else if constexpr (N - I > 1) { static_for<I, (I + N) / 2>(f); static_for<(I + N) / 2, N>(f); }
}
template <int N> DI void waitv() { asm volatile("s_waitcnt vmcnt(%0)" :: "n"(N) : "memory"); }
#define MFMA_DRAIN() do { asm volatile("s_nop 7\n\ts_nop 3" ::: "memory"); SB0(); } while (0)
typedef __attribute__((ext_vector_type(4))) int srd_t;
DI srd_t make_srd(const void* p, unsigned bytes) {
  const unsigned long long a = (unsigned long long)p;
  srd_t r = {(int)(unsigned)a, (int)((unsigned)(a >> 32) & 0xffffu), (int)bytes, 0x00020000};
  return r;
}
template <int LOFF, int SOFF> DI void bdma_i(srd_t srd, unsigned voff, unsigned sbase, unsigned lbase) {
  unsigned so;
  asm volatile("s_add_u32 m0, %4, %6\n\ts_add_u32 %0, %3, %5\n\tbuffer_load_dwordx4 %1, %2, %0 offen lds"
               : "=&s"(so) : "v"(voff), "s"(srd), "s"(sbase), "s"(lbase), "n"(SOFF), "n"(LOFF) : "memory", "scc");
}

template <int OFF> DI void bdma_sc1(srd_t srd, unsigned voff, unsigned lbase) {
  unsigned so;
  asm volatile("s_add_u32 m0, %3, %4\n\ts_mov_b32 %0, %4\n\tbuffer_load_dwordx4 %1, %2, %0 offen sc1 lds"
               : "=&s"(so) : "v"(voff), "s"(srd), "s"(lbase), "n"(OFF) : "memory", "scc");
}

template <int FJ>
DI void k3_body(const Ptrs& P, lds_char* lds, const int lane, const int mi, const int b, const int c, const int mb) {
  constexpr int fj = FJ;
  const unsigned lds_a = (unsigned)(uintptr_t)lds;
  const int mt = mb * 4 + mi, p = c & 1;
  const int g = lane >> 4;
  ST_DECL; ST();
  const unsigned voff = lane * 16;
  const srd_t sd = make_srd(P.wd, D_ * F_ * 2);
  const srd_t sg = make_srd(P.wg, D_ * F_ * 2), su = make_srd(P.wu, D_ * F_ * 2);
  const auto wsr = WS_RSRC(P);
  const int wid = fj * 4 + mi;
  const unsigned sgu = 8 * mi * (F_ * 2) + 576 * c + 64 * p;
  const unsigned ring_w = lds_a + K3_RING + mi * 1024;
  auto vgu_f = [&]() { return (unsigned)((lane >> 3) * (F_ * 2) + ((((lane >> 1) & 3) ^ (((lane >> 4) & 1) | ((mi & 1) << 1))) * 32) + (lane & 1) * 16); };
  auto first_slot = [&](auto tc) __attribute__((always_inline)) {
    constexpr int t = decltype(tc)::value;
    static_for<3 * FJ, 3 * FJ + 3>([&](auto kc) __attribute__((always_inline)) {
      constexpr int k = decltype(kc)::value;
      bdma_i<t * 24576 + k * 4096, 32 * (3 * t + (k >> 1)) * (F_ * 2)>((k & 1) ? su : sg, vgu_f(), sgu, ring_w);
    });
  };
  auto pf_gu = [&](unsigned& sink, const int i, const int col) {
    const int l36 = lane < 36 ? lane : 0, mat = l36 >= 18 ? 1 : 0, row = ((mb & 7) * 4 + i) * 18 + l36 - 18 * mat;
    const char* pa = (mat ? P.wu : P.wg) + (size_t)row * (F_ * 2) + 576 * c + 64 * p + col;
    asm volatile("global_load_dword %0, %1, off\n\tglobal_load_dword %0, %1, off offset:128" : "+v"(sink) : "v"(pa) : "memory");
  };
  {
    const int r = lane & 31, h = lane >> 5;
    const int hp = c;
    const int mt2 = mb * 4 + (c >> 2), cb = c & 3;
    u32x4 af[18], a4[5], xv0, xv1;
    float mean;
    const int k4 = FJ == 0 ? 5 * mi : 20 + 4 * mi;
    constexpr int n4 = FJ == 0 ? 5 : 4;
    const int row = mt2 * 32 + r, s0 = cb * 9 + 2 * (FJ == 0 ? mi : 4);
    auto prefetch2 = [&]() __attribute__((always_inline)) {
      const char* wo = P.ws + OFF_WOT + (size_t)cb * WOT_CB + h * 2336;
#pragma unroll
      for (int i = 0; i < 18; ++i) af[i] = *(const u32x4*)(wo + (18 * FJ + i) * 4672 + (32 * mi + r) * 16);
#pragma unroll
      for (int i = 0; i < n4; ++i) a4[i] = *(const u32x4*)(wo + (k4 + i) * 4672 + (128 + (r < 17 ? r : 17)) * 16);
      xv0 = *(const u32x4*)(P.x + (size_t)row * 1152 + s0 * 32 + h * 16);
      xv1 = *(const u32x4*)(P.x + (size_t)row * 1152 + (FJ == 0 ? s0 + 1 : s0) * 32 + h * 16);
      mean = ((const float*)(P.ws + OFF_MEANX))[row];
      SB0();
    };
    unsigned sink = 0;
    if constexpr (FJ == 1) {
      const char* wsrc = P.ws + OFF_WQT + (size_t)hp * WQT_HP + mi * 1024;
      const unsigned wdst = lds_a + K1_W + mi * 1024;
#pragma unroll
      for (int i = 0; i < 14; ++i) dma16x1(wsrc + i * 4096, voff, wdst + i * 4096);
      WAITV(8); BAR();
#pragma unroll
      for (int i = 14; i < 20; ++i) dma16x1(wsrc + i * 4096, voff, wdst + i * 4096);
      WAITV(9); BAR();
#pragma unroll
      for (int i = 20; i < 26; ++i) dma16x1(wsrc + i * 4096, voff, wdst + i * 4096);
      WAITV(9); BAR();
#pragma unroll
      for (int i = 26; i < 31; ++i) dma16x1(wsrc + i * 4096, voff, wdst + i * 4096);
      WAITV(9); BAR();
      WAITV(0);
      prefetch2();
      if (mi >= 2) { pf_gu(sink, (mi & 1) * 2, 0); pf_gu(sink, (mi & 1) * 2 + 1, 0); }
      BAR();
      ST();
      if (mi < 2) {
        __builtin_amdgcn_s_setprio(3);
        const int q = (lane >> 2) & 3, op = 4 * (g & 1) + (lane & 3);
        const int vq = op >= 5 ? 19 : (mi ? 14 : 10) + op, kq = (mi ? 5 : 1) + op;
        lds_char* va = lds + (8 * (g >> 1) + q) * KVP + vq * 8;
        lds_char* ka = lds + (8 * (g >> 1) + q) * KVP + kq * 8;
        f32x16 kv;
#pragma unroll
        for (int i = 0; i < 16; ++i) kv[i] = 0.f;
#pragma unroll
        for (int ks = 0; ks < 8; ++ks) {
          u32x4 a = tr_frag(va + ks * 16 * KVP, va + ks * 16 * KVP + 4 * KVP);
          u32x4 bb = tr_frag(ka + ks * 16 * KVP, ka + ks * 16 * KVP + 4 * KVP);
          kv = MFMA32(as_frag(a), as_frag(bb), kv);
        }
        const int d = r - 2 * mi;
        const int dd = mi ? (d >= 2 ? d - 2 : 16 + d) : d;
        float* dst = (float*)(P.ws + OFF_KV) + (2 * hp + mi) * 400 + dd;
        const bool dok = d >= 0 && d < 18;
#pragma unroll
        for (int i = 0; i < 12; ++i) {
          const int rw = (i & 3) + 8 * (i >> 2) + 4 * h;
          const int e = rw == 20 ? 18 : rw - 2 * mi;
          if (dok && (rw == 20 || (e >= 0 && e < 18 && rw < 20))) atomicAdd(dst + e * 20, kv[i]);
        }
        WAITV(0);
      }
    } else {
      u32x4 xf[36];
      const char* xsrc = P.ws + OFF_XN + (size_t)mt * 36864 + lane * 16;
#pragma unroll
      for (int s = 0; s < 18; ++s) xf[s] = *(const u32x4*)(xsrc + s * 1024);
      f32x16 acc[3], accq;
#pragma unroll
      for (int i = 0; i < 16; ++i) { acc[0][i] = 0.f; acc[1][i] = 0.f; acc[2][i] = 0.f; accq[i] = 0.f; }
      lds_char* wa = lds + K1_W + (h * 76 + r) * 16;
      lds_char* wa2 = lds + K1_W + (h * 76 + 64 + (r < 12 ? r : 11)) * 16;
      u32x4 wf[2][3];
#pragma unroll
      for (int s = 0; s < 36; ++s) {
        if (s % 9 == 0) {
          BAR();
          if (s < 18) {
#pragma unroll
            for (int k = s + 18; k < s + 27; ++k) xf[k] = *(const u32x4*)(xsrc + k * 1024);
          }
          wf[s & 1][0] = *(lds_u32x4*)(wa + s * 2432); wf[s & 1][1] = *(lds_u32x4*)(wa + s * 2432 + 512); wf[s & 1][2] = *(lds_u32x4*)(wa2 + s * 2432);
        }
        if (s % 9 != 8) {
          wf[(s + 1) & 1][0] = *(lds_u32x4*)(wa + (s + 1) * 2432); wf[(s + 1) & 1][1] = *(lds_u32x4*)(wa + (s + 1) * 2432 + 512);
          wf[(s + 1) & 1][2] = *(lds_u32x4*)(wa2 + (s + 1) * 2432);
        }
        SB0();
#pragma unroll
        for (int t = 0; t < 3; ++t) acc[t] = MFMA32(as_frag(wf[s & 1][t]), as_frag(xf[s]), acc[t]);
        SB0();
      }
      ST();
      lds_char* kvrow = lds + (mi * 32 + r) * KVP;
      u32x2 qrem;
#pragma unroll
      for (int gi = 0; gi < 10; ++gi) {
        const int t = gi >> 2, gg = gi & 3;
        u32x2 o;
        if (gi < 5) { o[0] = pk2(phi(acc[t][4 * gg]), phi(acc[t][4 * gg + 1])); o[1] = pk2(phi(acc[t][4 * gg + 2]), phi(acc[t][4 * gg + 3])); }
        else { o[0] = pk2(acc[t][4 * gg], acc[t][4 * gg + 1]); o[1] = pk2(acc[t][4 * gg + 2], acc[t][4 * gg + 3]); }
        if (gi == 9 && h == 1) { o[0] = 0x3f803f80u; o[1] = 0x3f803f80u; }
        *(lds_u32x2*)(kvrow + (2 * gi + h) * 8) = o;
        if (gi == 0) qrem = o;
      }
      WAITL0();
      BAR();
      lds_char* wq = lds + K1_W + WQT_Q + lane * 16;
      u32x4 aq[2][4];
#pragma unroll
      for (int i = 0; i < 4; ++i) aq[0][i] = *(lds_u32x4*)(wq + i * 1024);
#pragma unroll
      for (int g3 = 0; g3 < 9; ++g3) {
        if (g3 < 8) {
#pragma unroll
          for (int i = 0; i < 4; ++i) aq[(g3 + 1) & 1][i] = *(lds_u32x4*)(wq + (4 * g3 + 4 + i) * 1024);
        }
        SB0();
#pragma unroll
        for (int i = 0; i < 4; ++i) accq = MFMA32(as_frag(aq[g3 & 1][i]), as_frag(xf[4 * g3 + i]), accq);
        SB0();
      }
      const int pq = (int)OFF_PQ + mt * 36864;
#pragma unroll
      for (int e = 0; e < 2; ++e) {
        u32x4 o;
#pragma unroll
        for (int k = 0; k < 4; ++k) o[k] = pk2(phi(accq[8 * e + 2 * k]), phi(accq[8 * e + 2 * k + 1]));
        WS_ST16(P, pq + (2 * hp + e) * 1024 + lane * 16, o, 16);
      }
      if (h == 0) WS_ST8(P, pq + (32 + (hp >> 2)) * 1024 + (r + 32 * ((hp >> 1) & 1)) * 16 + 8 * (hp & 1), qrem, 16);
      WAITV(0);
    }
    BAR();
    ST();
    if (FJ == 1 && mi == 0) {
      constexpr int NSH = 256 / G1R;
      if (lane < G1R) __hip_atomic_fetch_add((unsigned*)(P.ws + OFF_G1 + (lane * NSH + (b % NSH)) * 128), 1u, __ATOMIC_RELAXED, __HIP_MEMORY_SCOPE_AGENT);
      poll_counters(P.ws, (int)OFF_G1 + ((b / NSH) * NSH + (lane % NSH)) * 128, (unsigned)G1R);
    }
    if constexpr (FJ == 0) { __builtin_amdgcn_s_sleep(4); prefetch2(); }
    BAR();
    ST();
    const int e = r < 16 ? r : (r == 16 ? 16 : (r == 20 ? 17 : 18));
    const bool rok = r <= 17 || r == 20 || r == 21;
    u32x4 k0[4], k1[4], qf[4]; u32x2 k2[4];
    dma16x3s(P.ws + OFF_KV + wid * 6400, voff, lds_a + wid * 6144);
    dma16x3s(P.ws + OFF_KV + wid * 6400 + 3072, voff, lds_a + wid * 6144 + 3072);
    const u32x4 ktail = __builtin_amdgcn_raw_buffer_load_b128(wsr, (lane & 15) * 16, (int)OFF_KV + wid * 6400 + 6144, 16);
#pragma unroll
    for (int i = 0; i < 4; ++i) qf[i] = __builtin_amdgcn_raw_buffer_load_b128(wsr, voff, (int)OFF_PQ + mt2 * 36864 + (4 * wid + i) * 1024, 16);
    const u32x4 qr = __builtin_amdgcn_raw_buffer_load_b128(wsr, voff, (int)OFF_PQ + mt2 * 36864 + (32 + (wid >> 1)) * 1024, 16);
    *(lds_u32x4*)(lds + K2_RED + wid * 256 + (lane & 15) * 16) = ktail;
    asm volatile("" ::: "memory");
    SB0();
#pragma unroll
    for (int i = 0; i < 4; ++i) {
      const int o = i * 1600 + e * 80;
      lds_char* kp0 = lds + (o + 16 * h < 6144 ? wid * 6144 + o + 16 * h : K2_RED + wid * 256 + o + 16 * h - 6144);
      lds_char* kp1 = lds + (o + 16 * h + 32 < 6144 ? wid * 6144 + o + 16 * h + 32 : K2_RED + wid * 256 + o + 16 * h + 32 - 6144);
      lds_char* kp2 = lds + (o + 64 < 6144 ? wid * 6144 + o + 64 : K2_RED + wid * 256 + o + 64 - 6144);
      k0[i] = *(lds_u32x4*)kp0; k1[i] = *(lds_u32x4*)kp1; k2[i] = *(lds_u32x2*)kp2;
    }
    float orem[4];
#define F_(x) __uint_as_float(x)
#pragma unroll
    for (int i = 0; i < 4; ++i) {
      u32x4 a1 = {pk2(F_(k0[i][0]), F_(k0[i][1])), pk2(F_(k0[i][2]), F_(k0[i][3])), pk2(F_(k1[i][0]), F_(k1[i][1])), pk2(F_(k1[i][2]), F_(k1[i][3]))};
      u32x4 a2 = {0u, 0u, 0u, 0u};
      a2[i] = (h == (mi & 1)) ? pk2(F_(k2[i][0]), F_(k2[i][1])) : 0u;
      if (!rok) { a1 = (u32x4){0u, 0u, 0u, 0u}; a2[i] = 0u; }
      f32x16 cc;
#pragma unroll
      for (int j = 0; j < 16; ++j) cc[j] = 0.f;
      cc = MFMA32(as_frag(a1), as_frag(qf[i]), cc);
      cc = MFMA32(as_frag(a2), as_frag(qr), cc);
      const float inv = __builtin_amdgcn_rcpf(cc[9] + 1e-6f);
      float o[9];
#pragma unroll
      for (int j = 0; j < 9; ++j) o[j] = cc[j] * inv;
      u32x4 of = {pk2(o[0], o[1]), pk2(o[2], o[3]), pk2(o[4], o[5]), pk2(o[6], o[7])};
      *(lds_u32x4*)(lds + K2_O + (4 * wid + i) * 1024 + lane * 16) = of;
      orem[i] = o[8];
    }
#undef F_
    {
      u32x2 o2 = {pk2(orem[0], orem[1]), pk2(orem[2], orem[3])};
      *(lds_u32x2*)(lds + K2_O + (32 + (wid >> 1)) * 1024 + lane * 16 + 8 * (mi & 1)) = o2;
    }
    WAITL0();
    BAR();
    ST();
    f32x16 acc, acc4;
#pragma unroll
    for (int j = 0; j < 16; ++j) { acc[j] = 0.f; acc4[j] = 0.f; }
    lds_char* ob = lds + K2_O + lane * 16;
#pragma unroll
    for (int i = 0; i < n4; ++i) acc4 = MFMA32(as_frag(a4[i]), as_frag(*(lds_u32x4*)(ob + (k4 + i) * 1024)), acc4);
#pragma unroll
    for (int i = 0; i < 18; ++i) acc = MFMA32(as_frag(af[i]), as_frag(*(lds_u32x4*)(ob + (18 * FJ + i) * 1024)), acc);
    {
      *(lds_float*)(lds + K2_MEAN + (wid * 64 + lane) * 4) = acc4[8] + acc4[9];
      lds_char* p4 = lds + K2_RED + 16384 + wid * 2048 + lane * 16;
      *(lds_f32x4*)(p4) = (f32x4){acc4[0], acc4[1], acc4[2], acc4[3]};
      *(lds_f32x4*)(p4 + 1024) = (f32x4){acc4[4], acc4[5], acc4[6], acc4[7]};
      if (FJ == 1) {
        lds_char* pr = lds + K2_RED + mi * 4096 + lane * 16;
#pragma unroll
        for (int k = 0; k < 4; ++k) *(lds_f32x4*)(pr + k * 1024) = (f32x4){acc[4 * k], acc[4 * k + 1], acc[4 * k + 2], acc[4 * k + 3]};
      }
    }
    WAITL0();
    BAR();
    if (FJ == 0 || mi == 0) {
#pragma unroll
      for (int w = 0; w < 8; ++w) mean += *(lds_float*)(lds + K2_MEAN + (w * 64 + r) * 4);
      if constexpr (FJ == 0) {
        lds_char* pr = lds + K2_RED + mi * 4096 + lane * 16;
#pragma unroll
        for (int k = 0; k < 4; ++k) {
          f32x4 v = *(lds_f32x4*)(pr + k * 1024);
#pragma unroll
          for (int j = 0; j < 4; ++j) acc[4 * k + j] += v[j];
        }
      } else {
#pragma unroll
        for (int j = 0; j < 8; ++j) acc[j] = 0.f;
#pragma unroll
        for (int w = 0; w < 8; ++w) {
          lds_char* p4 = lds + K2_RED + 16384 + w * 2048 + lane * 16;
          f32x4 v0 = *(lds_f32x4*)(p4), v1 = *(lds_f32x4*)(p4 + 1024);
#pragma unroll
          for (int j = 0; j < 4; ++j) { acc[j] += v0[j]; acc[4 + j] += v1[j]; }
        }
      }
      float ssq = 0.f;
#pragma unroll
      for (int i = 0; i < (FJ == 0 ? 2 : 1); ++i) {
        float v[8];
#pragma unroll
        for (int j = 0; j < 4; ++j) {
          auto sw2 = __builtin_amdgcn_permlane32_swap(__float_as_uint(acc[8 * i + j]), __float_as_uint(acc[8 * i + 4 + j]), false, false);
          v[j] = __uint_as_float(sw2[0]); v[4 + j] = __uint_as_float(sw2[1]);
        }
        const u32x4 xv = i ? xv1 : xv0;
        u32x4 o;
#pragma unroll
        for (int k = 0; k < 4; ++k) {
          const float a0 = v[2 * k] + bflo(xv[k]) - mean, a1 = v[2 * k + 1] + bfhi(xv[k]) - mean;
          ssq += a0 * a0 + a1 * a1;
          o[k] = pk2(a0, a1);
        }
        const int d8 = 2 * (s0 + i) + h;
        WS_ST16(P, OFF_HC + (((row >> 4) * 18 + (d8 >> 2)) * 64 + (row & 15) + 16 * (d8 & 3)) * 16, o, 16);
      }
      ssq += __shfl_xor(ssq, 32);
      if (h == 0) {
        WS_ST4(P, OFF_SSQ + ((mt2 * 20 + cb * 5 + wid) * 32 + r) * 4, __float_as_uint(ssq), 16);
        if (cb == 0 && wid == 0) WS_ST4(P, OFF_MEANH + row * 4, __float_as_uint(mean), 16);
      }
    }
    WAITV(0); WAITL0();
    asm volatile("" :: "v"(sink));
    BAR();
    ST();
    if (FJ == 1 && mi == 1 && lane == 0) __hip_atomic_fetch_add((unsigned*)(P.ws + OFF_G3 + mb * 128), 1u, __ATOMIC_RELAXED, __HIP_MEMORY_SCOPE_AGENT);
    first_slot(std::integral_constant<int, 0>{});
    first_slot(std::integral_constant<int, 1>{});
    if (FJ == 1 && mi == 1) poll_counters(P.ws, (int)OFF_G3 + mb * 128, 16u);
    BAR();
    ST();
  }
  const char* hsrc = P.ws + OFF_HC + (size_t)mt * 36864;
  const char* hmine = hsrc + fj * 18432;
  constexpr int ST0 = K3_RING + 2 * 24576, ST1 = K3_HH, ST2 = K3_HH + 24576;
  dma16x3s(hmine, voff, lds_a + ST0 + (mi * 2 + fj) * 3072);
  dma16x3s(hmine + 3072, voff, lds_a + ST1 + (mi * 2 + fj) * 3072);
  dma16x3s(hmine + 6144, voff, lds_a + ST2 + (mi * 2 + fj) * 6144);
  dma16x3s(hmine + 9216, voff, lds_a + ST2 + (mi * 2 + fj) * 6144 + 3072);
  u32x4 hf[2][18];
  WAITV(9);
  BAR();
  const int q = (lane >> 2) & 3, pp = lane & 3, xg = ((q >> 1) & 1) | ((g & 1) << 1);
  lds_char* rd_g0 = lds + K3_RING + (8 * g + q) * 128 + ((fj ^ xg) * 32) + 8 * pp;
  lds_char* rd_g1 = lds + K3_RING + (8 * g + q) * 128 + (((fj + 2) ^ xg) * 32) + 8 * pp;
  lds_char* hh_rd = lds + K3_HH + mi * 18432 + lane * 16;
  const unsigned vgu = vgu_f();
  u32x4 a[6];
  auto read_frag = [&](auto nc) __attribute__((always_inline)) {
    constexpr int n = decltype(nc)::value;
    if constexpr (n < NS) {
      constexpr int o = SCH.lo[n] * 1024;
      if constexpr (n < PH) { lds_char* r = ((n & 1) ? rd_g1 : rd_g0) + o; a[n % 6] = tr_frag(r, r + 512); }
      else if constexpr (n < P2) { lds_char* r = lds + K3_RING + (8 * g + q) * 64 + ((fj ^ (g & 1)) * 32) + 8 * pp + o; a[n % 6] = tr_frag(r, r + 256); }
      else {
        const int gr = SCH.db[n - P2] == 4 ? 2 * p + fj : 2 * fj + SCH.t01[n - P2];
        lds_char* r = lds + K3_RING + (4 * g + q) * 128 + ((gr ^ xg) * 32) + 8 * pp + o;
        a[n % 6] = tr_frag(r, r + 2048);
      }
    }
  };
#pragma unroll
  for (int i = 0; i < 6; ++i) hf[i & 1][i >> 1] = *(lds_u32x4*)(lds + ST0 + (mi * 2 + (i & 1)) * 3072 + (i >> 1) * 1024 + lane * 16);
  static_for<0, 3>(read_frag);
  WAITL0();
  BAR();
  first_slot(std::integral_constant<int, 2>{});
  ST();
  const int fl_mine = (int)OFF_FLAG + b * 128;
  const char* fl_other = P.ws + OFF_FLAG + (b ^ 8) * 128;
  const int xh_off = (int)OFF_XH + (b * 4 + mi) * 18432;
  const srd_t sx = make_srd(P.ws + OFF_XH + (size_t)((b ^ 8) * 4 + mi) * 18432, 18432);
  const int pbase = (int)OFF_XH + (b ^ 8) * 73728 + mi * 18432;
  lds_char* hh_wr = hh_rd + 8 * fj;
  float sigma[2], gsc[2];
  f32x4 acc[2][2][2][2];
  f32x4 oc[9][2];
  u32x4 ov[2], ho[2][2], hp[9][2], xl[2];
  u32x2 hpk;
  u32x2 ovs[2];
  unsigned pollv, pfs = 0;

  auto epi = [&](auto gc, auto ic) __attribute__((always_inline)) {
    constexpr int G = decltype(gc)::value, i = decltype(ic)::value;
    if constexpr (i < (G == 4 ? 46 : 92)) {
      constexpr int uu = i / 46, mh = (i % 46) / 23, r = i % 23, set = G & 1, U = 2 * G + uu;
      if constexpr (r < 20) {
        constexpr int e = (r / 10) * 2 + (r & 1), ph = (r % 10) >> 1;
        float tv = acc[set][uu][0][mh][e];
        if (ph == 0) tv = tv * gsc[mh];
        if (ph == 1) tv = __builtin_amdgcn_exp2f(tv);
        if (ph == 2) tv = __builtin_fmaf(tv, sigma[mh], sigma[mh]);
        if (ph == 3) tv = __builtin_amdgcn_rcpf(tv);
        if (ph == 4) tv = tv * acc[set][uu][1][mh][e];
        asm volatile("" : "+v"(tv));
        acc[set][uu][0][mh][e] = tv;
      } else if constexpr (r < 22) {
        unsigned t = pk2(acc[set][uu][0][mh][2 * (r - 20)], acc[set][uu][0][mh][2 * (r - 20) + 1]);
        asm volatile("" : "+v"(t));
        hpk[r - 20] = t;
      } else {
        *(lds_u32x2*)(hh_wr + mh * 9216 + U * 1024) = hpk;
      }
      SB0();
    }
  };
  auto opack = [&](const int pr, const int i) __attribute__((always_inline)) {
    const int mh = i >> 2, r = i & 3;
    if (pr == 4 && r >= 2) return;
    const int ti = pr == 4 ? 8 : 2 * pr + (r >> 1), e = pr == 4 ? 2 * r : 2 * (r & 1);
    unsigned t = pk2(oc[ti][mh][e], oc[ti][mh][e + 1]);
    asm volatile("" : "+v"(t));
    if (pr == 4) ovs[mh][r & 1] = t; else ov[mh][r] = t;
    SB0();
  };
  auto ostore = [&](const int x) __attribute__((always_inline)) {
    const int pr = x >> 1, mh = x & 1;
    if (pr < 4) st128(ov[mh], wsr, voff, pbase + (p + 2 * pr + fj + 9 * mh) * 1024);
    else __builtin_amdgcn_raw_buffer_store_b64(ovs[mh], wsr, voff, pbase + ((p ? 0 : 8) + 9 * mh) * 1024 + 8 * fj, 16);
    SB0();
  };

  static_for<0, NS>([&](auto nc) __attribute__((always_inline)) {
    constexpr int n = decltype(nc)::value;
    constexpr bool bar = SCH.bar[n];
    if constexpr (n == 9) WAITV(12);
    else if constexpr (FJ == 0 && SCH.wl[n] >= 0) waitv<SCH.wl[n]>();
    else if constexpr (FJ == 1 && SCH.wf[n] >= 0) waitv<SCH.wf[n]>();
    if constexpr (FJ == 1 && SCH.chk[n] >= 0) {
      asm volatile("" : "+v"(pollv));
      if (__builtin_amdgcn_readfirstlane(pollv) < (unsigned)SCH.chk[n]) {
#pragma unroll 1
        for (int spin = 0; spin < (1 << 22); ++spin) {
          asm volatile("global_load_dword %0, %1, %2 sc1\n\ts_waitcnt vmcnt(0)" : "=&v"(pollv) : "v"(0u), "s"(fl_other) : "memory");
          if (__builtin_amdgcn_readfirstlane(pollv) >= (unsigned)SCH.chk[n]) break;
          __builtin_amdgcn_s_sleep(2);
        }
      }
    }
    if constexpr (bar) { WAITL0(); BAR(); __builtin_amdgcn_s_setprio(3); }
    else if constexpr (n == SCH.ebar) BAR();
    const u32x4 af = a[n % 6];
    if constexpr (SCH.prio[n] >= 0) __builtin_amdgcn_s_setprio(SCH.prio[n]);
    if constexpr (n == 72 || n == P2) ST();
    SB0();
    static_for<0, 2>([&](auto mc) __attribute__((always_inline)) {
      constexpr int mh = decltype(mc)::value;
      if constexpr (n < P2) {
        constexpr int y = n % 72;
        constexpr int set = (n / 72) & 1, S = n >= PH ? y >> 1 : y >> 2, mat = n >= PH ? y & 1 : (y >> 1) & 1, uu = n >= PH ? 0 : y & 1;
        if constexpr (S == 0) asm volatile("v_mfma_f32_16x16x32_bf16 %0, %1, %2, 0" : "=&v"(acc[set][uu][mat][mh]) : "v"(af), "v"(hf[mh][S]));
        else asm volatile("v_mfma_f32_16x16x32_bf16 %0, %1, %2, %0" : "+v"(acc[set][uu][mat][mh]) : "v"(af), "v"(hf[mh][S]));
      } else {
        constexpr int s2 = n - P2, U = SCH.u[s2], ti = SCH.db[s2] == 4 ? 8 : 2 * SCH.db[s2] + SCH.t01[s2];
        if constexpr (s2 < 9) asm volatile("v_mfma_f32_16x16x32_bf16 %0, %1, %2, 0" : "=&v"(oc[ti][mh]) : "v"(af), "v"(ho[0][mh]));
        else if constexpr (SCH.own[s2]) asm volatile("v_mfma_f32_16x16x32_bf16 %0, %1, %2, %0" : "+v"(oc[ti][mh]) : "v"(af), "v"(ho[U & 1][mh]));
        else asm volatile("v_mfma_f32_16x16x32_bf16 %0, %1, %2, %0" : "+v"(oc[ti][mh]) : "v"(af), "v"(hp[U][mh]));
      }
      SB0();
      if constexpr (mh == 0) { read_frag(std::integral_constant<int, n + 3>{}); SB0(); }
      if constexpr (SCH.eg[n] >= 0) {
        constexpr int f = SCH.e0[n] + (mh ? SCH.ne0[n] : 0), cnt = mh ? SCH.ne1[n] : SCH.ne0[n];
        static_for<f, f + cnt>([&](auto ic) __attribute__((always_inline)) { epi(std::integral_constant<int, SCH.eg[n]>{}, ic); });
      }
      if constexpr (SCH.og[n] >= 0) opack(SCH.og[n], SCH.o0[n] + mh);
    });
    if constexpr (FJ == 0 && SCH.dslot[n] >= 0) {
      constexpr int t = SCH.dslot[n], k = SCH.dk[n];
      if constexpr (t < 24) {
        bdma_i<(t % 3) * 24576 + k * 4096, 32 * (3 * (t % 6) + (k >> 1)) * (F_ * 2) + (t / 6) * 128>((k & 1) ? su : sg, vgu, sgu, ring_w);
      } else if constexpr (t < 27) {
        const unsigned vhalf = (lane >> 2) * (F_ * 2) + ((((lane >> 1) & 1) ^ ((lane >> 5) & 1)) * 32) + (lane & 1) * 16;
        bdma_i<(t % 3) * 24576 + k * 4096, 32 * (6 * (t - 24) + k) * (F_ * 2)>((mi & 1) ? su : sg, vhalf, 16 * (mi >> 1) * (F_ * 2) + 576 * c + (p ? 0 : 512),
                                                                          lds_a + K3_RING + (mi & 1) * 2048 + (mi >> 1) * 1024);
      } else {
        constexpr int sb = (t - 27) * 6 + k, ow = SCH.sown[sb], U = SCH.su[sb], d = SCH.sdb[sb];
        const unsigned vd = (lane >> 3) * (D_ * 2) + ((((lane >> 1) & 3) ^ ((lane >> 4) & 3)) * 32) + (lane & 1) * 16;
        const int cc = ow ? c : c ^ 1;
        bdma_i<(t % 3) * 24576 + k * 4096, (U == 8 ? 0 : 32 * U * (D_ * 2)) + (d == 4 ? 512 : 128 * d)>(
            sd, vd, (288 * cc + 8 * mi + (U == 8 ? ((cc & 1) ? 0 : 256) : ((cc & 1) ? 32 : 0))) * (D_ * 2) + (d == 4 ? 0 : 640 * p), ring_w);
      }
      SB0();
    }
    if constexpr (SCH.ost[n] >= 0) ostore(SCH.ost[n]);
    if constexpr (n == 5) { dma16x3s(P.ws + OFF_SSQ + mt * 2560, voff, lds_a + K3_SSQ + mi * 3072); SB0(); }
    if constexpr (n == 28) { dma16x3s(hmine + 12288, voff, lds_a + ST1 + (mi * 2 + fj) * 3072); SB0(); }
    if constexpr (n == 40) { dma16x3s(hmine + 15360, voff, lds_a + ST2 + (mi * 2 + fj) * 3072); SB0(); }
    if constexpr (n == 9 || n == 45 || n == 57) {
      constexpr int s0 = n == 9 ? 3 : (n == 45 ? 12 : 15), stg = n == 57 ? ST2 : ST1;
#pragma unroll
      for (int i = 0; i < 6; ++i) hf[i & 1][s0 + (i >> 1)] = *(lds_u32x4*)(lds + stg + (mi * 2 + (i & 1)) * 3072 + (i >> 1) * 1024 + lane * 16);
      SB0();
    }
    if constexpr (n == 21) {
#pragma unroll
      for (int i = 0; i < 12; ++i) hf[i & 1][6 + (i >> 1)] = *(lds_u32x4*)(lds + ST2 + (mi * 2 + (i & 1)) * 6144 + (i >> 1) * 1024 + lane * 16);
      SB0();
    }
    if constexpr (n == 50) {
#pragma unroll
      for (int mh = 0; mh < 2; ++mh) {
        float ssq = 0.f;
#pragma unroll
        for (int i = 0; i < 20; ++i) ssq += *(lds_float*)(lds + K3_SSQ + mi * 3072 + (i * 32 + mh * 16 + (lane & 15)) * 4);
        sigma[mh] = sqrtf(ssq * (1.f / D_) + LN_EPS);
        gsc[mh] = -1.4426950408889634f / sigma[mh];
        asm volatile("" : "+v"(sigma[mh]), "+v"(gsc[mh]));
      }
      SB0();
    }
    if constexpr (n == P2 - 1) { ho[0][0] = *(lds_u32x4*)(hh_rd); ho[0][1] = *(lds_u32x4*)(hh_rd + 9216); SB0(); }
    if constexpr (n >= P2 && n < P2 + 72 && (n - P2) % 9 == 4) {
      constexpr int U = (n - P2) / 9 + 1;
      ho[U & 1][0] = *(lds_u32x4*)(hh_rd + U * 1024); ho[U & 1][1] = *(lds_u32x4*)(hh_rd + 9216 + U * 1024);
      SB0();
    }
    if constexpr (FJ == 1) {
      if constexpr (SCH.xst[n] >= 0) {
        st128(xl[0], wsr, voff, xh_off + SCH.xst[n] * 1024);
        st128(xl[1], wsr, voff, xh_off + 9216 + SCH.xst[n] * 1024);
        SB0();
      }
      if constexpr (SCH.flag[n] >= 0) { __builtin_amdgcn_raw_buffer_store_b32((unsigned)SCH.flag[n], wsr, 0, fl_mine, 16); SB0(); }
      if constexpr (SCH.poll[n]) { asm volatile("global_load_dword %0, %1, %2 sc1" : "=&v"(pollv) : "v"(0u), "s"(fl_other) : "memory"); SB0(); }
      if constexpr (SCH.ld[n] >= 0) {
        constexpr int u = SCH.ld[n];
        bdma_sc1<u * 1024>(sx, voff, lds_a + K3_HH + mi * 18432);
        bdma_sc1<9216 + u * 1024>(sx, voff, lds_a + K3_HH + mi * 18432);
        SB0();
      }
      if constexpr (n == SCH.ebar) {
        if (mi == 0 && lane == 0) __hip_atomic_fetch_add((unsigned*)(P.ws + OFF_CNT + (mb * 2 + p) * 128), 1u, __ATOMIC_RELAXED, __HIP_MEMORY_SCOPE_AGENT);
        SB0();
      }
      if constexpr (SCH.pf[n] == 1 || SCH.pf[n] == 2) { pf_gu(pfs, mi, SCH.pf[n] == 1 || p ? 256 : 512); SB0(); }
      if constexpr (SCH.pf[n] == 3) {
        const int l41 = lane < 41 ? lane : 0, o = (((mb & 7) * 2 + p) * 4 + mi) * 10368 + l41 * 256;
        const char* pa = P.wd + (size_t)(c >> 1) * 663552 + (o < 663552 - 256 ? o : 663552 - 256);
        asm volatile("global_load_dword %0, %1, off\n\tglobal_load_dword %0, %1, off offset:128" : "+v"(pfs) : "v"(pa) : "memory");
        SB0();
      }
      if constexpr (SCH.flag[n] == 6) asm volatile("" :: "v"(pfs));
      if constexpr (SCH.xrd[n] >= 0) {
        xl[0] = *(lds_u32x4*)(hh_rd + SCH.xrd[n] * 1024); xl[1] = *(lds_u32x4*)(hh_rd + 9216 + SCH.xrd[n] * 1024);
        SB0();
      }
    }
    if constexpr (SCH.rd[n] >= 0) {
      constexpr int u = SCH.rd[n];
      hp[u][0] = *(lds_u32x4*)(hh_rd + u * 1024); hp[u][1] = *(lds_u32x4*)(hh_rd + 9216 + u * 1024);
      SB0();
    }
  });
  ST();
  MFMA_DRAIN();
#pragma unroll
  for (int i = 0; i < 8; ++i) opack(3, i);
  ostore(6);
  ostore(7);
  unsigned vo2 = voff;
  asm volatile("" : "+v"(vo2));
  const int ln = vo2 >> 4, gl = ln >> 4;
  const int j = c >> 1, t16 = mb * 8 + j, f0 = t16 * 18 + 9 * p;
  const int qr = p + wid, qs = p ? 0 : 8;
  const int l16 = 16 * mi + (ln & 15);
  const int row = t16 * 16 + (ln & 15);
  const int hb = ((ln & 15) + 16 * (gl >> 1)) * 16 + 8 * (gl & 1), hbs = ((ln & 15) + 16 * (mi >> 1)) * 16 + 8 * (mi & 1);
  const u32x2 h0 = __builtin_amdgcn_raw_buffer_load_b64(wsr, hb, (int)OFF_HC + (f0 + qr) * 1024, 16);
  const u32x2 h1 = __builtin_amdgcn_raw_buffer_load_b64(wsr, hb + 512, (int)OFF_HC + (f0 + qr) * 1024, 16);
  u32x2 hs0, hs1;
  if constexpr (FJ == 0) {
    hs0 = __builtin_amdgcn_raw_buffer_load_b64(wsr, hbs, (int)OFF_HC + (f0 + qs) * 1024, 16);
    hs1 = __builtin_amdgcn_raw_buffer_load_b64(wsr, hbs + 512, (int)OFF_HC + (f0 + qs) * 1024, 16);
  }
  const float mean = __uint_as_float(__builtin_amdgcn_raw_buffer_load_b32(wsr, row * 4, (int)OFF_MEANH, 16));
  const int cnt_off = (int)OFF_CNT + (mb * 2 + p) * 128;
  if constexpr (FJ == 0) poll_counters(P.ws, cnt_off, 8u);
  else WAITV(0);
  BAR();
  ST();
  if constexpr (FJ == 1) {
    if (mi == 0 && ln == 0) __hip_atomic_fetch_add((unsigned*)(P.ws + cnt_off + 64), 1u, __ATOMIC_RELAXED, __HIP_MEMORY_SCOPE_AGENT);
    poll_counters(P.ws, cnt_off + 64, 8u);
  }
  ST();
  auto area = [&](const int k) { const int ck = 2 * k + (p ^ 1); return (int)OFF_XH + (((mb & 7) * 4 + (ck & 3)) * 8 + (ck >> 2) * 2 + (mb >> 3)) * 73728 + j * 9216; };
  u32x4 pv[8], ps[2];
#pragma unroll
  for (int k = 0; k < 8; ++k) pv[k] = __builtin_amdgcn_raw_buffer_load_b128(wsr, voff, area(k) + qr * 1024, 16);
  if constexpr (FJ == 0) {
#pragma unroll
    for (int k = 0; k < 2; ++k) ps[k] = __builtin_amdgcn_raw_buffer_load_b128(wsr, area(4 * k + gl) + l16 * 16, qs * 1024, 16);
  }
  float o[8];
  o[0] = bflo(h0[0]); o[1] = bfhi(h0[0]); o[2] = bflo(h0[1]); o[3] = bfhi(h0[1]);
  o[4] = bflo(h1[0]); o[5] = bfhi(h1[0]); o[6] = bflo(h1[1]); o[7] = bfhi(h1[1]);
#pragma unroll
  for (int k = 0; k < 8; ++k) o[k] += mean;
#pragma unroll
  for (int r = 0; r < 8; ++r)
#pragma unroll
    for (int k = 0; k < 4; ++k) { o[2 * k] += bflo(pv[r][k]); o[2 * k + 1] += bfhi(pv[r][k]); }
  {
    char* d = P.out + (size_t)row * 1152 + (9 * p + qr) * 64 + gl * 8;
    *(u32x2*)d = (u32x2){pk2(o[0], o[1]), pk2(o[2], o[3])};
    *(u32x2*)(d + 32) = (u32x2){pk2(o[4], o[5]), pk2(o[6], o[7])};
  }
  if constexpr (FJ == 0) {
    float os[8];
#pragma unroll
    for (int k = 0; k < 4; ++k) { os[2 * k] = bflo(ps[0][k]) + bflo(ps[1][k]); os[2 * k + 1] = bfhi(ps[0][k]) + bfhi(ps[1][k]); }
#pragma unroll
    for (int k = 0; k < 8; ++k) { os[k] += __shfl_xor(os[k], 16); os[k] += __shfl_xor(os[k], 32); }
    if (ln < 16) {
      char* d = P.out + (size_t)row * 1152 + (9 * p + qs) * 64 + mi * 8;
      *(u32x2*)d = (u32x2){pk2(os[0] + bflo(hs0[0]) + mean, os[1] + bfhi(hs0[0]) + mean), pk2(os[2] + bflo(hs0[1]) + mean, os[3] + bfhi(hs0[1]) + mean)};
      *(u32x2*)(d + 32) = (u32x2){pk2(os[4] + bflo(hs1[0]) + mean, os[5] + bfhi(hs1[0]) + mean), pk2(os[6] + bflo(hs1[1]) + mean, os[7] + bfhi(hs1[1]) + mean)};
    }
  }
}

__global__ __launch_bounds__(512, 2) void k3_ffn(Ptrs P) {
  extern __shared__ __attribute__((aligned(16))) char smem[];
  const int lane = threadIdx.x & 63, wid = __builtin_amdgcn_readfirstlane(threadIdx.x >> 6);
  const int b = blockIdx.x;
  const int c = ((b & 7) >> 1) * 4 + ((b >> 3) & 3), mb = (b & 1) * 8 + (b >> 5);
  if (wid < 4) k3_body<0>(P, (lds_char*)smem, lane, wid, b, c, mb);
  else k3_body<1>(P, (lds_char*)smem, lane, wid - 4, b, c, mb);
}

constexpr int K0_IMG = 4608;
constexpr int K0_RED = 3 * K0_IMG;
constexpr int K0_LDS = K0_RED + 9 * 8 * 8;

DI float dpp_sum8(float v) {
  v += __shfl_xor(v, 1); v += __shfl_xor(v, 2); v += __shfl_xor(v, 4);
  return v;
}

__global__ __launch_bounds__(768) void k0_prep(Ptrs P) {
  extern __shared__ __attribute__((aligned(16))) char smem[];
  lds_char* lds = (lds_char*)smem;
  const int lane = threadIdx.x & 63, wid = __builtin_amdgcn_readfirstlane(threadIdx.x >> 6);
  const int b = blockIdx.x;
  ST_DECL; ST();
  if (wid < 9) {
    const int row = b * 8 + (lane >> 3), i = lane & 7;
    const u32x4 v = *(const u32x4*)(P.x + (size_t)row * 1152 + wid * 128 + i * 16);
    float e[8];
#pragma unroll
    for (int k = 0; k < 4; ++k) { e[2 * k] = bflo(v[k]); e[2 * k + 1] = bfhi(v[k]); }
    const float sum = dpp_sum8(((e[0] + e[1]) + (e[2] + e[3])) + ((e[4] + e[5]) + (e[6] + e[7])));
    const float lm = sum * (1.f / 64.f);
    float m2 = 0.f;
#pragma unroll
    for (int k = 0; k < 8; ++k) { const float d = e[k] - lm; m2 = __builtin_fmaf(d, d, m2); }
    m2 = dpp_sum8(m2);
    if (i == 0) *(lds_f32x2*)(lds + K0_RED + (wid * 8 + (lane >> 3)) * 8) = (f32x2){lm, m2};
    WAITL0();
    BAR();
    ST();
    f32x2 pr[9];
#pragma unroll
    for (int w = 0; w < 9; ++w) pr[w] = *(lds_f32x2*)(lds + K0_RED + (w * 8 + (lane >> 3)) * 8);
    float mean = 0.f;
#pragma unroll
    for (int w = 0; w < 9; ++w) mean += pr[w][0];
    mean *= (1.f / 9.f);
    float tot = 0.f;
#pragma unroll
    for (int w = 0; w < 9; ++w) { const float d = pr[w][0] - mean; tot += __builtin_fmaf(64.f * d, d, pr[w][1]); }
    const float rstd = rsqrtf(tot * (1.f / D_) + LN_EPS);
    u32x4 o;
#pragma unroll
    for (int k = 0; k < 4; ++k) o[k] = pk2((e[2 * k] - mean) * rstd, (e[2 * k + 1] - mean) * rstd);
    WS_ST16(P, (int)OFF_XN + ((row >> 5) * 36 * 64 + (row & 31) + 32 * (i & 1) + (4 * wid + (i >> 1)) * 64) * 16, o, ST_AUX);
    if (wid == 0 && i == 0) WS_ST4(P, OFF_MEANX + row * 4, __float_as_uint(mean), ST_AUX);
    return;
  }
  const int id = (wid - 9) * 256 + b;
  lds_char* img = lds + (wid - 9) * K0_IMG;
  const unsigned img_a = (unsigned)(uintptr_t)img;
  const int g = lane >> 4, q = (lane >> 2) & 3, p = lane & 3;
  unsigned pf_sink = 0;
  if (wid == 10 && b < 96) {
    unsigned long long pc, k1a;
    asm volatile("s_getpc_b64 %0" : "=s"(pc));
    asm volatile("s_getpc_b64 s[40:41]\n\ts_add_u32 s40, s40, _Z6k3_ffn4Ptrs@rel32@lo+4\n\ts_addc_u32 s41, s41, _Z6k3_ffn4Ptrs@rel32@hi+12\n\t"
                 "s_mov_b64 %0, s[40:41]" : "=s"(k1a) :: "s40", "s41", "scc");
    const char* pp = (const char*)k1a + ((b >> 3) * 64 + lane) * 128;
    pp = pp < (const char*)pc ? pp : (const char*)k1a;
    asm volatile("global_load_dword %0, %1, off" : "=&v"(pf_sink) : "v"(pp) : "memory");
  }
  if (id < 576) {
    const int s = id >> 4, h = (id >> 3) & 1, hp0 = (id & 7) * 2;
    const char* src = P.wqkv + (size_t)(16 * s + 8 * h) * 3456 + hp0 * 72;
#pragma unroll
    for (int it = 0; it < 4; ++it) {
      int e = it * 64 + lane; e = e < 216 ? e : 215;
      const int sgm = e / 9, ch = e - sgm * 9;
      dma16x1(src, sgm * 1152 + ch * 16, img_a + it * 1024);
    }
    BAR();
    ST();
    WAITV(0);
    asm volatile("" :: "v"(pf_sink));
    ST();
#pragma unroll
    for (int it = 0; it < 4; ++it) {
      int Q = 16 * it + 4 * g + p; Q = Q < 54 ? Q : 53;
      const int hl = Q >= 27 ? 1 : 0, sg = Q - 27 * hl;
      const int cb = sg < 4 ? 8 * sg : (sg < 8 ? 8 * sg + 8 : (sg == 8 ? 32 : (sg < 18 ? 8 * sg + 72 : 8 * sg + 144)));
      lds_char* a = img + q * 432 + cb + 72 * hl;
      u32x4 v = tr_frag(a, a + 4 * 432);
      const int o = it * 64 + lane, ol = o >= 108 ? 1 : 0, slot = o - 108 * ol;
      const int off = slot < 32 ? WQT_Q + s * 1024 + (h * 32 + slot) * 16 : s * 2432 + (h * 76 + slot - 32) * 16;
      if (o < 216) WS_ST16(P, OFF_WQT + (hp0 + ol) * (int)WQT_HP + off, v, ST_AUX);
    }
  } else if (id < 720) {
    const int j = id - 576, ks = j >> 2, cb = j & 3;
#pragma unroll
    for (int it = 0; it < 5; ++it) {
      int e = it * 64 + lane; e = e < 288 ? e : 287;
      const int kk = e / 18, ch = e - kk * 18, h = kk >> 3, jj = kk & 7;
      const int row = ks < 32 ? 18 * ks + (jj & 3) + 8 * (jj >> 2) + 4 * h : 18 * (8 * (ks - 32) + jj) + 16 + h;
      if (it < 4 || lane < 32) dma16x1(P.wo + cb * 288, row * 1152 + ch * 16, img_a + it * 1024);
    }
    BAR();
    WAITV(0);
#pragma unroll
    for (int it = 0; it < 5; ++it) {
      int G16 = 4 * it + g; G16 = G16 < 18 ? G16 : 17;
      const int h = G16 >= 9 ? 1 : 0, sg = G16 - 9 * h;
      lds_char* a = img + (8 * h + q) * 288 + (16 * sg + 4 * p) * 2;
      u32x4 v = tr_frag(a, a + 4 * 288);
      if (it < 4 || lane < 32) WS_ST16(P, OFF_WOT + cb * (int)WOT_CB + ks * 4672 + (h * 146 + 16 * sg + (lane & 15)) * 16, v, ST_AUX);
    }
  } else if (id < 756) {
    const int row = (id - 720) * 16 + (lane >> 2), i = lane & 3;
    const char* src = P.wo + (size_t)row * 1152 + i * 16;
    u32x4 v[18];
#pragma unroll
    for (int j = 0; j < 18; ++j) v[j] = *(const u32x4*)(src + j * 64);
    BAR();
    float s4[4] = {0.f, 0.f, 0.f, 0.f};
    const bf16x2 one2 = __builtin_bit_cast(bf16x2, 0x3f803f80u);
#pragma unroll
    for (int j = 0; j < 18; ++j)
#pragma unroll
      for (int k = 0; k < 4; ++k) { const unsigned t = v[j][k]; s4[k] = __builtin_amdgcn_fdot2_f32_bf16(__builtin_bit_cast(bf16x2, t), one2, s4[k], false); }
    float sum = (s4[0] + s4[1]) + (s4[2] + s4[3]);
    sum += __shfl_xor(sum, 1); sum += __shfl_xor(sum, 2);
    const float mean = sum * (1.f / D_);
    const int hd = row / 18, e = row % 18;
    const int ks = e < 16 ? hd : 32 + (hd >> 3), jj = e < 16 ? (e & 3) + 4 * (e >> 3) : (hd & 7), hh = e < 16 ? (e >> 2) & 1 : e - 16;
    const unsigned hi = pk2(mean, 0.f) & 0xffffu;
    const unsigned lo = pk2(mean - bflo(hi), 0.f) & 0xffffu;
    unsigned short* d = (unsigned short*)(P.ws + OFF_WOT + (size_t)i * WOT_CB + ks * 4672 + (hh * 146 + 144) * 16 + jj * 2);
    d[0] = (unsigned short)hi; d[8] = (unsigned short)lo;
  } else {
    const u32x4 z = {0u, 0u, 0u, 0u};
    for (int k = id - 756; k < 50; k += 12) WS_ST16(P, OFF_KV + k * 1024 + lane * 16, z, ST_AUX);
    for (int k = id - 756; k < 70; k += 12) WS_ST16(P, OFF_FLAG + k * 1024 + lane * 16, z, ST_AUX);
    BAR();
  }
}

extern "C" void kernel_launch(void* const* d_in, const int* in_sizes, int n_in, void* d_out, int out_size, void* d_ws,
                              size_t ws_size, hipStream_t stream) {
  Ptrs P;
  P.x = (const char*)d_in[0]; P.wqkv = (const char*)d_in[1]; P.wo = (const char*)d_in[2];
  P.wg = (const char*)d_in[4]; P.wu = (const char*)d_in[5]; P.wd = (const char*)d_in[6];
  P.ws = (char*)d_ws; P.out = (char*)d_out;
  (void)hipFuncSetAttribute((const void*)k3_ffn, hipFuncAttributeMaxDynamicSharedMemorySize, K3_LDS);
  k0_prep<<<256, 768, K0_LDS, stream>>>(P);
  k3_ffn<<<256, 512, K3_LDS, stream>>>(P);
}
```

```cpp
#include <hip/hip_runtime.h>
#include <hip/hip_bf16.h>
#include <stdint.h>
#include <type_traits>

typedef __attribute__((ext_vector_type(8))) short bf16x8;
typedef __attribute__((ext_vector_type(4))) short s16x4;
typedef __attribute__((ext_vector_type(16))) float f32x16;
typedef __attribute__((ext_vector_type(4))) float f32x4;
typedef __attribute__((ext_vector_type(2))) float f32x2;
typedef __attribute__((ext_vector_type(2))) __bf16 bf16x2;
typedef __attribute__((ext_vector_type(4))) unsigned u32x4;
typedef __attribute__((ext_vector_type(2))) unsigned u32x2;
typedef __attribute__((address_space(3))) char lds_char;
typedef __attribute__((address_space(3))) s16x4 lds_s16x4;
typedef __attribute__((address_space(3))) u32x4 lds_u32x4;
typedef __attribute__((address_space(3))) u32x2 lds_u32x2;
typedef __attribute__((address_space(3))) f32x4 lds_f32x4;
typedef __attribute__((address_space(3))) float lds_float;
typedef __attribute__((address_space(3))) f32x2 lds_f32x2;
typedef __attribute__((address_space(3))) unsigned lds_uint;

#define DI __device__ __forceinline__
#define MFMA32(a, b, c) __builtin_amdgcn_mfma_f32_32x32x16_bf16((a), (b), (c), 0, 0, 0)
#define WAITV(n) asm volatile("s_waitcnt vmcnt(" #n ")" ::: "memory")
#define WAITL0() asm volatile("s_waitcnt lgkmcnt(0)" ::: "memory")
#define BAR() asm volatile("s_barrier" ::: "memory")
#define SB0() __builtin_amdgcn_sched_barrier(0)

constexpr int S_ = 2048, D_ = 576, F_ = 4608;
constexpr float LN_EPS = 1e-5f;

constexpr size_t FLB = (size_t)S_ * D_ * 2;
constexpr size_t WQT_HP = 36 * 3456;
constexpr int WQT_Q = 36 * 2432;
constexpr size_t WOT_CB = 36 * 4672;
constexpr size_t OFF_XN = 0;
constexpr size_t OFF_PQ = OFF_XN + FLB;
constexpr size_t OFF_HC = OFF_PQ + FLB;
constexpr size_t OFF_MEANX = OFF_HC + FLB;
constexpr size_t OFF_MEANH = OFF_MEANX + 8192;
constexpr size_t OFF_SSQ = OFF_MEANH + 8192;
constexpr size_t OFF_KV = OFF_SSQ + 20 * 8192 + 4096;
constexpr size_t OFF_WQT = OFF_KV + 53248;
constexpr size_t OFF_WOT = OFF_WQT + 16 * WQT_HP + 16384;
constexpr size_t OFF_XH = OFF_WOT + 4 * WOT_CB + 4096;
constexpr size_t OFF_FLAG = OFF_XH + 256 * 73728;
constexpr size_t OFF_CNT = OFF_FLAG + 256 * 128;
constexpr size_t OFF_G1 = OFF_CNT + 32 * 128;
constexpr size_t OFF_G3 = OFF_G1 + 256 * 128;
constexpr size_t WS_END = OFF_G3 + 16 * 128;
static_assert(OFF_XH % 1024 == 0 && (WS_END - OFF_FLAG) == 70 * 1024, "workspace");

#define ST_DECL
#define ST()
#define ST_OUT(k, slot)
DI unsigned pk2(float a, float b) {
  f32x2 v = {a, b};
  bf16x2 r = __builtin_convertvector(v, bf16x2);
  return __builtin_bit_cast(unsigned, r);
}
DI float bflo(unsigned u) { return __uint_as_float(u << 16); }
DI float bfhi(unsigned u) { return __uint_as_float(u & 0xffff0000u); }
DI float phi(float t) {
  const float e = __builtin_amdgcn_fmed3f(__builtin_amdgcn_exp2f(t * 1.4426950408889634f), 0.f, 1.f);
  return (__builtin_amdgcn_fmed3f(t, 0.f, __builtin_inff()) + 1e-6f) + e;
}
DI bf16x8 as_frag(u32x4 v) { return __builtin_bit_cast(bf16x8, v); }

DI void dma16x3s(const void* sbase, unsigned voff, unsigned lds_dst) {
  unsigned keep;
  asm volatile("s_mov_b32 %0, m0\n\ts_mov_b32 m0, %3\n\ts_nop 0\n\t"
               "global_load_lds_dwordx4 %1, %2 sc1\n\t"
               "global_load_lds_dwordx4 %1, %2 offset:1024 sc1\n\t"
               "global_load_lds_dwordx4 %1, %2 offset:2048 sc1\n\t"
               "s_mov_b32 m0, %0"
               : "=&s"(keep) : "v"(voff), "s"(sbase), "s"(lds_dst) : "memory");
}
DI void dma16x1(const void* sbase, unsigned voff, unsigned lds_dst) {
  unsigned keep;
  asm volatile("s_mov_b32 %0, m0\n\ts_mov_b32 m0, %3\n\ts_nop 0\n\t"
               "global_load_lds_dwordx4 %1, %2\n\t"
               "s_mov_b32 m0, %0"
               : "=&s"(keep) : "v"(voff), "s"(sbase), "s"(lds_dst) : "memory");
}
DI u32x4 tr_frag(lds_char* p0, lds_char* p1) {
  s16x4 lo = __builtin_amdgcn_ds_read_tr16_b64_v4i16((lds_s16x4*)p0);
  s16x4 hi = __builtin_amdgcn_ds_read_tr16_b64_v4i16((lds_s16x4*)p1);
  u32x2 a = __builtin_bit_cast(u32x2, lo), b = __builtin_bit_cast(u32x2, hi);
  u32x4 r = {a[0], a[1], b[0], b[1]};
  return r;
}

#define ST_AUX 16
#define WS_RSRC(P) __builtin_amdgcn_make_buffer_rsrc((P).ws, 0, (int)WS_END, 0x00020000)
#define WS_ST16(P, off, v, aux) __builtin_amdgcn_raw_buffer_store_b128((v), WS_RSRC(P), (int)(off), 0, (aux))
#define WS_ST8(P, off, v, aux) __builtin_amdgcn_raw_buffer_store_b64((v), WS_RSRC(P), (int)(off), 0, (aux))
#define WS_ST4(P, off, v, aux) __builtin_amdgcn_raw_buffer_store_b32((v), WS_RSRC(P), (int)(off), 0, (aux))
template <class R> DI void st128(u32x4 v, R rsrc, int voff, int soff) {
  __builtin_amdgcn_raw_buffer_store_b128(v, rsrc, voff, soff, 16);
  asm volatile("s_nop 1" :: "v"(v));
}
struct Ptrs {
  const char* x; const char* wqkv; const char* wo; const char* wg; const char* wu; const char* wd;
  char* ws; char* out;
};

#define TSTRIDE 2097152
constexpr int G1R = 16;

constexpr int K1_W = 0;
constexpr int KVP = 192;
constexpr int K2_O = 49152;
constexpr int K2_RED = K2_O + 36864;
constexpr int K2_MEAN = K2_RED + 32768;

DI void poll_counters(const char* base, const int off, const unsigned need) {
#pragma unroll 1
  for (int spin = 0; spin < (1 << 22); ++spin) {
    unsigned v;
    asm volatile("global_load_dword %0, %1, %2 sc1\n\ts_waitcnt vmcnt(0)" : "=&v"(v) : "v"(off), "s"(base) : "memory");
    if (__builtin_amdgcn_ballot_w64(v < need) == 0ull) break;
    __builtin_amdgcn_s_sleep(2);
  }
}

constexpr int K3_RING = 0;
constexpr int K3_HH = 73728;
constexpr int K3_SSQ = K3_HH + 73728;
constexpr int K3_LDS = K3_SSQ + 4 * 3072;
constexpr int EBAR = 156, XOFF = 1;
constexpr int NS = 486, PH = 288, P2 = 324;

struct Sch {
  signed char own[162] = {}, u[162] = {}, db[162] = {}, t01[162] = {};
  short rs[NS + 4] = {}, lo[NS + 4] = {};
  signed char sown[90] = {}, su[90] = {}, sdb[90] = {}, pf[NS] = {};
  bool bar[NS] = {};
  signed char dslot[NS] = {}, dk[NS] = {}, prio[NS] = {};
  signed char e0[NS] = {}, ne0[NS] = {}, ne1[NS] = {}, eg[NS] = {};
  signed char og[NS] = {}, o0[NS] = {};
  signed char xrd[NS] = {}, cbar[NS] = {}, xst[NS] = {}, ost[NS] = {}, xop[NS] = {};
  signed char flag[NS] = {}, fbar[NS] = {}, poll[NS] = {}, chk[NS] = {}, ld[NS] = {}, wr[NS] = {}, rd[NS] = {};
  signed char wl[NS] = {}, wf[NS] = {};
  int ebar = 0;
  bool ok = true;
};
constexpr Sch make_sch() {
  Sch s;
  int n2 = 0, nb = 0;
  auto put = [&](int own, int u, int db) {
    s.sown[nb] = own; s.su[nb] = u; s.sdb[nb] = db;
    for (int t = 0; t < (db == 4 ? 1 : 2); ++t) {
      s.own[n2] = own; s.u[n2] = u; s.db[n2] = db; s.t01[n2] = t;
      s.rs[P2 + n2] = 27 + nb / 6; s.lo[P2 + n2] = ((27 + nb / 6) % 3) * 24 + (nb % 6) * 4; ++n2;
    }
    ++nb;
  };
  for (int u = 0; u < 9; ++u) for (int d = 0; d < 5; ++d) put(1, u, d);
  for (int u = 0; u < 6; ++u) { put(0, u, 4); put(0, u, 0); put(0, u, 1); }
  for (int u = 6; u < 9; ++u) { put(0, u, 4); put(0, u, 0); }
  for (int u = 6; u < 9; ++u) put(0, u, 1);
  for (int d = 2; d < 4; ++d) for (int u = 0; u < 9; ++u) put(0, u, d);
  for (int n = 0; n < PH; ++n) { s.rs[n] = n / 12; s.lo[n] = ((n / 12) % 3) * 24 + ((n % 12) >> 1) * 4; }
  for (int n = PH; n < P2; ++n) { s.rs[n] = n / 12; s.lo[n] = ((n / 12) % 3) * 24 + (n % 12) * 2; }
  for (int n = NS; n < NS + 4; ++n) { s.rs[n] = s.rs[NS - 1]; s.lo[n] = s.lo[NS - 1]; }
  for (int n = 0; n < NS; ++n) {
    s.bar[n] = s.rs[n + 3] != s.rs[n + 2];
    s.dslot[n] = -1; s.prio[n] = -1; s.eg[n] = -1; s.og[n] = -1; s.flag[n] = -1; s.fbar[n] = -1; s.poll[n] = 0; s.chk[n] = -1;
    s.ld[n] = -1; s.rd[n] = -1; s.wl[n] = -1; s.wf[n] = -1; s.xst[n] = -1; s.ost[n] = -1; s.xrd[n] = -1; s.cbar[n] = -1; s.pf[n] = 0;
  }
  for (int n = 0; n < NS; ++n) if (s.bar[n]) {
    int e = n + 1; while (e < NS && !s.bar[e]) ++e;
    const int L = e - n, tgt = s.rs[n + 3] + 2;
    if (tgt <= 41) for (int k = 0; k < 6; ++k) { s.dslot[n + (k * L) / 6 + 1] = tgt; s.dk[n + (k * L) / 6 + 1] = k; if ((k * L) / 6 + 1 >= L) s.ok = false; }
    if (e < NS) { s.prio[n + L / 2] = 2; s.prio[e - 3] = 1; s.prio[e - 1] = 0; }
  }
  auto nbar = [&](int n) { while (!s.bar[n]) ++n; return n; };
  for (int g = 0; g < 5; ++g) {
    const int start = (g < 4 ? 72 * (g + 1) : P2) + 2, tot = g == 4 ? 46 : 92;
    int e = 0;
    for (int n = start; e < tot; ++n) {
      const int c0 = g == 4 ? 2 : 1, c1 = g >= 3 ? 2 : ((n - start) & 1);
      if (s.eg[n] >= 0) s.ok = false;
      s.eg[n] = g; s.e0[n] = e; s.ne0[n] = c0; s.ne1[n] = c1;
      for (int i = e; i < e + c0 + c1 && i < tot; ++i) if (i % 46 == 45) { int m = n + 1; while (!s.bar[m]) ++m; m += XOFF; while (s.xrd[m] >= 0) m += 3; s.xrd[m] = 2 * g + i / 46; s.xst[m + 2] = 2 * g + i / 46; }
      e += c0 + c1;
      if (g < 3 && n >= start + 68) s.ok = false;
    }
  }
  s.og[P2 + 119] = 4; s.o0[P2 + 119] = 0; s.og[P2 + 120] = 4; s.o0[P2 + 120] = 4; s.ost[P2 + 120] = 8; s.ost[P2 + 122] = 9;
  const int opk[3] = {121, 128, 145};
  for (int k = 0; k < 3; ++k) {
    for (int i = 0; i < 4; ++i) { const int n = P2 + opk[k] + i; s.og[n] = k; s.o0[n] = 2 * i; }
    s.ost[P2 + opk[k] + 3] = 2 * k; s.ost[P2 + opk[k] + 6] = 2 * k + 1;
  }
  int lds_[9] = {16, 18, 24, 34, 43, 44, 76, 78, 80}, rds[9] = {};
  for (int u = 0; u < 9; ++u) {
    const int cbn = nbar(P2 + lds_[(u / 3) * 3 + 2] + 17);
    rds[u] = cbn - P2 + 1 + (u < 6 ? 3 : 2) * (u % 3);
    s.ld[P2 + lds_[u]] = u; s.rd[P2 + rds[u]] = u; s.cbar[cbn] = u;
  }
  s.poll[P2 + 3] = 1; s.chk[P2 + 15] = 6;
  s.poll[P2 + 63] = 1; s.chk[P2 + 75] = 9;
  s.xop[28] = 3; s.xop[40] = 3;
  s.pf[62] = 1; s.pf[110] = 2; s.pf[160] = 3;
  const int fb[2] = {PH + 21, nbar(P2 + 40)}, fv[2] = {6, 9};
  for (int k = 0; k < 2; ++k) { if (!s.bar[fb[k]]) s.ok = false; s.fbar[fb[k]] = fv[k]; s.flag[fb[k] + 1] = fv[k]; }
  s.ebar = P2 + EBAR;
  int cl[NS + 1] = {}, cf[NS + 1] = {};
  for (int n = 9; n < NS; ++n) {
    const int both = (s.ost[n] >= 0 ? 1 : 0) + s.xop[n];
    cl[n + 1] = cl[n] + both + (s.dslot[n] >= 0 ? 1 : 0);
    cf[n + 1] = cf[n] + both + (s.xst[n] >= 0 ? 2 : 0) + (s.flag[n] >= 0 ? 1 : 0) + s.poll[n] + (s.ld[n] >= 0 ? 2 : 0) + (s.pf[n] ? 2 : 0);
  }
  auto need = [&](int n, int ul_, int uf_) {
    if (ul_ >= 0) { const int w = cl[n] - ul_; if (s.wl[n] < 0 || w < s.wl[n]) s.wl[n] = w; }
    if (uf_ >= 0) { const int w = cf[n] - uf_; if (s.wf[n] < 0 || w < s.wf[n]) s.wf[n] = w; }
  };
  for (int n = 21; n < NS; ++n) {
    if (s.bar[n]) {
      int last = -1;
      for (int i = 9; i < n; ++i) if (s.dslot[i] == s.rs[n + 3]) last = i;
      need(n, last >= 0 ? cl[last] + 1 : 0, n == 21 ? 0 : -1);
      if (n == 45) need(n, cl[29], cf[29]);
      if (n == 57) need(n, cl[41], cf[41]);
      if (s.fbar[n] >= 0) {
        int at = -1;
        for (int i = 9; i < n; ++i) if (s.xst[i] == s.fbar[n] - 1) at = i;
        if (at < 0) s.ok = false;
        for (int i = 9; i < n; ++i) if (s.xst[i] >= 0 && s.xst[i] < s.fbar[n] && i > at) at = i;
        need(n, -1, cf[at + 1]);
      }
    }
    if (n == s.ebar) need(n, cl[P2 + 135], cf[P2 + 135]);
    if (s.chk[n] >= 0) { int i = n; while (!s.poll[i]) --i; need(n, -1, cf[i + 1]); }
    if (s.cbar[n] >= 0) { int i = n; while (s.ld[i] != s.cbar[n]) --i; need(n, -1, cf[i + 1]); }
  }
  for (int u = 0; u < 9; ++u) {
    int own_rd = u == 0 ? P2 - 1 : P2 + 9 * (u - 1) + 4;
    for (int n = 0; n < NS; ++n) if (s.xrd[n] == u && n > own_rd) own_rd = n;
    bool b1 = false;
    for (int n = own_rd + 1; n <= P2 + lds_[u]; ++n) b1 = b1 || s.bar[n];
    int use = 81; while (s.u[use] != u) ++use;
    if (!b1 || rds[u] + 2 > use) s.ok = false;
  }
  return s;
}
constexpr Sch SCH = make_sch();
static_assert(SCH.ok, "schedule");

template <int I, int N, class F> DI void static_for(F&& f) {
  if constexpr (N - I == 1) f(std::integral_constant<int, I>{});
  else if constexpr (N - I > 1) { static_for<I, (I + N) / 2>(f); static_for<(I + N) / 2, N>(f); }
}
template <int N> DI void waitv() { asm volatile("s_waitcnt vmcnt(%0)" :: "n"(N) : "memory"); }
#define MFMA_DRAIN() do { asm volatile("s_nop 7\n\ts_nop 3" ::: "memory"); SB0(); } while (0)
typedef __attribute__((ext_vector_type(4))) int srd_t;
DI srd_t make_srd(const void* p, unsigned bytes) {
  const unsigned long long a = (unsigned long long)p;
  srd_t r = {(int)(unsigned)a, (int)((unsigned)(a >> 32) & 0xffffu), (int)bytes, 0x00020000};
  return r;
}
template <int LOFF, int SOFF> DI void bdma_i(srd_t srd, unsigned voff, unsigned sbase, unsigned lbase) {
  unsigned so;
  asm volatile("s_add_u32 m0, %4, %6\n\ts_add_u32 %0, %3, %5\n\tbuffer_load_dwordx4 %1, %2, %0 offen lds"
               : "=&s"(so) : "v"(voff), "s"(srd), "s"(sbase), "s"(lbase), "n"(SOFF), "n"(LOFF) : "memory", "scc");
}

template <int OFF> DI void bdma_sc1(srd_t srd, unsigned voff, unsigned lbase) {
  unsigned so;
  asm volatile("s_add_u32 m0, %3, %4\n\ts_mov_b32 %0, %4\n\tbuffer_load_dwordx4 %1, %2, %0 offen sc1 lds"
               : "=&s"(so) : "v"(voff), "s"(srd), "s"(lbase), "n"(OFF) : "memory", "scc");
}

template <int FJ>
DI void k3_body(const Ptrs& P, lds_char* lds, const int lane, const int mi, const int b, const int c, const int mb) {
  constexpr int fj = FJ;
  const unsigned lds_a = (unsigned)(uintptr_t)lds;
  const int mt = mb * 4 + mi, p = c & 1;
  const int g = lane >> 4;
  ST_DECL; ST();
  const unsigned voff = lane * 16;
  const srd_t sd = make_srd(P.wd, D_ * F_ * 2);
  const srd_t sg = make_srd(P.wg, D_ * F_ * 2), su = make_srd(P.wu, D_ * F_ * 2);
  const auto wsr = WS_RSRC(P);
  const int wid = fj * 4 + mi;
  const unsigned sgu = 8 * mi * (F_ * 2) + 576 * c + 64 * p;
  const unsigned ring_w = lds_a + K3_RING + mi * 1024;
  auto vgu_f = [&]() { return (unsigned)((lane >> 3) * (F_ * 2) + ((((lane >> 1) & 3) ^ (((lane >> 4) & 1) | ((mi & 1) << 1))) * 32) + (lane & 1) * 16); };
  auto first_slot = [&](auto tc) __attribute__((always_inline)) {
    constexpr int t = decltype(tc)::value;
    static_for<3 * FJ, 3 * FJ + 3>([&](auto kc) __attribute__((always_inline)) {
      constexpr int k = decltype(kc)::value;
      bdma_i<t * 24576 + k * 4096, 32 * (3 * t + (k >> 1)) * (F_ * 2)>((k & 1) ? su : sg, vgu_f(), sgu, ring_w);
    });
  };
  auto pf_gu = [&](unsigned& sink, const int i, const int col) {
    const int l36 = lane < 36 ? lane : 0, mat = l36 >= 18 ? 1 : 0, row = ((mb & 7) * 4 + i) * 18 + l36 - 18 * mat;
    const char* pa = (mat ? P.wu : P.wg) + (size_t)row * (F_ * 2) + 576 * c + 64 * p + col;
    asm volatile("global_load_dword %0, %1, off\n\tglobal_load_dword %0, %1, off offset:128" : "+v"(sink) : "v"(pa) : "memory");
  };
  {
    const int r = lane & 31, h = lane >> 5;
    const int hp = c;
    const int mt2 = mb * 4 + (c >> 2), cb = c & 3;
    u32x4 af[18], a4[5], xv0, xv1;
    float mean;
    const int k4 = FJ == 0 ? 5 * mi : 20 + 4 * mi;
    constexpr int n4 = FJ == 0 ? 5 : 4;
    const int row = mt2 * 32 + r, s0 = cb * 9 + 2 * (FJ == 0 ? mi : 4);
    auto prefetch2 = [&]() __attribute__((always_inline)) {
      const char* wo = P.ws + OFF_WOT + (size_t)cb * WOT_CB + h * 2336;
#pragma unroll
      for (int i = 0; i < 18; ++i) af[i] = *(const u32x4*)(wo + (18 * FJ + i) * 4672 + (32 * mi + r) * 16);
#pragma unroll
      for (int i = 0; i < n4; ++i) a4[i] = *(const u32x4*)(wo + (k4 + i) * 4672 + (128 + (r < 17 ? r : 17)) * 16);
      xv0 = *(const u32x4*)(P.x + (size_t)row * 1152 + s0 * 32 + h * 16);
      xv1 = *(const u32x4*)(P.x + (size_t)row * 1152 + (FJ == 0 ? s0 + 1 : s0) * 32 + h * 16);
      mean = ((const float*)(P.ws + OFF_MEANX))[row];
      SB0();
    };
    unsigned sink = 0;
    if constexpr (FJ == 1) {
      const char* wsrc = P.ws + OFF_WQT + (size_t)hp * WQT_HP + mi * 1024;
      const unsigned wdst = lds_a + K1_W + mi * 1024;
#pragma unroll
      for (int i = 0; i < 14; ++i) dma16x1(wsrc + i * 4096, voff, wdst + i * 4096);
      WAITV(8); BAR();
#pragma unroll
      for (int i = 14; i < 20; ++i) dma16x1(wsrc + i * 4096, voff, wdst + i * 4096);
      WAITV(9); BAR();
#pragma unroll
      for (int i = 20; i < 26; ++i) dma16x1(wsrc + i * 4096, voff, wdst + i * 4096);
      WAITV(9); BAR();
#pragma unroll
      for (int i = 26; i < 31; ++i) dma16x1(wsrc + i * 4096, voff, wdst + i * 4096);
      WAITV(9); BAR();
      WAITV(0);
      prefetch2();
      if (mi >= 2) { pf_gu(sink, (mi & 1) * 2, 0); pf_gu(sink, (mi & 1) * 2 + 1, 0); }
      BAR();
      ST();
      if (mi < 2) {
        __builtin_amdgcn_s_setprio(3);
        const int q = (lane >> 2) & 3, op = 4 * (g & 1) + (lane & 3);
        const int vq = op >= 5 ? 19 : (mi ? 14 : 10) + op, kq = (mi ? 5 : 1) + op;
        lds_char* va = lds + (8 * (g >> 1) + q) * KVP + vq * 8;
        lds_char* ka = lds + (8 * (g >> 1) + q) * KVP + kq * 8;
        f32x16 kv;
#pragma unroll
        for (int i = 0; i < 16; ++i) kv[i] = 0.f;
#pragma unroll
        for (int ks = 0; ks < 8; ++ks) {
          u32x4 a = tr_frag(va + ks * 16 * KVP, va + ks * 16 * KVP + 4 * KVP);
          u32x4 bb = tr_frag(ka + ks * 16 * KVP, ka + ks * 16 * KVP + 4 * KVP);
          kv = MFMA32(as_frag(a), as_frag(bb), kv);
        }
        const int d = r - 2 * mi;
        const int dd = mi ? (d >= 2 ? d - 2 : 16 + d) : d;
        float* dst = (float*)(P.ws + OFF_KV) + (2 * hp + mi) * 400 + dd;
        const bool dok = d >= 0 && d < 18;
#pragma unroll
        for (int i = 0; i < 12; ++i) {
          const int rw = (i & 3) + 8 * (i >> 2) + 4 * h;
          const int e = rw == 20 ? 18 : rw - 2 * mi;
          if (dok && (rw == 20 || (e >= 0 && e < 18 && rw < 20))) atomicAdd(dst + e * 20, kv[i]);
        }
        WAITV(0);
      }
    } else {
      u32x4 xf[36];
      const char* xsrc = P.ws + OFF_XN + (size_t)mt * 36864 + lane * 16;
#pragma unroll
      for (int s = 0; s < 18; ++s) xf[s] = *(const u32x4*)(xsrc + s * 1024);
      f32x16 acc[3], accq;
#pragma unroll
      for (int i = 0; i < 16; ++i) { acc[0][i] = 0.f; acc[1][i] = 0.f; acc[2][i] = 0.f; accq[i] = 0.f; }
      lds_char* wa = lds + K1_W + (h * 76 + r) * 16;
      lds_char* wa2 = lds + K1_W + (h * 76 + 64 + (r < 12 ? r : 11)) * 16;
      u32x4 wf[2][3];
#pragma unroll
      for (int s = 0; s < 36; ++s) {
        if (s % 9 == 0) {
          BAR();
          if (s < 18) {
#pragma unroll
            for (int k = s + 18; k < s + 27; ++k) xf[k] = *(const u32x4*)(xsrc + k * 1024);
          }
          wf[s & 1][0] = *(lds_u32x4*)(wa + s * 2432); wf[s & 1][1] = *(lds_u32x4*)(wa + s * 2432 + 512); wf[s & 1][2] = *(lds_u32x4*)(wa2 + s * 2432);
        }
        if (s % 9 != 8) {
          wf[(s + 1) & 1][0] = *(lds_u32x4*)(wa + (s + 1) * 2432); wf[(s + 1) & 1][1] = *(lds_u32x4*)(wa + (s + 1) * 2432 + 512);
          wf[(s + 1) & 1][2] = *(lds_u32x4*)(wa2 + (s + 1) * 2432);
        }
        SB0();
#pragma unroll
        for (int t = 0; t < 3; ++t) acc[t] = MFMA32(as_frag(wf[s & 1][t]), as_frag(xf[s]), acc[t]);
        SB0();
      }
      ST();
      lds_char* kvrow = lds + (mi * 32 + r) * KVP;
      u32x2 qrem;
#pragma unroll
      for (int gi = 0; gi < 10; ++gi) {
        const int t = gi >> 2, gg = gi & 3;
        u32x2 o;
        if (gi < 5) { o[0] = pk2(phi(acc[t][4 * gg]), phi(acc[t][4 * gg + 1])); o[1] = pk2(phi(acc[t][4 * gg + 2]), phi(acc[t][4 * gg + 3])); }
        else { o[0] = pk2(acc[t][4 * gg], acc[t][4 * gg + 1]); o[1] = pk2(acc[t][4 * gg + 2], acc[t][4 * gg + 3]); }
        if (gi == 9 && h == 1) { o[0] = 0x3f803f80u; o[1] = 0x3f803f80u; }
        *(lds_u32x2*)(kvrow + (2 * gi + h) * 8) = o;
        if (gi == 0) qrem = o;
      }
      WAITL0();
      BAR();
      lds_char* wq = lds + K1_W + WQT_Q + lane * 16;
      u32x4 aq[2][4];
#pragma unroll
      for (int i = 0; i < 4; ++i) aq[0][i] = *(lds_u32x4*)(wq + i * 1024);
#pragma unroll
      for (int g3 = 0; g3 < 9; ++g3) {
        if (g3 < 8) {
#pragma unroll
          for (int i = 0; i < 4; ++i) aq[(g3 + 1) & 1][i] = *(lds_u32x4*)(wq + (4 * g3 + 4 + i) * 1024);
        }
        SB0();
#pragma unroll
        for (int i = 0; i < 4; ++i) accq = MFMA32(as_frag(aq[g3 & 1][i]), as_frag(xf[4 * g3 + i]), accq);
        SB0();
      }
      const int pq = (int)OFF_PQ + mt * 36864;
#pragma unroll
      for (int e = 0; e < 2; ++e) {
        u32x4 o;
#pragma unroll
        for (int k = 0; k < 4; ++k) o[k] = pk2(phi(accq[8 * e + 2 * k]), phi(accq[8 * e + 2 * k + 1]));
        WS_ST16(P, pq + (2 * hp + e) * 1024 + lane * 16, o, 16);
      }
      if (h == 0) WS_ST8(P, pq + (32 + (hp >> 2)) * 1024 + (r + 32 * ((hp >> 1) & 1)) * 16 + 8 * (hp & 1), qrem, 16);
      WAITV(0);
    }
    BAR();
    ST();
    if (FJ == 1 && mi == 0) {
      constexpr int NSH = 256 / G1R;
      if (lane < G1R) __hip_atomic_fetch_add((unsigned*)(P.ws + OFF_G1 + (lane * NSH + (b % NSH)) * 128), 1u, __ATOMIC_RELAXED, __HIP_MEMORY_SCOPE_AGENT);
      poll_counters(P.ws, (int)OFF_G1 + ((b / NSH) * NSH + (lane % NSH)) * 128, (unsigned)G1R);
    }
    if constexpr (FJ == 0) { __builtin_amdgcn_s_sleep(4); prefetch2(); }
    BAR();
    ST();
    const int e = r < 16 ? r : (r == 16 ? 16 : (r == 20 ? 17 : 18));
    const bool rok = r <= 17 || r == 20 || r == 21;
    u32x4 k0[4], k1[4], qf[4]; u32x2 k2[4];
    dma16x3s(P.ws + OFF_KV + wid * 6400, voff, lds_a + wid * 6144);
    dma16x3s(P.ws + OFF_KV + wid * 6400 + 3072, voff, lds_a + wid * 6144 + 3072);
    const u32x4 ktail = __builtin_amdgcn_raw_buffer_load_b128(wsr, (lane & 15) * 16, (int)OFF_KV + wid * 6400 + 6144, 16);
#pragma unroll
    for (int i = 0; i < 4; ++i) qf[i] = __builtin_amdgcn_raw_buffer_load_b128(wsr, voff, (int)OFF_PQ + mt2 * 36864 + (4 * wid + i) * 1024, 16);
    const u32x4 qr = __builtin_amdgcn_raw_buffer_load_b128(wsr, voff, (int)OFF_PQ + mt2 * 36864 + (32 + (wid >> 1)) * 1024, 16);
    *(lds_u32x4*)(lds + K2_RED + wid * 256 + (lane & 15) * 16) = ktail;
    asm volatile("" ::: "memory");
    SB0();
#pragma unroll
    for (int i = 0; i < 4; ++i) {
      const int o = i * 1600 + e * 80;
      lds_char* kp0 = lds + (o + 16 * h < 6144 ? wid * 6144 + o + 16 * h : K2_RED + wid * 256 + o + 16 * h - 6144);
      lds_char* kp1 = lds + (o + 16 * h + 32 < 6144 ? wid * 6144 + o + 16 * h + 32 : K2_RED + wid * 256 + o + 16 * h + 32 - 6144);
      lds_char* kp2 = lds + (o + 64 < 6144 ? wid * 6144 + o + 64 : K2_RED + wid * 256 + o + 64 - 6144);
      k0[i] = *(lds_u32x4*)kp0; k1[i] = *(lds_u32x4*)kp1; k2[i] = *(lds_u32x2*)kp2;
    }
    float orem[4];
#define F_(x) __uint_as_float(x)
#pragma unroll
    for (int i = 0; i < 4; ++i) {
      u32x4 a1 = {pk2(F_(k0[i][0]), F_(k0[i][1])), pk2(F_(k0[i][2]), F_(k0[i][3])), pk2(F_(k1[i][0]), F_(k1[i][1])), pk2(F_(k1[i][2]), F_(k1[i][3]))};
      u32x4 a2 = {0u, 0u, 0u, 0u};
      a2[i] = (h == (mi & 1)) ? pk2(F_(k2[i][0]), F_(k2[i][1])) : 0u;
      if (!rok) { a1 = (u32x4){0u, 0u, 0u, 0u}; a2[i] = 0u; }
      f32x16 cc;
#pragma unroll
      for (int j = 0; j < 16; ++j) cc[j] = 0.f;
      cc = MFMA32(as_frag(a1), as_frag(qf[i]), cc);
      cc = MFMA32(as_frag(a2), as_frag(qr), cc);
      const float inv = __builtin_amdgcn_rcpf(cc[9] + 1e-6f);
      float o[9];
#pragma unroll
      for (int j = 0; j < 9; ++j) o[j] = cc[j] * inv;
      u32x4 of = {pk2(o[0], o[1]), pk2(o[2], o[3]), pk2(o[4], o[5]), pk2(o[6], o[7])};
      *(lds_u32x4*)(lds + K2_O + (4 * wid + i) * 1024 + lane * 16) = of;
      orem[i] = o[8];
    }
#undef F_
    {
      u32x2 o2 = {pk2(orem[0], orem[1]), pk2(orem[2], orem[3])};
      *(lds_u32x2*)(lds + K2_O + (32 + (wid >> 1)) * 1024 + lane * 16 + 8 * (mi & 1)) = o2;
    }
    WAITL0();
    BAR();
    ST();
    f32x16 acc, acc4;
#pragma unroll
    for (int j = 0; j < 16; ++j) { acc[j] = 0.f; acc4[j] = 0.f; }
    lds_char* ob = lds + K2_O + lane * 16;
#pragma unroll
    for (int i = 0; i < n4; ++i) acc4 = MFMA32(as_frag(a4[i]), as_frag(*(lds_u32x4*)(ob + (k4 + i) * 1024)), acc4);
#pragma unroll
    for (int i = 0; i < 18; ++i) acc = MFMA32(as_frag(af[i]), as_frag(*(lds_u32x4*)(ob + (18 * FJ + i) * 1024)), acc);
    {
      *(lds_float*)(lds + K2_MEAN + (wid * 64 + lane) * 4) = acc4[8] + acc4[9];
      lds_char* p4 = lds + K2_RED + 16384 + wid * 2048 + lane * 16;
      *(lds_f32x4*)(p4) = (f32x4){acc4[0], acc4[1], acc4[2], acc4[3]};
      *(lds_f32x4*)(p4 + 1024) = (f32x4){acc4[4], acc4[5], acc4[6], acc4[7]};
      if (FJ == 1) {
        lds_char* pr = lds + K2_RED + mi * 4096 + lane * 16;
#pragma unroll
        for (int k = 0; k < 4; ++k) *(lds_f32x4*)(pr + k * 1024) = (f32x4){acc[4 * k], acc[4 * k + 1], acc[4 * k + 2], acc[4 * k + 3]};
      }
    }
    WAITL0();
    BAR();
    if (FJ == 0 || mi == 0) {
#pragma unroll
      for (int w = 0; w < 8; ++w) mean += *(lds_float*)(lds + K2_MEAN + (w * 64 + r) * 4);
      if constexpr (FJ == 0) {
        lds_char* pr = lds + K2_RED + mi * 4096 + lane * 16;
#pragma unroll
        for (int k = 0; k < 4; ++k) {
          f32x4 v = *(lds_f32x4*)(pr + k * 1024);
#pragma unroll
          for (int j = 0; j < 4; ++j) acc[4 * k + j] += v[j];
        }
      } else {
#pragma unroll
        for (int j = 0; j < 8; ++j) acc[j] = 0.f;
#pragma unroll
        for (int w = 0; w < 8; ++w) {
          lds_char* p4 = lds + K2_RED + 16384 + w * 2048 + lane * 16;
          f32x4 v0 = *(lds_f32x4*)(p4), v1 = *(lds_f32x4*)(p4 + 1024);
#pragma unroll
          for (int j = 0; j < 4; ++j) { acc[j] += v0[j]; acc[4 + j] += v1[j]; }
        }
      }
      float ssq = 0.f;
#pragma unroll
      for (int i = 0; i < (FJ == 0 ? 2 : 1); ++i) {
        float v[8];
#pragma unroll
        for (int j = 0; j < 4; ++j) {
          auto sw2 = __builtin_amdgcn_permlane32_swap(__float_as_uint(acc[8 * i + j]), __float_as_uint(acc[8 * i + 4 + j]), false, false);
          v[j] = __uint_as_float(sw2[0]); v[4 + j] = __uint_as_float(sw2[1]);
        }
        const u32x4 xv = i ? xv1 : xv0;
        u32x4 o;
#pragma unroll
        for (int k = 0; k < 4; ++k) {
          const float a0 = v[2 * k] + bflo(xv[k]) - mean, a1 = v[2 * k + 1] + bfhi(xv[k]) - mean;
          ssq += a0 * a0 + a1 * a1;
          o[k] = pk2(a0, a1);
        }
        const int d8 = 2 * (s0 + i) + h;
        WS_ST16(P, OFF_HC + (((row >> 4) * 18 + (d8 >> 2)) * 64 + (row & 15) + 16 * (d8 & 3)) * 16, o, 16);
      }
      ssq += __shfl_xor(ssq, 32);
      if (h == 0) {
        WS_ST4(P, OFF_SSQ + ((mt2 * 20 + cb * 5 + wid) * 32 + r) * 4, __float_as_uint(ssq), 16);
        if (cb == 0 && wid == 0) WS_ST4(P, OFF_MEANH + row * 4, __float_as_uint(mean), 16);
      }
    }
    WAITV(0); WAITL0();
    asm volatile("" :: "v"(sink));
    BAR();
    ST();
    if (FJ == 1 && mi == 1 && lane == 0) __hip_atomic_fetch_add((unsigned*)(P.ws + OFF_G3 + mb * 128), 1u, __ATOMIC_RELAXED, __HIP_MEMORY_SCOPE_AGENT);
    first_slot(std::integral_constant<int, 0>{});
    first_slot(std::integral_constant<int, 1>{});
    if (FJ == 1 && mi == 1) poll_counters(P.ws, (int)OFF_G3 + mb * 128, 16u);
    BAR();
    ST();
  }
  const char* hsrc = P.ws + OFF_HC + (size_t)mt * 36864;
  const char* hmine = hsrc + fj * 18432;
  constexpr int ST0 = K3_RING + 2 * 24576, ST1 = K3_HH, ST2 = K3_HH + 24576;
  dma16x3s(hmine, voff, lds_a + ST0 + (mi * 2 + fj) * 3072);
  dma16x3s(hmine + 3072, voff, lds_a + ST1 + (mi * 2 + fj) * 3072);
  dma16x3s(hmine + 6144, voff, lds_a + ST2 + (mi * 2 + fj) * 6144);
  dma16x3s(hmine + 9216, voff, lds_a + ST2 + (mi * 2 + fj) * 6144 + 3072);
  u32x4 hf[2][18];
  WAITV(9);
  BAR();
  const int q = (lane >> 2) & 3, pp = lane & 3, xg = ((q >> 1) & 1) | ((g & 1) << 1);
  lds_char* rd_g0 = lds + K3_RING + (8 * g + q) * 128 + ((fj ^ xg) * 32) + 8 * pp;
  lds_char* rd_g1 = lds + K3_RING + (8 * g + q) * 128 + (((fj + 2) ^ xg) * 32) + 8 * pp;
  lds_char* hh_rd = lds + K3_HH + mi * 18432 + lane * 16;
  const unsigned vgu = vgu_f();
  u32x4 a[6];
  auto read_frag = [&](auto nc) __attribute__((always_inline)) {
    constexpr int n = decltype(nc)::value;
    if constexpr (n < NS) {
      constexpr int o = SCH.lo[n] * 1024;
      if constexpr (n < PH) { lds_char* r = ((n & 1) ? rd_g1 : rd_g0) + o; a[n % 6] = tr_frag(r, r + 512); }
      else if constexpr (n < P2) { lds_char* r = lds + K3_RING + (8 * g + q) * 64 + ((fj ^ (g & 1)) * 32) + 8 * pp + o; a[n % 6] = tr_frag(r, r + 256); }
      else {
        const int gr = SCH.db[n - P2] == 4 ? 2 * p + fj : 2 * fj + SCH.t01[n - P2];
        lds_char* r = lds + K3_RING + (4 * g + q) * 128 + ((gr ^ xg) * 32) + 8 * pp + o;
        a[n % 6] = tr_frag(r, r + 2048);
      }
    }
  };
#pragma unroll
  for (int i = 0; i < 6; ++i) hf[i & 1][i >> 1] = *(lds_u32x4*)(lds + ST0 + (mi * 2 + (i & 1)) * 3072 + (i >> 1) * 1024 + lane * 16);
  static_for<0, 3>(read_frag);
  WAITL0();
  BAR();
  first_slot(std::integral_constant<int, 2>{});
  ST();
  const int fl_mine = (int)OFF_FLAG + b * 128;
  const char* fl_other = P.ws + OFF_FLAG + (b ^ 8) * 128;
  const int xh_off = (int)OFF_XH + (b * 4 + mi) * 18432;
  const srd_t sx = make_srd(P.ws + OFF_XH + (size_t)((b ^ 8) * 4 + mi) * 18432, 18432);
  const int pbase = (int)OFF_XH + (b ^ 8) * 73728 + mi * 18432;
  lds_char* hh_wr = hh_rd + 8 * fj;
  float sigma[2], gsc[2];
  f32x4 acc[2][2][2][2];
  f32x4 oc[9][2];
  u32x4 ov[2], ho[2][2], hp[9][2], xl[2];
  u32x2 hpk;
  u32x2 ovs[2];
  unsigned pollv, pfs = 0;

  auto epi = [&](auto gc, auto ic) __attribute__((always_inline)) {
    constexpr int G = decltype(gc)::value, i = decltype(ic)::value;
    if constexpr (i < (G == 4 ? 46 : 92)) {
      constexpr int uu = i / 46, mh = (i % 46) / 23, r = i % 23, set = G & 1, U = 2 * G + uu;
      if constexpr (r < 20) {
        constexpr int e = (r / 10) * 2 + (r & 1), ph = (r % 10) >> 1;
        float tv = acc[set][uu][0][mh][e];
        if (ph == 0) tv = tv * gsc[mh];
        if (ph == 1) tv = __builtin_amdgcn_exp2f(tv);
        if (ph == 2) tv = __builtin_fmaf(tv, sigma[mh], sigma[mh]);
        if (ph == 3) tv = __builtin_amdgcn_rcpf(tv);
        if (ph == 4) tv = tv * acc[set][uu][1][mh][e];
        asm volatile("" : "+v"(tv));
        acc[set][uu][0][mh][e] = tv;
      } else if constexpr (r < 22) {
        unsigned t = pk2(acc[set][uu][0][mh][2 * (r - 20)], acc[set][uu][0][mh][2 * (r - 20) + 1]);
        asm volatile("" : "+v"(t));
        hpk[r - 20] = t;
      } else {
        *(lds_u32x2*)(hh_wr + mh * 9216 + U * 1024) = hpk;
      }
      SB0();
    }
  };
  auto opack = [&](const int pr, const int i) __attribute__((always_inline)) {
    const int mh = i >> 2, r = i & 3;
    if (pr == 4 && r >= 2) return;
    const int ti = pr == 4 ? 8 : 2 * pr + (r >> 1), e = pr == 4 ? 2 * r : 2 * (r & 1);
    unsigned t = pk2(oc[ti][mh][e], oc[ti][mh][e + 1]);
    asm volatile("" : "+v"(t));
    if (pr == 4) ovs[mh][r & 1] = t; else ov[mh][r] = t;
    SB0();
  };
  auto ostore = [&](const int x) __attribute__((always_inline)) {
    const int pr = x >> 1, mh = x & 1;
    if (pr < 4) st128(ov[mh], wsr, voff, pbase + (p + 2 * pr + fj + 9 * mh) * 1024);
    else __builtin_amdgcn_raw_buffer_store_b64(ovs[mh], wsr, voff, pbase + ((p ? 0 : 8) + 9 * mh) * 1024 + 8 * fj, 16);
    SB0();
  };

  static_for<0, NS>([&](auto nc) __attribute__((always_inline)) {
    constexpr int n = decltype(nc)::value;
    constexpr bool bar = SCH.bar[n];
    if constexpr (n == 9) WAITV(12);
    else if constexpr (FJ == 0 && SCH.wl[n] >= 0) waitv<SCH.wl[n]>();
    else if constexpr (FJ == 1 && SCH.wf[n] >= 0) waitv<SCH.wf[n]>();
    if constexpr (FJ == 1 && SCH.chk[n] >= 0) {
      asm volatile("" : "+v"(pollv));
      if (__builtin_amdgcn_readfirstlane(pollv) < (unsigned)SCH.chk[n]) {
#pragma unroll 1
        for (int spin = 0; spin < (1 << 22); ++spin) {
          asm volatile("global_load_dword %0, %1, %2 sc1\n\ts_waitcnt vmcnt(0)" : "=&v"(pollv) : "v"(0u), "s"(fl_other) : "memory");
          if (__builtin_amdgcn_readfirstlane(pollv) >= (unsigned)SCH.chk[n]) break;
          __builtin_amdgcn_s_sleep(2);
        }
      }
    }
    if constexpr (bar) { WAITL0(); BAR(); __builtin_amdgcn_s_setprio(3); }
    else if constexpr (n == SCH.ebar) BAR();
    const u32x4 af = a[n % 6];
    if constexpr (SCH.prio[n] >= 0) __builtin_amdgcn_s_setprio(SCH.prio[n]);
    if constexpr (n == 72 || n == P2) ST();
    SB0();
    static_for<0, 2>([&](auto mc) __attribute__((always_inline)) {
      constexpr int mh = decltype(mc)::value;
      if constexpr (n < P2) {
        constexpr int y = n % 72;
        constexpr int set = (n / 72) & 1, S = n >= PH ? y >> 1 : y >> 2, mat = n >= PH ? y & 1 : (y >> 1) & 1, uu = n >= PH ? 0 : y & 1;
        if constexpr (S == 0) asm volatile("v_mfma_f32_16x16x32_bf16 %0, %1, %2, 0" : "=&v"(acc[set][uu][mat][mh]) : "v"(af), "v"(hf[mh][S]));
        else asm volatile("v_mfma_f32_16x16x32_bf16 %0, %1, %2, %0" : "+v"(acc[set][uu][mat][mh]) : "v"(af), "v"(hf[mh][S]));
      } else {
        constexpr int s2 = n - P2, U = SCH.u[s2], ti = SCH.db[s2] == 4 ? 8 : 2 * SCH.db[s2] + SCH.t01[s2];
        if constexpr (s2 < 9) asm volatile("v_mfma_f32_16x16x32_bf16 %0, %1, %2, 0" : "=&v"(oc[ti][mh]) : "v"(af), "v"(ho[0][mh]));
        else if constexpr (SCH.own[s2]) asm volatile("v_mfma_f32_16x16x32_bf16 %0, %1, %2, %0" : "+v"(oc[ti][mh]) : "v"(af), "v"(ho[U & 1][mh]));
        else asm volatile("v_mfma_f32_16x16x32_bf16 %0, %1, %2, %0" : "+v"(oc[ti][mh]) : "v"(af), "v"(hp[U][mh]));
      }
      SB0();
      if constexpr (mh == 0) { read_frag(std::integral_constant<int, n + 3>{}); SB0(); }
      if constexpr (SCH.eg[n] >= 0) {
        constexpr int f = SCH.e0[n] + (mh ? SCH.ne0[n] : 0), cnt = mh ? SCH.ne1[n] : SCH.ne0[n];
        static_for<f, f + cnt>([&](auto ic) __attribute__((always_inline)) { epi(std::integral_constant<int, SCH.eg[n]>{}, ic); });
      }
      if constexpr (SCH.og[n] >= 0) opack(SCH.og[n], SCH.o0[n] + mh);
    });
    if constexpr (FJ == 0 && SCH.dslot[n] >= 0) {
      constexpr int t = SCH.dslot[n], k = SCH.dk[n];
      if constexpr (t < 24) {
        bdma_i<(t % 3) * 24576 + k * 4096, 32 * (3 * (t % 6) + (k >> 1)) * (F_ * 2) + (t / 6) * 128>((k & 1) ? su : sg, vgu, sgu, ring_w);
      } else if constexpr (t < 27) {
        const unsigned vhalf = (lane >> 2) * (F_ * 2) + ((((lane >> 1) & 1) ^ ((lane >> 5) & 1)) * 32) + (lane & 1) * 16;
        bdma_i<(t % 3) * 24576 + k * 4096, 32 * (6 * (t - 24) + k) * (F_ * 2)>((mi & 1) ? su : sg, vhalf, 16 * (mi >> 1) * (F_ * 2) + 576 * c + (p ? 0 : 512),
                                                                          lds_a + K3_RING + (mi & 1) * 2048 + (mi >> 1) * 1024);
      } else {
        constexpr int sb = (t - 27) * 6 + k, ow = SCH.sown[sb], U = SCH.su[sb], d = SCH.sdb[sb];
        const unsigned vd = (lane >> 3) * (D_ * 2) + ((((lane >> 1) & 3) ^ ((lane >> 4) & 3)) * 32) + (lane & 1) * 16;
        const int cc = ow ? c : c ^ 1;
        bdma_i<(t % 3) * 24576 + k * 4096, (U == 8 ? 0 : 32 * U * (D_ * 2)) + (d == 4 ? 512 : 128 * d)>(
            sd, vd, (288 * cc + 8 * mi + (U == 8 ? ((cc & 1) ? 0 : 256) : ((cc & 1) ? 32 : 0))) * (D_ * 2) + (d == 4 ? 0 : 640 * p), ring_w);
      }
      SB0();
    }
    if constexpr (SCH.ost[n] >= 0) ostore(SCH.ost[n]);
    if constexpr (n == 5) { dma16x3s(P.ws + OFF_SSQ + mt * 2560, voff, lds_a + K3_SSQ + mi * 3072); SB0(); }
    if constexpr (n == 28) { dma16x3s(hmine + 12288, voff, lds_a + ST1 + (mi * 2 + fj) * 3072); SB0(); }
    if constexpr (n == 40) { dma16x3s(hmine + 15360, voff, lds_a + ST2 + (mi * 2 + fj) * 3072); SB0(); }
    if constexpr (n == 9 || n == 45 || n == 57) {
      constexpr int s0 = n == 9 ? 3 : (n == 45 ? 12 : 15), stg = n == 57 ? ST2 : ST1;
#pragma unroll
      for (int i = 0; i < 6; ++i) hf[i & 1][s0 + (i >> 1)] = *(lds_u32x4*)(lds + stg + (mi * 2 + (i & 1)) * 3072 + (i >> 1) * 1024 + lane * 16);
      SB0();
    }
    if constexpr (n == 21) {
#pragma unroll
      for (int i = 0; i < 12; ++i) hf[i & 1][6 + (i >> 1)] = *(lds_u32x4*)(lds + ST2 + (mi * 2 + (i & 1)) * 6144 + (i >> 1) * 1024 + lane * 16);
      SB0();
    }
    if constexpr (n == 50) {
#pragma unroll
      for (int mh = 0; mh < 2; ++mh) {
        float ssq = 0.f;
#pragma unroll
        for (int i = 0; i < 20; ++i) ssq += *(lds_float*)(lds + K3_SSQ + mi * 3072 + (i * 32 + mh * 16 + (lane & 15)) * 4);
        sigma[mh] = sqrtf(ssq * (1.f / D_) + LN_EPS);
        gsc[mh] = -1.4426950408889634f / sigma[mh];
        asm volatile("" : "+v"(sigma[mh]), "+v"(gsc[mh]));
      }
      SB0();
    }
    if constexpr (n == P2 - 1) { ho[0][0] = *(lds_u32x4*)(hh_rd); ho[0][1] = *(lds_u32x4*)(hh_rd + 9216); SB0(); }
    if constexpr (n >= P2 && n < P2 + 72 && (n - P2) % 9 == 4) {
      constexpr int U = (n - P2) / 9 + 1;
      ho[U & 1][0] = *(lds_u32x4*)(hh_rd + U * 1024); ho[U & 1][1] = *(lds_u32x4*)(hh_rd + 9216 + U * 1024);
      SB0();
    }
    if constexpr (FJ == 1) {
      if constexpr (SCH.xst[n] >= 0) {
        st128(xl[0], wsr, voff, xh_off + SCH.xst[n] * 1024);
        st128(xl[1], wsr, voff, xh_off + 9216 + SCH.xst[n] * 1024);
        SB0();
      }
      if constexpr (SCH.flag[n] >= 0) { __builtin_amdgcn_raw_buffer_store_b32((unsigned)SCH.flag[n], wsr, 0, fl_mine, 16); SB0(); }
      if constexpr (SCH.poll[n]) { asm volatile("global_load_dword %0, %1, %2 sc1" : "=&v"(pollv) : "v"(0u), "s"(fl_other) : "memory"); SB0(); }
      if constexpr (SCH.ld[n] >= 0) {
        constexpr int u = SCH.ld[n];
        bdma_sc1<u * 1024>(sx, voff, lds_a + K3_HH + mi * 18432);
        bdma_sc1<9216 + u * 1024>(sx, voff, lds_a + K3_HH + mi * 18432);
        SB0();
      }
      if constexpr (n == SCH.ebar) {
        if (mi == 0 && lane == 0) __hip_atomic_fetch_add((unsigned*)(P.ws + OFF_CNT + (mb * 2 + p) * 128), 1u, __ATOMIC_RELAXED, __HIP_MEMORY_SCOPE_AGENT);
        SB0();
      }
      if constexpr (SCH.pf[n] == 1 || SCH.pf[n] == 2) { pf_gu(pfs, mi, SCH.pf[n] == 1 || p ? 256 : 512); SB0(); }
      if constexpr (SCH.pf[n] == 3) {
        const int l41 = lane < 41 ? lane : 0, o = (((mb & 7) * 2 + p) * 4 + mi) * 10368 + l41 * 256;
        const char* pa = P.wd + (size_t)(c >> 1) * 663552 + (o < 663552 - 256 ? o : 663552 - 256);
        asm volatile("global_load_dword %0, %1, off\n\tglobal_load_dword %0, %1, off offset:128" : "+v"(pfs) : "v"(pa) : "memory");
        SB0();
      }
      if constexpr (SCH.flag[n] == 6) asm volatile("" :: "v"(pfs));
      if constexpr (SCH.xrd[n] >= 0) {
        xl[0] = *(lds_u32x4*)(hh_rd + SCH.xrd[n] * 1024); xl[1] = *(lds_u32x4*)(hh_rd + 9216 + SCH.xrd[n] * 1024);
        SB0();
      }
    }
    if constexpr (SCH.rd[n] >= 0) {
      constexpr int u = SCH.rd[n];
      hp[u][0] = *(lds_u32x4*)(hh_rd + u * 1024); hp[u][1] = *(lds_u32x4*)(hh_rd + 9216 + u * 1024);
      SB0();
    }
  });
  ST();
  MFMA_DRAIN();
#pragma unroll
  for (int i = 0; i < 8; ++i) opack(3, i);
  ostore(6);
  ostore(7);
  unsigned vo2 = voff;
  asm volatile("" : "+v"(vo2));
  const int ln = vo2 >> 4, gl = ln >> 4;
  const int j = c >> 1, t16 = mb * 8 + j, f0 = t16 * 18 + 9 * p;
  const int qr = p + wid, qs = p ? 0 : 8;
  const int l16 = 16 * mi + (ln & 15);
  const int row = t16 * 16 + (ln & 15);
  const int hb = ((ln & 15) + 16 * (gl >> 1)) * 16 + 8 * (gl & 1), hbs = ((ln & 15) + 16 * (mi >> 1)) * 16 + 8 * (mi & 1);
  const u32x2 h0 = __builtin_amdgcn_raw_buffer_load_b64(wsr, hb, (int)OFF_HC + (f0 + qr) * 1024, 16);
  const u32x2 h1 = __builtin_amdgcn_raw_buffer_load_b64(wsr, hb + 512, (int)OFF_HC + (f0 + qr) * 1024, 16);
  u32x2 hs0, hs1;
  if constexpr (FJ == 0) {
    hs0 = __builtin_amdgcn_raw_buffer_load_b64(wsr, hbs, (int)OFF_HC + (f0 + qs) * 1024, 16);
    hs1 = __builtin_amdgcn_raw_buffer_load_b64(wsr, hbs + 512, (int)OFF_HC + (f0 + qs) * 1024, 16);
  }
  const float mean = __uint_as_float(__builtin_amdgcn_raw_buffer_load_b32(wsr, row * 4, (int)OFF_MEANH, 16));
  const int cnt_off = (int)OFF_CNT + (mb * 2 + p) * 128;
  if constexpr (FJ == 0) poll_counters(P.ws, cnt_off, 8u);
  else WAITV(0);
  BAR();
  ST();
  if constexpr (FJ == 1) {
    if (mi == 0 && ln == 0) __hip_atomic_fetch_add((unsigned*)(P.ws + cnt_off + 64), 1u, __ATOMIC_RELAXED, __HIP_MEMORY_SCOPE_AGENT);
    poll_counters(P.ws, cnt_off + 64, 8u);
  }
  ST();
  auto area = [&](const int k) { const int ck = 2 * k + (p ^ 1); return (int)OFF_XH + (((mb & 7) * 4 + (ck & 3)) * 8 + (ck >> 2) * 2 + (mb >> 3)) * 73728 + j * 9216; };
  u32x4 pv[8], ps[2];
#pragma unroll
  for (int k = 0; k < 8; ++k) pv[k] = __builtin_amdgcn_raw_buffer_load_b128(wsr, voff, area(k) + qr * 1024, 16);
  if constexpr (FJ == 0) {
#pragma unroll
    for (int k = 0; k < 2; ++k) ps[k] = __builtin_amdgcn_raw_buffer_load_b128(wsr, area(4 * k + gl) + l16 * 16, qs * 1024, 16);
  }
  float o[8];
  o[0] = bflo(h0[0]); o[1] = bfhi(h0[0]); o[2] = bflo(h0[1]); o[3] = bfhi(h0[1]);
  o[4] = bflo(h1[0]); o[5] = bfhi(h1[0]); o[6] = bflo(h1[1]); o[7] = bfhi(h1[1]);
#pragma unroll
  for (int k = 0; k < 8; ++k) o[k] += mean;
#pragma unroll
  for (int r = 0; r < 8; ++r)
#pragma unroll
    for (int k = 0; k < 4; ++k) { o[2 * k] += bflo(pv[r][k]); o[2 * k + 1] += bfhi(pv[r][k]); }
  {
    char* d = P.out + (size_t)row * 1152 + (9 * p + qr) * 64 + gl * 8;
    *(u32x2*)d = (u32x2){pk2(o[0], o[1]), pk2(o[2], o[3])};
    *(u32x2*)(d + 32) = (u32x2){pk2(o[4], o[5]), pk2(o[6], o[7])};
  }
  if constexpr (FJ == 0) {
    float os[8];
#pragma unroll
    for (int k = 0; k < 4; ++k) { os[2 * k] = bflo(ps[0][k]) + bflo(ps[1][k]); os[2 * k + 1] = bfhi(ps[0][k]) + bfhi(ps[1][k]); }
#pragma unroll
    for (int k = 0; k < 8; ++k) { os[k] += __shfl_xor(os[k], 16); os[k] += __shfl_xor(os[k], 32); }
    if (ln < 16) {
      char* d = P.out + (size_t)row * 1152 + (9 * p + qs) * 64 + mi * 8;
      *(u32x2*)d = (u32x2){pk2(os[0] + bflo(hs0[0]) + mean, os[1] + bfhi(hs0[0]) + mean), pk2(os[2] + bflo(hs0[1]) + mean, os[3] + bfhi(hs0[1]) + mean)};
      *(u32x2*)(d + 32) = (u32x2){pk2(os[4] + bflo(hs1[0]) + mean, os[5] + bfhi(hs1[0]) + mean), pk2(os[6] + bflo(hs1[1]) + mean, os[7] + bfhi(hs1[1]) + mean)};
    }
  }
}

__global__ __launch_bounds__(512, 2) void k3_ffn(Ptrs P) {
  extern __shared__ __attribute__((aligned(16))) char smem[];
  const int lane = threadIdx.x & 63, wid = __builtin_amdgcn_readfirstlane(threadIdx.x >> 6);
  const int b = blockIdx.x;
  const int c = ((b & 7) >> 1) * 4 + ((b >> 3) & 3), mb = (b & 1) * 8 + (b >> 5);
  if (wid < 4) k3_body<0>(P, (lds_char*)smem, lane, wid, b, c, mb);
  else k3_body<1>(P, (lds_char*)smem, lane, wid - 4, b, c, mb);
}

constexpr int K0_IMG = 4608;
constexpr int K0_RED = 3 * K0_IMG;
constexpr int K0_LDS = K0_RED + 9 * 8 * 8;

DI float dpp_sum8(float v) {
  v += __shfl_xor(v, 1); v += __shfl_xor(v, 2); v += __shfl_xor(v, 4);
  return v;
}

__global__ __launch_bounds__(768) void k0_prep(Ptrs P) {
  extern __shared__ __attribute__((aligned(16))) char smem[];
  lds_char* lds = (lds_char*)smem;
  const int lane = threadIdx.x & 63, wid = __builtin_amdgcn_readfirstlane(threadIdx.x >> 6);
  const int b = blockIdx.x;
  ST_DECL; ST();
  if (wid < 9) {
    const int row = b * 8 + (lane >> 3), i = lane & 7;
    const u32x4 v = *(const u32x4*)(P.x + (size_t)row * 1152 + wid * 128 + i * 16);
    float e[8];
#pragma unroll
    for (int k = 0; k < 4; ++k) { e[2 * k] = bflo(v[k]); e[2 * k + 1] = bfhi(v[k]); }
    const float sum = dpp_sum8(((e[0] + e[1]) + (e[2] + e[3])) + ((e[4] + e[5]) + (e[6] + e[7])));
    const float lm = sum * (1.f / 64.f);
    float m2 = 0.f;
#pragma unroll
    for (int k = 0; k < 8; ++k) { const float d = e[k] - lm; m2 = __builtin_fmaf(d, d, m2); }
    m2 = dpp_sum8(m2);
    if (i == 0) *(lds_f32x2*)(lds + K0_RED + (wid * 8 + (lane >> 3)) * 8) = (f32x2){lm, m2};
    WAITL0();
    BAR();
    ST();
    f32x2 pr[9];
#pragma unroll
    for (int w = 0; w < 9; ++w) pr[w] = *(lds_f32x2*)(lds + K0_RED + (w * 8 + (lane >> 3)) * 8);
    float mean = 0.f;
#pragma unroll
    for (int w = 0; w < 9; ++w) mean += pr[w][0];
    mean *= (1.f / 9.f);
    float tot = 0.f;
#pragma unroll
    for (int w = 0; w < 9; ++w) { const float d = pr[w][0] - mean; tot += __builtin_fmaf(64.f * d, d, pr[w][1]); }
    const float rstd = rsqrtf(tot * (1.f / D_) + LN_EPS);
    u32x4 o;
#pragma unroll
    for (int k = 0; k < 4; ++k) o[k] = pk2((e[2 * k] - mean) * rstd, (e[2 * k + 1] - mean) * rstd);
    WS_ST16(P, (int)OFF_XN + ((row >> 5) * 36 * 64 + (row & 31) + 32 * (i & 1) + (4 * wid + (i >> 1)) * 64) * 16, o, ST_AUX);
    if (wid == 0 && i == 0) WS_ST4(P, OFF_MEANX + row * 4, __float_as_uint(mean), ST_AUX);
    return;
  }
  const int id = (wid - 9) * 256 + b;
  lds_char* img = lds + (wid - 9) * K0_IMG;
  const unsigned img_a = (unsigned)(uintptr_t)img;
  const int g = lane >> 4, q = (lane >> 2) & 3, p = lane & 3;
  unsigned pf_sink = 0;
  if (wid == 9) {
    constexpr unsigned NW = (D_ * F_ * 2 + TSTRIDE - 1) / TSTRIDE, NS_ = (unsigned)((WS_END + TSTRIDE - 1) / TSTRIDE), NO = (S_ * D_ * 2 + TSTRIDE - 1) / TSTRIDE;
    constexpr unsigned NT = 3 * NW + NS_ + NO;
    for (unsigned t = (b >> 3) * 64 + lane; t < ((NT + 2047) / 2048) * 2048; t += 2048) {
      const unsigned u = t < NT ? t : 0;
      const char* pp = u < NW ? P.wg + (size_t)u * TSTRIDE : u < 2 * NW ? P.wu + (size_t)(u - NW) * TSTRIDE : u < 3 * NW ? P.wd + (size_t)(u - 2 * NW) * TSTRIDE
                     : u < 3 * NW + NS_ ? P.ws + (size_t)(u - 3 * NW) * TSTRIDE : P.out + (size_t)(u - 3 * NW - NS_) * TSTRIDE;
      asm volatile("global_load_dword %0, %1, off" : "+v"(pf_sink) : "v"(pp) : "memory");
    }
  }
  if (wid == 10 && b < 96) {
    unsigned long long pc, k1a;
    asm volatile("s_getpc_b64 %0" : "=s"(pc));
    asm volatile("s_getpc_b64 s[40:41]\n\ts_add_u32 s40, s40, _Z6k3_ffn4Ptrs@rel32@lo+4\n\ts_addc_u32 s41, s41, _Z6k3_ffn4Ptrs@rel32@hi+12\n\t"
                 "s_mov_b64 %0, s[40:41]" : "=s"(k1a) :: "s40", "s41", "scc");
    const char* pp = (const char*)k1a + ((b >> 3) * 64 + lane) * 128;
    pp = pp < (const char*)pc ? pp : (const char*)k1a;
    asm volatile("global_load_dword %0, %1, off" : "+v"(pf_sink) : "v"(pp) : "memory");
  }
  if (id < 576) {
    const int s = id >> 4, h = (id >> 3) & 1, hp0 = (id & 7) * 2;
    const char* src = P.wqkv + (size_t)(16 * s + 8 * h) * 3456 + hp0 * 72;
#pragma unroll
    for (int it = 0; it < 4; ++it) {
      int e = it * 64 + lane; e = e < 216 ? e : 215;
      const int sgm = e / 9, ch = e - sgm * 9;
      dma16x1(src, sgm * 1152 + ch * 16, img_a + it * 1024);
    }
    BAR();
    ST();
    WAITV(0);
    asm volatile("" :: "v"(pf_sink));
    ST();
#pragma unroll
    for (int it = 0; it < 4; ++it) {
      int Q = 16 * it + 4 * g + p; Q = Q < 54 ? Q : 53;
      const int hl = Q >= 27 ? 1 : 0, sg = Q - 27 * hl;
      const int cb = sg < 4 ? 8 * sg : (sg < 8 ? 8 * sg + 8 : (sg == 8 ? 32 : (sg < 18 ? 8 * sg + 72 : 8 * sg + 144)));
      lds_char* a = img + q * 432 + cb + 72 * hl;
      u32x4 v = tr_frag(a, a + 4 * 432);
      const int o = it * 64 + lane, ol = o >= 108 ? 1 : 0, slot = o - 108 * ol;
      const int off = slot < 32 ? WQT_Q + s * 1024 + (h * 32 + slot) * 16 : s * 2432 + (h * 76 + slot - 32) * 16;
      if (o < 216) WS_ST16(P, OFF_WQT + (hp0 + ol) * (int)WQT_HP + off, v, ST_AUX);
    }
  } else if (id < 720) {
    const int j = id - 576, ks = j >> 2, cb = j & 3;
#pragma unroll
    for (int it = 0; it < 5; ++it) {
      int e = it * 64 + lane; e = e < 288 ? e : 287;
      const int kk = e / 18, ch = e - kk * 18, h = kk >> 3, jj = kk & 7;
      const int row = ks < 32 ? 18 * ks + (jj & 3) + 8 * (jj >> 2) + 4 * h : 18 * (8 * (ks - 32) + jj) + 16 + h;
      if (it < 4 || lane < 32) dma16x1(P.wo + cb * 288, row * 1152 + ch * 16, img_a + it * 1024);
    }
    BAR();
    WAITV(0);
#pragma unroll
    for (int it = 0; it < 5; ++it) {
      int G16 = 4 * it + g; G16 = G16 < 18 ? G16 : 17;
      const int h = G16 >= 9 ? 1 : 0, sg = G16 - 9 * h;
      lds_char* a = img + (8 * h + q) * 288 + (16 * sg + 4 * p) * 2;
      u32x4 v = tr_frag(a, a + 4 * 288);
      if (it < 4 || lane < 32) WS_ST16(P, OFF_WOT + cb * (int)WOT_CB + ks * 4672 + (h * 146 + 16 * sg + (lane & 15)) * 16, v, ST_AUX);
    }
  } else if (id < 756) {
    const int row = (id - 720) * 16 + (lane >> 2), i = lane & 3;
    const char* src = P.wo + (size_t)row * 1152 + i * 16;
    u32x4 v[18];
#pragma unroll
    for (int j = 0; j < 18; ++j) v[j] = *(const u32x4*)(src + j * 64);
    BAR();
    float s4[4] = {0.f, 0.f, 0.f, 0.f};
    const bf16x2 one2 = __builtin_bit_cast(bf16x2, 0x3f803f80u);
#pragma unroll
    for (int j = 0; j < 18; ++j)
#pragma unroll
      for (int k = 0; k < 4; ++k) { const unsigned t = v[j][k]; s4[k] = __builtin_amdgcn_fdot2_f32_bf16(__builtin_bit_cast(bf16x2, t), one2, s4[k], false); }
    float sum = (s4[0] + s4[1]) + (s4[2] + s4[3]);
    sum += __shfl_xor(sum, 1); sum += __shfl_xor(sum, 2);
    const float mean = sum * (1.f / D_);
    const int hd = row / 18, e = row % 18;
    const int ks = e < 16 ? hd : 32 + (hd >> 3), jj = e < 16 ? (e & 3) + 4 * (e >> 3) : (hd & 7), hh = e < 16 ? (e >> 2) & 1 : e - 16;
    const unsigned hi = pk2(mean, 0.f) & 0xffffu;
    const unsigned lo = pk2(mean - bflo(hi), 0.f) & 0xffffu;
    unsigned short* d = (unsigned short*)(P.ws + OFF_WOT + (size_t)i * WOT_CB + ks * 4672 + (hh * 146 + 144) * 16 + jj * 2);
    d[0] = (unsigned short)hi; d[8] = (unsigned short)lo;
  } else {
    const u32x4 z = {0u, 0u, 0u, 0u};
    for (int k = id - 756; k < 50; k += 12) WS_ST16(P, OFF_KV + k * 1024 + lane * 16, z, ST_AUX);
    for (int k = id - 756; k < 70; k += 12) WS_ST16(P, OFF_FLAG + k * 1024 + lane * 16, z, ST_AUX);
    BAR();
  }
}

extern "C" void kernel_launch(void* const* d_in, const int* in_sizes, int n_in, void* d_out, int out_size, void* d_ws,
                              size_t ws_size, hipStream_t stream) {
  Ptrs P;
  P.x = (const char*)d_in[0]; P.wqkv = (const char*)d_in[1]; P.wo = (const char*)d_in[2];
  P.wg = (const char*)d_in[4]; P.wu = (const char*)d_in[5]; P.wd = (const char*)d_in[6];
  P.ws = (char*)d_ws; P.out = (char*)d_out;
  (void)hipFuncSetAttribute((const void*)k3_ffn, hipFuncAttributeMaxDynamicSharedMemorySize, K3_LDS);
  k0_prep<<<256, 768, K0_LDS, stream>>>(P);
  k3_ffn<<<256, 512, K3_LDS, stream>>>(P);
}
```

```cpp
#include <hip/hip_runtime.h>
#include <hip/hip_bf16.h>
#include <stdint.h>
#include <type_traits>

typedef __attribute__((ext_vector_type(8))) short bf16x8;
typedef __attribute__((ext_vector_type(4))) short s16x4;
typedef __attribute__((ext_vector_type(16))) float f32x16;
typedef __attribute__((ext_vector_type(4))) float f32x4;
typedef __attribute__((ext_vector_type(2))) float f32x2;
typedef __attribute__((ext_vector_type(2))) __bf16 bf16x2;
typedef __attribute__((ext_vector_type(4))) unsigned u32x4;
typedef __attribute__((ext_vector_type(2))) unsigned u32x2;
typedef __attribute__((address_space(3))) char lds_char;
typedef __attribute__((address_space(3))) s16x4 lds_s16x4;
typedef __attribute__((address_space(3))) u32x4 lds_u32x4;
typedef __attribute__((address_space(3))) u32x2 lds_u32x2;
typedef __attribute__((address_space(3))) f32x4 lds_f32x4;
typedef __attribute__((address_space(3))) float lds_float;
typedef __attribute__((address_space(3))) f32x2 lds_f32x2;
typedef __attribute__((address_space(3))) unsigned lds_uint;

#define DI __device__ __forceinline__
#define MFMA32(a, b, c) __builtin_amdgcn_mfma_f32_32x32x16_bf16((a), (b), (c), 0, 0, 0)
#define WAITV(n) asm volatile("s_waitcnt vmcnt(" #n ")" ::: "memory")
#define WAITL0() asm volatile("s_waitcnt lgkmcnt(0)" ::: "memory")
#define BAR() asm volatile("s_barrier" ::: "memory")
#define SB0() __builtin_amdgcn_sched_barrier(0)

constexpr int S_ = 2048, D_ = 576, F_ = 4608;
constexpr float LN_EPS = 1e-5f;

constexpr size_t FLB = (size_t)S_ * D_ * 2;
constexpr size_t WQT_HP = 36 * 3456;
constexpr int WQT_Q = 36 * 2432;
constexpr size_t WOT_CB = 36 * 4672;
constexpr size_t OFF_XN = 0;
constexpr size_t OFF_PQ = OFF_XN + FLB;
constexpr size_t OFF_HC = OFF_PQ + FLB;
constexpr size_t OFF_MEANX = OFF_HC + FLB;
constexpr size_t OFF_MEANH = OFF_MEANX + 8192;
constexpr size_t OFF_SSQ = OFF_MEANH + 8192;
constexpr size_t OFF_KV = OFF_SSQ + 20 * 8192 + 4096;
constexpr size_t OFF_WQT = OFF_KV + 53248;
constexpr size_t OFF_WOT = OFF_WQT + 16 * WQT_HP + 16384;
constexpr size_t OFF_XH = OFF_WOT + 4 * WOT_CB + 4096;
constexpr size_t OFF_FLAG = OFF_XH + 256 * 73728;
constexpr size_t OFF_CNT = OFF_FLAG + 256 * 128;
constexpr size_t OFF_G1 = OFF_CNT + 32 * 128;
constexpr size_t OFF_G3 = OFF_G1 + 256 * 128;
constexpr size_t WS_END = OFF_G3 + 16 * 128;
static_assert(OFF_XH % 1024 == 0 && (WS_END - OFF_FLAG) == 70 * 1024, "workspace");

#define ST_DECL
#define ST()
#define ST_OUT(k, slot)
DI unsigned pk2(float a, float b) {
  f32x2 v = {a, b};
  bf16x2 r = __builtin_convertvector(v, bf16x2);
  return __builtin_bit_cast(unsigned, r);
}
DI float bflo(unsigned u) { return __uint_as_float(u << 16); }
DI float bfhi(unsigned u) { return __uint_as_float(u & 0xffff0000u); }
DI float phi(float t) {
  const float e = __builtin_amdgcn_fmed3f(__builtin_amdgcn_exp2f(t * 1.4426950408889634f), 0.f, 1.f);
  return (__builtin_amdgcn_fmed3f(t, 0.f, __builtin_inff()) + 1e-6f) + e;
}
DI bf16x8 as_frag(u32x4 v) { return __builtin_bit_cast(bf16x8, v); }

DI void dma16x3s(const void* sbase, unsigned voff, unsigned lds_dst) {
  unsigned keep;
  asm volatile("s_mov_b32 %0, m0\n\ts_mov_b32 m0, %3\n\ts_nop 0\n\t"
               "global_load_lds_dwordx4 %1, %2 sc1\n\t"
               "global_load_lds_dwordx4 %1, %2 offset:1024 sc1\n\t"
               "global_load_lds_dwordx4 %1, %2 offset:2048 sc1\n\t"
               "s_mov_b32 m0, %0"
               : "=&s"(keep) : "v"(voff), "s"(sbase), "s"(lds_dst) : "memory");
}
DI void dma16x1(const void* sbase, unsigned voff, unsigned lds_dst) {
  unsigned keep;
  asm volatile("s_mov_b32 %0, m0\n\ts_mov_b32 m0, %3\n\ts_nop 0\n\t"
               "global_load_lds_dwordx4 %1, %2\n\t"
               "s_mov_b32 m0, %0"
               : "=&s"(keep) : "v"(voff), "s"(sbase), "s"(lds_dst) : "memory");
}
DI void touch1(const void* pa, unsigned lds_dump) {
  unsigned keep;
  asm volatile("s_mov_b32 %0, m0\n\ts_mov_b32 m0, %2\n\ts_nop 0\n\tglobal_load_lds_dword %1, off\n\ts_mov_b32 m0, %0"
               : "=&s"(keep) : "v"(pa), "s"(lds_dump) : "memory");
}
DI void touch2(const void* pa, unsigned lds_dump) {
  unsigned keep;
  asm volatile("s_mov_b32 %0, m0\n\ts_mov_b32 m0, %2\n\ts_nop 0\n\tglobal_load_lds_dword %1, off\n\t"
               "global_load_lds_dword %1, off offset:128\n\ts_mov_b32 m0, %0"
               : "=&s"(keep) : "v"(pa), "s"(lds_dump) : "memory");
}
DI u32x4 tr_frag(lds_char* p0, lds_char* p1) {
  s16x4 lo = __builtin_amdgcn_ds_read_tr16_b64_v4i16((lds_s16x4*)p0);
  s16x4 hi = __builtin_amdgcn_ds_read_tr16_b64_v4i16((lds_s16x4*)p1);
  u32x2 a = __builtin_bit_cast(u32x2, lo), b = __builtin_bit_cast(u32x2, hi);
  u32x4 r = {a[0], a[1], b[0], b[1]};
  return r;
}

#define ST_AUX 16
#define WS_RSRC(P) __builtin_amdgcn_make_buffer_rsrc((P).ws, 0, (int)WS_END, 0x00020000)
#define WS_ST16(P, off, v, aux) __builtin_amdgcn_raw_buffer_store_b128((v), WS_RSRC(P), (int)(off), 0, (aux))
#define WS_ST8(P, off, v, aux) __builtin_amdgcn_raw_buffer_store_b64((v), WS_RSRC(P), (int)(off), 0, (aux))
#define WS_ST4(P, off, v, aux) __builtin_amdgcn_raw_buffer_store_b32((v), WS_RSRC(P), (int)(off), 0, (aux))
template <class R> DI void st128(u32x4 v, R rsrc, int voff, int soff) {
  __builtin_amdgcn_raw_buffer_store_b128(v, rsrc, voff, soff, 16);
  asm volatile("s_nop 1" :: "v"(v));
}
struct Ptrs {
  const char* x; const char* wqkv; const char* wo; const char* wg; const char* wu; const char* wd;
  char* ws; char* out;
};

constexpr unsigned TSTRIDE = 2097152;
constexpr int G1R = 16;

constexpr int K1_W = 0;
constexpr int KVP = 192;
constexpr int K2_O = 49152;
constexpr int K2_RED = K2_O + 36864;
constexpr int K2_MEAN = K2_RED + 32768;

DI void poll_counters(const char* base, const int off, const unsigned need) {
#pragma unroll 1
  for (int spin = 0; spin < (1 << 22); ++spin) {
    unsigned v;
    asm volatile("global_load_dword %0, %1, %2 sc1\n\ts_waitcnt vmcnt(0)" : "=&v"(v) : "v"(off), "s"(base) : "memory");
    if (__builtin_amdgcn_ballot_w64(v < need) == 0ull) break;
    __builtin_amdgcn_s_sleep(2);
  }
}

constexpr int K3_RING = 0;
constexpr int K3_HH = 73728;
constexpr int K3_SSQ = K3_HH + 73728;
constexpr int K3_DUMP = K3_SSQ + 4 * 3072;
constexpr int K3_LDS = K3_DUMP + 512;
constexpr int PF0 = 69, PFD1 = 48, PFD2 = 96;
constexpr int EBAR = 156, XOFF = 1;
constexpr int NS = 486, PH = 288, P2 = 324;

struct Sch {
  signed char own[162] = {}, u[162] = {}, db[162] = {}, t01[162] = {};
  short rs[NS + 4] = {}, lo[NS + 4] = {};
  signed char sown[90] = {}, su[90] = {}, sdb[90] = {}, pf[NS] = {};
  bool bar[NS] = {};
  signed char dslot[NS] = {}, dk[NS] = {}, prio[NS] = {};
  signed char e0[NS] = {}, ne0[NS] = {}, ne1[NS] = {}, eg[NS] = {};
  signed char og[NS] = {}, o0[NS] = {};
  signed char xrd[NS] = {}, cbar[NS] = {}, xst[NS] = {}, ost[NS] = {}, xop[NS] = {};
  signed char flag[NS] = {}, fbar[NS] = {}, poll[NS] = {}, chk[NS] = {}, ld[NS] = {}, wr[NS] = {}, rd[NS] = {};
  signed char wl[NS] = {}, wf[NS] = {};
  int ebar = 0;
  bool ok = true;
};
constexpr Sch make_sch() {
  Sch s;
  int n2 = 0, nb = 0;
  auto put = [&](int own, int u, int db) {
    s.sown[nb] = own; s.su[nb] = u; s.sdb[nb] = db;
    for (int t = 0; t < (db == 4 ? 1 : 2); ++t) {
      s.own[n2] = own; s.u[n2] = u; s.db[n2] = db; s.t01[n2] = t;
      s.rs[P2 + n2] = 27 + nb / 6; s.lo[P2 + n2] = ((27 + nb / 6) % 3) * 24 + (nb % 6) * 4; ++n2;
    }
    ++nb;
  };
  for (int u = 0; u < 9; ++u) for (int d = 0; d < 5; ++d) put(1, u, d);
  for (int u = 0; u < 6; ++u) { put(0, u, 4); put(0, u, 0); put(0, u, 1); }
  for (int u = 6; u < 9; ++u) { put(0, u, 4); put(0, u, 0); }
  for (int u = 6; u < 9; ++u) put(0, u, 1);
  for (int d = 2; d < 4; ++d) for (int u = 0; u < 9; ++u) put(0, u, d);
  for (int n = 0; n < PH; ++n) { s.rs[n] = n / 12; s.lo[n] = ((n / 12) % 3) * 24 + ((n % 12) >> 1) * 4; }
  for (int n = PH; n < P2; ++n) { s.rs[n] = n / 12; s.lo[n] = ((n / 12) % 3) * 24 + (n % 12) * 2; }
  for (int n = NS; n < NS + 4; ++n) { s.rs[n] = s.rs[NS - 1]; s.lo[n] = s.lo[NS - 1]; }
  for (int n = 0; n < NS; ++n) {
    s.bar[n] = s.rs[n + 3] != s.rs[n + 2];
    s.dslot[n] = -1; s.prio[n] = -1; s.eg[n] = -1; s.og[n] = -1; s.flag[n] = -1; s.fbar[n] = -1; s.poll[n] = 0; s.chk[n] = -1;
    s.ld[n] = -1; s.rd[n] = -1; s.wl[n] = -1; s.wf[n] = -1; s.xst[n] = -1; s.ost[n] = -1; s.xrd[n] = -1; s.cbar[n] = -1; s.pf[n] = 0;
  }
  for (int n = 0; n < NS; ++n) if (s.bar[n]) {
    int e = n + 1; while (e < NS && !s.bar[e]) ++e;
    const int L = e - n, tgt = s.rs[n + 3] + 2;
    if (tgt <= 41) for (int k = 0; k < 6; ++k) { s.dslot[n + (k * L) / 6 + 1] = tgt; s.dk[n + (k * L) / 6 + 1] = k; if ((k * L) / 6 + 1 >= L) s.ok = false; }
    if (e < NS) { s.prio[n + L / 2] = 2; s.prio[e - 3] = 1; s.prio[e - 1] = 0; }
  }
  auto nbar = [&](int n) { while (!s.bar[n]) ++n; return n; };
  for (int g = 0; g < 5; ++g) {
    const int start = (g < 4 ? 72 * (g + 1) : P2) + 2, tot = g == 4 ? 46 : 92;
    int e = 0;
    for (int n = start; e < tot; ++n) {
      const int c0 = g == 4 ? 2 : 1, c1 = g >= 3 ? 2 : ((n - start) & 1);
      if (s.eg[n] >= 0) s.ok = false;
      s.eg[n] = g; s.e0[n] = e; s.ne0[n] = c0; s.ne1[n] = c1;
      for (int i = e; i < e + c0 + c1 && i < tot; ++i) if (i % 46 == 45) { int m = n + 1; while (!s.bar[m]) ++m; m += XOFF; while (s.xrd[m] >= 0) m += 3; s.xrd[m] = 2 * g + i / 46; s.xst[m + 2] = 2 * g + i / 46; }
      e += c0 + c1;
      if (g < 3 && n >= start + 68) s.ok = false;
    }
  }
  s.og[P2 + 119] = 4; s.o0[P2 + 119] = 0; s.og[P2 + 120] = 4; s.o0[P2 + 120] = 4; s.ost[P2 + 120] = 8; s.ost[P2 + 122] = 9;
  const int opk[3] = {121, 128, 145};
  for (int k = 0; k < 3; ++k) {
    for (int i = 0; i < 4; ++i) { const int n = P2 + opk[k] + i; s.og[n] = k; s.o0[n] = 2 * i; }
    s.ost[P2 + opk[k] + 3] = 2 * k; s.ost[P2 + opk[k] + 6] = 2 * k + 1;
  }
  int lds_[9] = {16, 18, 24, 34, 43, 44, 76, 78, 80}, rds[9] = {};
  for (int u = 0; u < 9; ++u) {
    const int cbn = nbar(P2 + lds_[(u / 3) * 3 + 2] + 17);
    rds[u] = cbn - P2 + 1 + (u < 6 ? 3 : 2) * (u % 3);
    s.ld[P2 + lds_[u]] = u; s.rd[P2 + rds[u]] = u; s.cbar[cbn] = u;
  }
  s.poll[P2 + 3] = 1; s.chk[P2 + 15] = 6;
  s.poll[P2 + 63] = 1; s.chk[P2 + 75] = 9;
  s.xop[28] = 3; s.xop[40] = 3;
  s.pf[PF0] = 1; s.pf[PF0 + PFD1] = 2; s.pf[PF0 + PFD2] = 3;
  const int fb[2] = {PH + 21, nbar(P2 + 40)}, fv[2] = {6, 9};
  for (int k = 0; k < 2; ++k) { if (!s.bar[fb[k]]) s.ok = false; s.fbar[fb[k]] = fv[k]; s.flag[fb[k] + 1] = fv[k]; }
  s.ebar = P2 + EBAR;
  int cl[NS + 1] = {}, cf[NS + 1] = {};
  for (int n = 9; n < NS; ++n) {
    const int both = (s.ost[n] >= 0 ? 1 : 0) + s.xop[n];
    cl[n + 1] = cl[n] + both + (s.dslot[n] >= 0 ? 1 : 0);
    cf[n + 1] = cf[n] + both + (s.xst[n] >= 0 ? 2 : 0) + (s.flag[n] >= 0 ? 1 : 0) + s.poll[n] + (s.ld[n] >= 0 ? 2 : 0) + (s.pf[n] ? 2 : 0);
  }
  auto need = [&](int n, int ul_, int uf_) {
    if (ul_ >= 0) { const int w = cl[n] - ul_; if (s.wl[n] < 0 || w < s.wl[n]) s.wl[n] = w; }
    if (uf_ >= 0) { const int w = cf[n] - uf_; if (s.wf[n] < 0 || w < s.wf[n]) s.wf[n] = w; }
  };
  for (int n = 21; n < NS; ++n) {
    if (s.bar[n]) {
      int last = -1;
      for (int i = 9; i < n; ++i) if (s.dslot[i] == s.rs[n + 3]) last = i;
      need(n, last >= 0 ? cl[last] + 1 : 0, n == 21 ? 0 : -1);
      if (n == 45) need(n, cl[29], cf[29]);
      if (n == 57) need(n, cl[41], cf[41]);
      if (s.fbar[n] >= 0) {
        int at = -1;
        for (int i = 9; i < n; ++i) if (s.xst[i] == s.fbar[n] - 1) at = i;
        if (at < 0) s.ok = false;
        for (int i = 9; i < n; ++i) if (s.xst[i] >= 0 && s.xst[i] < s.fbar[n] && i > at) at = i;
        need(n, -1, cf[at + 1]);
      }
    }
    if (n == s.ebar) need(n, cl[P2 + 135], cf[P2 + 135]);
    if (s.chk[n] >= 0) { int i = n; while (!s.poll[i]) --i; need(n, -1, cf[i + 1]); }
    if (s.cbar[n] >= 0) { int i = n; while (s.ld[i] != s.cbar[n]) --i; need(n, -1, cf[i + 1]); }
  }
  for (int u = 0; u < 9; ++u) {
    int own_rd = u == 0 ? P2 - 1 : P2 + 9 * (u - 1) + 4;
    for (int n = 0; n < NS; ++n) if (s.xrd[n] == u && n > own_rd) own_rd = n;
    bool b1 = false;
    for (int n = own_rd + 1; n <= P2 + lds_[u]; ++n) b1 = b1 || s.bar[n];
    int use = 81; while (s.u[use] != u) ++use;
    if (!b1 || rds[u] + 2 > use) s.ok = false;
  }
  return s;
}
constexpr Sch SCH = make_sch();
static_assert(SCH.ok, "schedule");

template <int I, int N, class F> DI void static_for(F&& f) {
  if constexpr (N - I == 1) f(std::integral_constant<int, I>{});
  else if constexpr (N - I > 1) { static_for<I, (I + N) / 2>(f); static_for<(I + N) / 2, N>(f); }
}
template <int N> DI void waitv() { asm volatile("s_waitcnt vmcnt(%0)" :: "n"(N) : "memory"); }
#define MFMA_DRAIN() do { asm volatile("s_nop 7\n\ts_nop 3" ::: "memory"); SB0(); } while (0)
typedef __attribute__((ext_vector_type(4))) int srd_t;
DI srd_t make_srd(const void* p, unsigned bytes) {
  const unsigned long long a = (unsigned long long)p;
  srd_t r = {(int)(unsigned)a, (int)((unsigned)(a >> 32) & 0xffffu), (int)bytes, 0x00020000};
  return r;
}
template <int LOFF, int SOFF> DI void bdma_i(srd_t srd, unsigned voff, unsigned sbase, unsigned lbase) {
  unsigned so;
  asm volatile("s_add_u32 m0, %4, %6\n\ts_add_u32 %0, %3, %5\n\tbuffer_load_dwordx4 %1, %2, %0 offen lds"
               : "=&s"(so) : "v"(voff), "s"(srd), "s"(sbase), "s"(lbase), "n"(SOFF), "n"(LOFF) : "memory", "scc");
}

template <int OFF> DI void bdma_sc1(srd_t srd, unsigned voff, unsigned lbase) {
  unsigned so;
  asm volatile("s_add_u32 m0, %3, %4\n\ts_mov_b32 %0, %4\n\tbuffer_load_dwordx4 %1, %2, %0 offen sc1 lds"
               : "=&s"(so) : "v"(voff), "s"(srd), "s"(lbase), "n"(OFF) : "memory", "scc");
}

template <int FJ>
DI void k3_body(const Ptrs& P, lds_char* lds, const int lane, const int mi, const int b, const int c, const int mb) {
  constexpr int fj = FJ;
  const unsigned lds_a = (unsigned)(uintptr_t)lds;
  const int mt = mb * 4 + mi, p = c & 1;
  const int g = lane >> 4;
  ST_DECL; ST();
  const unsigned voff = lane * 16;
  const srd_t sd = make_srd(P.wd, D_ * F_ * 2);
  const srd_t sg = make_srd(P.wg, D_ * F_ * 2), su = make_srd(P.wu, D_ * F_ * 2);
  const auto wsr = WS_RSRC(P);
  const int wid = fj * 4 + mi;
  const unsigned sgu = 8 * mi * (F_ * 2) + 576 * c + 64 * p;
  const unsigned ring_w = lds_a + K3_RING + mi * 1024;
  auto vgu_f = [&]() { return (unsigned)((lane >> 3) * (F_ * 2) + ((((lane >> 1) & 3) ^ (((lane >> 4) & 1) | ((mi & 1) << 1))) * 32) + (lane & 1) * 16); };
  auto first_slot = [&](auto tc) __attribute__((always_inline)) {
    constexpr int t = decltype(tc)::value;
    static_for<3 * FJ, 3 * FJ + 3>([&](auto kc) __attribute__((always_inline)) {
      constexpr int k = decltype(kc)::value;
      bdma_i<t * 24576 + k * 4096, 32 * (3 * t + (k >> 1)) * (F_ * 2)>((k & 1) ? su : sg, vgu_f(), sgu, ring_w);
    });
  };
  auto pf_gu = [&](const int i, const int col) {
    const int l36 = lane < 36 ? lane : 0, mat = l36 >= 18 ? 1 : 0, row = ((mb & 7) * 4 + i) * 18 + l36 - 18 * mat;
    const char* pa = (mat ? P.wu : P.wg) + (size_t)row * (F_ * 2) + 576 * c + 64 * p + col;
    touch2(pa, lds_a + K3_DUMP);
  };
  {
    const int r = lane & 31, h = lane >> 5;
    const int hp = c;
    const int mt2 = mb * 4 + (c >> 2), cb = c & 3;
    u32x4 af[18], a4[5], xv0, xv1;
    float mean;
    const int k4 = FJ == 0 ? 5 * mi : 20 + 4 * mi;
    constexpr int n4 = FJ == 0 ? 5 : 4;
    const int row = mt2 * 32 + r, s0 = cb * 9 + 2 * (FJ == 0 ? mi : 4);
    auto prefetch2 = [&]() __attribute__((always_inline)) {
      const char* wo = P.ws + OFF_WOT + (size_t)cb * WOT_CB + h * 2336;
#pragma unroll
      for (int i = 0; i < 18; ++i) af[i] = *(const u32x4*)(wo + (18 * FJ + i) * 4672 + (32 * mi + r) * 16);
#pragma unroll
      for (int i = 0; i < n4; ++i) a4[i] = *(const u32x4*)(wo + (k4 + i) * 4672 + (128 + (r < 17 ? r : 17)) * 16);
      xv0 = *(const u32x4*)(P.x + (size_t)row * 1152 + s0 * 32 + h * 16);
      xv1 = *(const u32x4*)(P.x + (size_t)row * 1152 + (FJ == 0 ? s0 + 1 : s0) * 32 + h * 16);
      mean = ((const float*)(P.ws + OFF_MEANX))[row];
      SB0();
    };
    if constexpr (FJ == 1) {
      const char* wsrc = P.ws + OFF_WQT + (size_t)hp * WQT_HP + mi * 1024;
      const unsigned wdst = lds_a + K1_W + mi * 1024;
#pragma unroll
      for (int i = 0; i < 14; ++i) dma16x1(wsrc + i * 4096, voff, wdst + i * 4096);
      WAITV(8); BAR();
#pragma unroll
      for (int i = 14; i < 20; ++i) dma16x1(wsrc + i * 4096, voff, wdst + i * 4096);
      WAITV(9); BAR();
#pragma unroll
      for (int i = 20; i < 26; ++i) dma16x1(wsrc + i * 4096, voff, wdst + i * 4096);
      WAITV(9); BAR();
#pragma unroll
      for (int i = 26; i < 31; ++i) dma16x1(wsrc + i * 4096, voff, wdst + i * 4096);
      WAITV(9); BAR();
      WAITV(0);
      prefetch2();
      if (mi >= 2) { pf_gu((mi & 1) * 2, 0); pf_gu((mi & 1) * 2 + 1, 0); }
      BAR();
      ST();
      if (mi < 2) {
        __builtin_amdgcn_s_setprio(3);
        const int q = (lane >> 2) & 3, op = 4 * (g & 1) + (lane & 3);
        const int vq = op >= 5 ? 19 : (mi ? 14 : 10) + op, kq = (mi ? 5 : 1) + op;
        lds_char* va = lds + (8 * (g >> 1) + q) * KVP + vq * 8;
        lds_char* ka = lds + (8 * (g >> 1) + q) * KVP + kq * 8;
        f32x16 kv;
#pragma unroll
        for (int i = 0; i < 16; ++i) kv[i] = 0.f;
#pragma unroll
        for (int ks = 0; ks < 8; ++ks) {
          u32x4 a = tr_frag(va + ks * 16 * KVP, va + ks * 16 * KVP + 4 * KVP);
          u32x4 bb = tr_frag(ka + ks * 16 * KVP, ka + ks * 16 * KVP + 4 * KVP);
          kv = MFMA32(as_frag(a), as_frag(bb), kv);
        }
        const int d = r - 2 * mi;
        const int dd = mi ? (d >= 2 ? d - 2 : 16 + d) : d;
        float* dst = (float*)(P.ws + OFF_KV) + (2 * hp + mi) * 400 + dd;
        const bool dok = d >= 0 && d < 18;
#pragma unroll
        for (int i = 0; i < 12; ++i) {
          const int rw = (i & 3) + 8 * (i >> 2) + 4 * h;
          const int e = rw == 20 ? 18 : rw - 2 * mi;
          if (dok && (rw == 20 || (e >= 0 && e < 18 && rw < 20))) atomicAdd(dst + e * 20, kv[i]);
        }
        WAITV(0);
      }
    } else {
      u32x4 xf[36];
      const char* xsrc = P.ws + OFF_XN + (size_t)mt * 36864 + lane * 16;
#pragma unroll
      for (int s = 0; s < 18; ++s) xf[s] = *(const u32x4*)(xsrc + s * 1024);
      f32x16 acc[3], accq;
#pragma unroll
      for (int i = 0; i < 16; ++i) { acc[0][i] = 0.f; acc[1][i] = 0.f; acc[2][i] = 0.f; accq[i] = 0.f; }
      lds_char* wa = lds + K1_W + (h * 76 + r) * 16;
      lds_char* wa2 = lds + K1_W + (h * 76 + 64 + (r < 12 ? r : 11)) * 16;
      u32x4 wf[2][3];
#pragma unroll
      for (int s = 0; s < 36; ++s) {
        if (s % 9 == 0) {
          BAR();
          if (s < 18) {
#pragma unroll
            for (int k = s + 18; k < s + 27; ++k) xf[k] = *(const u32x4*)(xsrc + k * 1024);
          }
          wf[s & 1][0] = *(lds_u32x4*)(wa + s * 2432); wf[s & 1][1] = *(lds_u32x4*)(wa + s * 2432 + 512); wf[s & 1][2] = *(lds_u32x4*)(wa2 + s * 2432);
        }
        if (s % 9 != 8) {
          wf[(s + 1) & 1][0] = *(lds_u32x4*)(wa + (s + 1) * 2432); wf[(s + 1) & 1][1] = *(lds_u32x4*)(wa + (s + 1) * 2432 + 512);
          wf[(s + 1) & 1][2] = *(lds_u32x4*)(wa2 + (s + 1) * 2432);
        }
        SB0();
#pragma unroll
        for (int t = 0; t < 3; ++t) acc[t] = MFMA32(as_frag(wf[s & 1][t]), as_frag(xf[s]), acc[t]);
        SB0();
      }
      ST();
      lds_char* kvrow = lds + (mi * 32 + r) * KVP;
      u32x2 qrem;
#pragma unroll
      for (int gi = 0; gi < 10; ++gi) {
        const int t = gi >> 2, gg = gi & 3;
        u32x2 o;
        if (gi < 5) { o[0] = pk2(phi(acc[t][4 * gg]), phi(acc[t][4 * gg + 1])); o[1] = pk2(phi(acc[t][4 * gg + 2]), phi(acc[t][4 * gg + 3])); }
        else { o[0] = pk2(acc[t][4 * gg], acc[t][4 * gg + 1]); o[1] = pk2(acc[t][4 * gg + 2], acc[t][4 * gg + 3]); }
        if (gi == 9 && h == 1) { o[0] = 0x3f803f80u; o[1] = 0x3f803f80u; }
        *(lds_u32x2*)(kvrow + (2 * gi + h) * 8) = o;
        if (gi == 0) qrem = o;
      }
      WAITL0();
      BAR();
      lds_char* wq = lds + K1_W + WQT_Q + lane * 16;
      u32x4 aq[2][4];
#pragma unroll
      for (int i = 0; i < 4; ++i) aq[0][i] = *(lds_u32x4*)(wq + i * 1024);
#pragma unroll
      for (int g3 = 0; g3 < 9; ++g3) {
        if (g3 < 8) {
#pragma unroll
          for (int i = 0; i < 4; ++i) aq[(g3 + 1) & 1][i] = *(lds_u32x4*)(wq + (4 * g3 + 4 + i) * 1024);
        }
        SB0();
#pragma unroll
        for (int i = 0; i < 4; ++i) accq = MFMA32(as_frag(aq[g3 & 1][i]), as_frag(xf[4 * g3 + i]), accq);
        SB0();
      }
      const int pq = (int)OFF_PQ + mt * 36864;
#pragma unroll
      for (int e = 0; e < 2; ++e) {
        u32x4 o;
#pragma unroll
        for (int k = 0; k < 4; ++k) o[k] = pk2(phi(accq[8 * e + 2 * k]), phi(accq[8 * e + 2 * k + 1]));
        WS_ST16(P, pq + (2 * hp + e) * 1024 + lane * 16, o, 16);
      }
      if (h == 0) WS_ST8(P, pq + 32768 + hp * 256 + r * 8, qrem, 16);
      WAITV(0);
    }
    BAR();
    ST();
    if (FJ == 1 && mi == 0) {
      constexpr int NSH = 256 / G1R;
      if (lane < G1R) __hip_atomic_fetch_add((unsigned*)(P.ws + OFF_G1 + (lane * NSH + (b % NSH)) * 128), 1u, __ATOMIC_RELAXED, __HIP_MEMORY_SCOPE_AGENT);
      poll_counters(P.ws, (int)OFF_G1 + ((b / NSH) * NSH + (lane % NSH)) * 128, (unsigned)G1R);
    }
    if constexpr (FJ == 0) { __builtin_amdgcn_s_sleep(4); prefetch2(); }
    BAR();
    ST();
    const int e = r < 16 ? r : (r == 16 ? 16 : (r == 20 ? 17 : 18));
    const bool rok = r <= 17 || r == 20 || r == 21;
    u32x4 k0[4], k1[4], qf[4]; u32x2 k2[4];
    dma16x3s(P.ws + OFF_KV + wid * 6400, voff, lds_a + wid * 6144);
    dma16x3s(P.ws + OFF_KV + wid * 6400 + 3072, voff, lds_a + wid * 6144 + 3072);
    const u32x4 ktail = __builtin_amdgcn_raw_buffer_load_b128(wsr, (lane & 15) * 16, (int)OFF_KV + wid * 6400 + 6144, 16);
#pragma unroll
    for (int i = 0; i < 4; ++i) qf[i] = __builtin_amdgcn_raw_buffer_load_b128(wsr, voff, (int)OFF_PQ + mt2 * 36864 + (4 * wid + i) * 1024, 16);
    const u32x2 qr0 = __builtin_amdgcn_raw_buffer_load_b64(wsr, r * 8, (int)OFF_PQ + mt2 * 36864 + 32768 + wid * 512, 16);
    const u32x2 qr1 = __builtin_amdgcn_raw_buffer_load_b64(wsr, r * 8 + 256, (int)OFF_PQ + mt2 * 36864 + 32768 + wid * 512, 16);
    const u32x4 qr = {qr0[0], qr0[1], qr1[0], qr1[1]};
    *(lds_u32x4*)(lds + K2_RED + wid * 256 + (lane & 15) * 16) = ktail;
    asm volatile("" ::: "memory");
    SB0();
#pragma unroll
    for (int i = 0; i < 4; ++i) {
      const int o = i * 1600 + e * 80;
      lds_char* kp0 = lds + (o + 16 * h < 6144 ? wid * 6144 + o + 16 * h : K2_RED + wid * 256 + o + 16 * h - 6144);
      lds_char* kp1 = lds + (o + 16 * h + 32 < 6144 ? wid * 6144 + o + 16 * h + 32 : K2_RED + wid * 256 + o + 16 * h + 32 - 6144);
      lds_char* kp2 = lds + (o + 64 < 6144 ? wid * 6144 + o + 64 : K2_RED + wid * 256 + o + 64 - 6144);
      k0[i] = *(lds_u32x4*)kp0; k1[i] = *(lds_u32x4*)kp1; k2[i] = *(lds_u32x2*)kp2;
    }
    float orem[4];
#define F_(x) __uint_as_float(x)
#pragma unroll
    for (int i = 0; i < 4; ++i) {
      u32x4 a1 = {pk2(F_(k0[i][0]), F_(k0[i][1])), pk2(F_(k0[i][2]), F_(k0[i][3])), pk2(F_(k1[i][0]), F_(k1[i][1])), pk2(F_(k1[i][2]), F_(k1[i][3]))};
      u32x4 a2 = {0u, 0u, 0u, 0u};
      a2[i] = (h == (mi & 1)) ? pk2(F_(k2[i][0]), F_(k2[i][1])) : 0u;
      if (!rok) { a1 = (u32x4){0u, 0u, 0u, 0u}; a2[i] = 0u; }
      f32x16 cc;
#pragma unroll
      for (int j = 0; j < 16; ++j) cc[j] = 0.f;
      cc = MFMA32(as_frag(a1), as_frag(qf[i]), cc);
      cc = MFMA32(as_frag(a2), as_frag(qr), cc);
      const float inv = __builtin_amdgcn_rcpf(cc[9] + 1e-6f);
      float o[9];
#pragma unroll
      for (int j = 0; j < 9; ++j) o[j] = cc[j] * inv;
      u32x4 of = {pk2(o[0], o[1]), pk2(o[2], o[3]), pk2(o[4], o[5]), pk2(o[6], o[7])};
      *(lds_u32x4*)(lds + K2_O + (4 * wid + i) * 1024 + lane * 16) = of;
      orem[i] = o[8];
    }
#undef F_
    {
      u32x2 o2 = {pk2(orem[0], orem[1]), pk2(orem[2], orem[3])};
      *(lds_u32x2*)(lds + K2_O + (32 + (wid >> 1)) * 1024 + lane * 16 + 8 * (mi & 1)) = o2;
    }
    WAITL0();
    BAR();
    ST();
    f32x16 acc, acc4;
#pragma unroll
    for (int j = 0; j < 16; ++j) { acc[j] = 0.f; acc4[j] = 0.f; }
    lds_char* ob = lds + K2_O + lane * 16;
#pragma unroll
    for (int i = 0; i < n4; ++i) acc4 = MFMA32(as_frag(a4[i]), as_frag(*(lds_u32x4*)(ob + (k4 + i) * 1024)), acc4);
#pragma unroll
    for (int i = 0; i < 18; ++i) acc = MFMA32(as_frag(af[i]), as_frag(*(lds_u32x4*)(ob + (18 * FJ + i) * 1024)), acc);
    {
      *(lds_float*)(lds + K2_MEAN + (wid * 64 + lane) * 4) = acc4[8] + acc4[9];
      lds_char* p4 = lds + K2_RED + 16384 + wid * 2048 + lane * 16;
      *(lds_f32x4*)(p4) = (f32x4){acc4[0], acc4[1], acc4[2], acc4[3]};
      *(lds_f32x4*)(p4 + 1024) = (f32x4){acc4[4], acc4[5], acc4[6], acc4[7]};
      if (FJ == 1) {
        lds_char* pr = lds + K2_RED + mi * 4096 + lane * 16;
#pragma unroll
        for (int k = 0; k < 4; ++k) *(lds_f32x4*)(pr + k * 1024) = (f32x4){acc[4 * k], acc[4 * k + 1], acc[4 * k + 2], acc[4 * k + 3]};
      }
    }
    WAITL0();
    BAR();
    if (FJ == 0 || mi == 0) {
#pragma unroll
      for (int w = 0; w < 8; ++w) mean += *(lds_float*)(lds + K2_MEAN + (w * 64 + r) * 4);
      if constexpr (FJ == 0) {
        lds_char* pr = lds + K2_RED + mi * 4096 + lane * 16;
#pragma unroll
        for (int k = 0; k < 4; ++k) {
          f32x4 v = *(lds_f32x4*)(pr + k * 1024);
#pragma unroll
          for (int j = 0; j < 4; ++j) acc[4 * k + j] += v[j];
        }
      } else {
#pragma unroll
        for (int j = 0; j < 8; ++j) acc[j] = 0.f;
#pragma unroll
        for (int w = 0; w < 8; ++w) {
          lds_char* p4 = lds + K2_RED + 16384 + w * 2048 + lane * 16;
          f32x4 v0 = *(lds_f32x4*)(p4), v1 = *(lds_f32x4*)(p4 + 1024);
#pragma unroll
          for (int j = 0; j < 4; ++j) { acc[j] += v0[j]; acc[4 + j] += v1[j]; }
        }
      }
      float ssq = 0.f;
#pragma unroll
      for (int i = 0; i < (FJ == 0 ? 2 : 1); ++i) {
        float v[8];
#pragma unroll
        for (int j = 0; j < 4; ++j) {
          auto sw2 = __builtin_amdgcn_permlane32_swap(__float_as_uint(acc[8 * i + j]), __float_as_uint(acc[8 * i + 4 + j]), false, false);
          v[j] = __uint_as_float(sw2[0]); v[4 + j] = __uint_as_float(sw2[1]);
        }
        const u32x4 xv = i ? xv1 : xv0;
        u32x4 o;
#pragma unroll
        for (int k = 0; k < 4; ++k) {
          const float a0 = v[2 * k] + bflo(xv[k]) - mean, a1 = v[2 * k + 1] + bfhi(xv[k]) - mean;
          ssq += a0 * a0 + a1 * a1;
          o[k] = pk2(a0, a1);
        }
        const int d8 = 2 * (s0 + i) + h;
        WS_ST16(P, OFF_HC + (((row >> 4) * 18 + (d8 >> 2)) * 64 + (row & 15) + 16 * (d8 & 3)) * 16, o, 16);
      }
      ssq += __shfl_xor(ssq, 32);
      if (h == 0) {
        WS_ST4(P, OFF_SSQ + ((mt2 * 20 + cb * 5 + wid) * 32 + r) * 4, __float_as_uint(ssq), 16);
        if (cb == 0 && wid == 0) WS_ST4(P, OFF_MEANH + row * 4, __float_as_uint(mean), 16);
      }
    }
    WAITV(0); WAITL0();
    BAR();
    ST();
    if (FJ == 1 && mi == 1 && lane == 0) __hip_atomic_fetch_add((unsigned*)(P.ws + OFF_G3 + mb * 128), 1u, __ATOMIC_RELAXED, __HIP_MEMORY_SCOPE_AGENT);
    first_slot(std::integral_constant<int, 0>{});
    first_slot(std::integral_constant<int, 1>{});
    if (FJ == 1 && mi == 1) poll_counters(P.ws, (int)OFF_G3 + mb * 128, 16u);
    BAR();
    ST();
  }
  const char* hsrc = P.ws + OFF_HC + (size_t)mt * 36864;
  const char* hmine = hsrc + fj * 18432;
  constexpr int ST0 = K3_RING + 2 * 24576, ST1 = K3_HH, ST2 = K3_HH + 24576;
  dma16x3s(hmine, voff, lds_a + ST0 + (mi * 2 + fj) * 3072);
  dma16x3s(hmine + 3072, voff, lds_a + ST1 + (mi * 2 + fj) * 3072);
  dma16x3s(hmine + 6144, voff, lds_a + ST2 + (mi * 2 + fj) * 6144);
  dma16x3s(hmine + 9216, voff, lds_a + ST2 + (mi * 2 + fj) * 6144 + 3072);
  u32x4 hf[2][18];
  WAITV(9);
  BAR();
  const int q = (lane >> 2) & 3, pp = lane & 3, xg = ((q >> 1) & 1) | ((g & 1) << 1);
  lds_char* rd_g0 = lds + K3_RING + (8 * g + q) * 128 + ((fj ^ xg) * 32) + 8 * pp;
  lds_char* rd_g1 = lds + K3_RING + (8 * g + q) * 128 + (((fj + 2) ^ xg) * 32) + 8 * pp;
  lds_char* hh_rd = lds + K3_HH + mi * 18432 + lane * 16;
  const unsigned vgu = vgu_f();
  u32x4 a[6];
  auto read_frag = [&](auto nc) __attribute__((always_inline)) {
    constexpr int n = decltype(nc)::value;
    if constexpr (n < NS) {
      constexpr int o = SCH.lo[n] * 1024;
      if constexpr (n < PH) { lds_char* r = ((n & 1) ? rd_g1 : rd_g0) + o; a[n % 6] = tr_frag(r, r + 512); }
      else if constexpr (n < P2) { lds_char* r = lds + K3_RING + (8 * g + q) * 64 + ((fj ^ (g & 1)) * 32) + 8 * pp + o; a[n % 6] = tr_frag(r, r + 256); }
      else {
        const int gr = SCH.db[n - P2] == 4 ? 2 * p + fj : 2 * fj + SCH.t01[n - P2];
        lds_char* r = lds + K3_RING + (4 * g + q) * 128 + ((gr ^ xg) * 32) + 8 * pp + o;
        a[n % 6] = tr_frag(r, r + 2048);
      }
    }
  };
#pragma unroll
  for (int i = 0; i < 6; ++i) hf[i & 1][i >> 1] = *(lds_u32x4*)(lds + ST0 + (mi * 2 + (i & 1)) * 3072 + (i >> 1) * 1024 + lane * 16);
  static_for<0, 3>(read_frag);
  WAITL0();
  BAR();
  first_slot(std::integral_constant<int, 2>{});
  ST();
  const int fl_mine = (int)OFF_FLAG + b * 128;
  const char* fl_other = P.ws + OFF_FLAG + (b ^ 8) * 128;
  const int xh_off = (int)OFF_XH + (b * 4 + mi) * 18432;
  const srd_t sx = make_srd(P.ws + OFF_XH + (size_t)((b ^ 8) * 4 + mi) * 18432, 18432);
  const int pbase = (int)OFF_XH + (b ^ 8) * 73728 + mi * 18432;
  lds_char* hh_wr = hh_rd + 8 * fj;
  float sigma[2], gsc[2];
  float sq[5];
  f32x4 acc[2][2][2][2];
  f32x4 oc[9][2];
  u32x4 ov[2], ho[2][2], hp[9][2], xl[2];
  u32x2 hpk;
  u32x2 ovs[2];
  unsigned pollv;

  auto epi = [&](auto gc, auto ic) __attribute__((always_inline)) {
    constexpr int G = decltype(gc)::value, i = decltype(ic)::value;
    if constexpr (i < (G == 4 ? 46 : 92)) {
      constexpr int uu = i / 46, mh = (i % 46) / 23, r = i % 23, set = G & 1, U = 2 * G + uu;
      if constexpr (r < 20) {
        constexpr int e = (r / 10) * 2 + (r & 1), ph = (r % 10) >> 1;
        float tv = acc[set][uu][0][mh][e];
        if (ph == 0) tv = tv * gsc[mh];
        if (ph == 1) tv = __builtin_amdgcn_exp2f(tv);
        if (ph == 2) tv = __builtin_fmaf(tv, sigma[mh], sigma[mh]);
        if (ph == 3) tv = __builtin_amdgcn_rcpf(tv);
        if (ph == 4) tv = tv * acc[set][uu][1][mh][e];
        asm volatile("" : "+v"(tv));
        acc[set][uu][0][mh][e] = tv;
      } else if constexpr (r < 22) {
        unsigned t = pk2(acc[set][uu][0][mh][2 * (r - 20)], acc[set][uu][0][mh][2 * (r - 20) + 1]);
        asm volatile("" : "+v"(t));
        hpk[r - 20] = t;
      } else {
        *(lds_u32x2*)(hh_wr + mh * 9216 + U * 1024) = hpk;
      }
      SB0();
    }
  };
  auto opack = [&](const int pr, const int i) __attribute__((always_inline)) {
    const int mh = i >> 2, r = i & 3;
    if (pr == 4 && r >= 2) return;
    const int ti = pr == 4 ? 8 : 2 * pr + (r >> 1), e = pr == 4 ? 2 * r : 2 * (r & 1);
    unsigned t = pk2(oc[ti][mh][e], oc[ti][mh][e + 1]);
    asm volatile("" : "+v"(t));
    if (pr == 4) ovs[mh][r & 1] = t; else ov[mh][r] = t;
    SB0();
  };
  auto ostore = [&](const int x) __attribute__((always_inline)) {
    const int pr = x >> 1, mh = x & 1;
    if (pr < 4) st128(ov[mh], wsr, voff, pbase + (p + 2 * pr + fj + 9 * mh) * 1024);
    else __builtin_amdgcn_raw_buffer_store_b64(ovs[mh], wsr, voff >> 1, pbase + ((p ? 0 : 8) + 9 * mh) * 1024 + 512 * fj, 16);
    SB0();
  };

  static_for<0, NS>([&](auto nc) __attribute__((always_inline)) {
    constexpr int n = decltype(nc)::value;
    constexpr bool bar = SCH.bar[n];
    if constexpr (n == 9) WAITV(12);
    else if constexpr (FJ == 0 && SCH.wl[n] >= 0) waitv<SCH.wl[n]>();
    else if constexpr (FJ == 1 && SCH.wf[n] >= 0) waitv<SCH.wf[n]>();
    if constexpr (FJ == 1 && SCH.chk[n] >= 0) {
      asm volatile("" : "+v"(pollv));
      if (__builtin_amdgcn_readfirstlane(pollv) < (unsigned)SCH.chk[n]) {
#pragma unroll 1
        for (int spin = 0; spin < (1 << 22); ++spin) {
          asm volatile("global_load_dword %0, %1, %2 sc1\n\ts_waitcnt vmcnt(0)" : "=&v"(pollv) : "v"(0u), "s"(fl_other) : "memory");
          if (__builtin_amdgcn_readfirstlane(pollv) >= (unsigned)SCH.chk[n]) break;
          __builtin_amdgcn_s_sleep(2);
        }
      }
    }
    if constexpr (bar) { WAITL0(); BAR(); __builtin_amdgcn_s_setprio(3); }
    else if constexpr (n == SCH.ebar) BAR();
    const u32x4 af = a[n % 6];
    if constexpr (SCH.prio[n] >= 0) __builtin_amdgcn_s_setprio(SCH.prio[n]);
    if constexpr (n == 72 || n == P2) ST();
    SB0();
    static_for<0, 2>([&](auto mc) __attribute__((always_inline)) {
      constexpr int mh = decltype(mc)::value;
      if constexpr (n < P2) {
        constexpr int y = n % 72;
        constexpr int set = (n / 72) & 1, S = n >= PH ? y >> 1 : y >> 2, mat = n >= PH ? y & 1 : (y >> 1) & 1, uu = n >= PH ? 0 : y & 1;
        if constexpr (S == 0) asm volatile("v_mfma_f32_16x16x32_bf16 %0, %1, %2, 0" : "=&v"(acc[set][uu][mat][mh]) : "v"(af), "v"(hf[mh][S]));
        else asm volatile("v_mfma_f32_16x16x32_bf16 %0, %1, %2, %0" : "+v"(acc[set][uu][mat][mh]) : "v"(af), "v"(hf[mh][S]));
      } else {
        constexpr int s2 = n - P2, U = SCH.u[s2], ti = SCH.db[s2] == 4 ? 8 : 2 * SCH.db[s2] + SCH.t01[s2];
        if constexpr (s2 < 9) asm volatile("v_mfma_f32_16x16x32_bf16 %0, %1, %2, 0" : "=&v"(oc[ti][mh]) : "v"(af), "v"(ho[0][mh]));
        else if constexpr (SCH.own[s2]) asm volatile("v_mfma_f32_16x16x32_bf16 %0, %1, %2, %0" : "+v"(oc[ti][mh]) : "v"(af), "v"(ho[U & 1][mh]));
        else asm volatile("v_mfma_f32_16x16x32_bf16 %0, %1, %2, %0" : "+v"(oc[ti][mh]) : "v"(af), "v"(hp[U][mh]));
      }
      SB0();
      if constexpr (mh == 0) { read_frag(std::integral_constant<int, n + 3>{}); SB0(); }
      if constexpr (SCH.eg[n] >= 0) {
        constexpr int f = SCH.e0[n] + (mh ? SCH.ne0[n] : 0), cnt = mh ? SCH.ne1[n] : SCH.ne0[n];
        static_for<f, f + cnt>([&](auto ic) __attribute__((always_inline)) { epi(std::integral_constant<int, SCH.eg[n]>{}, ic); });
      }
      if constexpr (SCH.og[n] >= 0) opack(SCH.og[n], SCH.o0[n] + mh);
    });
    if constexpr (FJ == 0 && SCH.dslot[n] >= 0) {
      constexpr int t = SCH.dslot[n], k = SCH.dk[n];
      if constexpr (t < 24) {
        bdma_i<(t % 3) * 24576 + k * 4096, 32 * (3 * (t % 6) + (k >> 1)) * (F_ * 2) + (t / 6) * 128>((k & 1) ? su : sg, vgu, sgu, ring_w);
      } else if constexpr (t < 27) {
        const unsigned vhalf = (lane >> 2) * (F_ * 2) + ((((lane >> 1) & 1) ^ ((lane >> 5) & 1)) * 32) + (lane & 1) * 16;
        bdma_i<(t % 3) * 24576 + k * 4096, 32 * (6 * (t - 24) + k) * (F_ * 2)>((mi & 1) ? su : sg, vhalf, 16 * (mi >> 1) * (F_ * 2) + 576 * c + (p ? 0 : 512),
                                                                          lds_a + K3_RING + (mi & 1) * 2048 + (mi >> 1) * 1024);
      } else {
        constexpr int sb = (t - 27) * 6 + k, ow = SCH.sown[sb], U = SCH.su[sb], d = SCH.sdb[sb];
        const unsigned vd = (lane >> 3) * (D_ * 2) + ((((lane >> 1) & 3) ^ ((lane >> 4) & 3)) * 32) + (lane & 1) * 16;
        const int cc = ow ? c : c ^ 1;
        bdma_i<(t % 3) * 24576 + k * 4096, (U == 8 ? 0 : 32 * U * (D_ * 2)) + (d == 4 ? 512 : 128 * d)>(
            sd, vd, (288 * cc + 8 * mi + (U == 8 ? ((cc & 1) ? 0 : 256) : ((cc & 1) ? 32 : 0))) * (D_ * 2) + (d == 4 ? 0 : 640 * p), ring_w);
      }
      SB0();
    }
    if constexpr (SCH.ost[n] >= 0) ostore(SCH.ost[n]);
    if constexpr (n == 5) { dma16x3s(P.ws + OFF_SSQ + mt * 2560, voff, lds_a + K3_SSQ + mi * 3072); SB0(); }
    if constexpr (n == 28) { dma16x3s(hmine + 12288, voff, lds_a + ST1 + (mi * 2 + fj) * 3072); SB0(); }
    if constexpr (n == 40) { dma16x3s(hmine + 15360, voff, lds_a + ST2 + (mi * 2 + fj) * 3072); SB0(); }
    if constexpr (n == 9 || n == 45 || n == 57) {
      constexpr int s0 = n == 9 ? 3 : (n == 45 ? 12 : 15), stg = n == 57 ? ST2 : ST1;
#pragma unroll
      for (int i = 0; i < 6; ++i) hf[i & 1][s0 + (i >> 1)] = *(lds_u32x4*)(lds + stg + (mi * 2 + (i & 1)) * 3072 + (i >> 1) * 1024 + lane * 16);
      SB0();
    }
    if constexpr (n == 21) {
#pragma unroll
      for (int i = 0; i < 12; ++i) hf[i & 1][6 + (i >> 1)] = *(lds_u32x4*)(lds + ST2 + (mi * 2 + (i & 1)) * 6144 + (i >> 1) * 1024 + lane * 16);
      SB0();
    }
    if constexpr (n == 48 || n == 60) {
      constexpr int mh = n == 60;
#pragma unroll
      for (int i = 0; i < 5; ++i) sq[i] = *(lds_float*)(lds + K3_SSQ + mi * 3072 + ((5 * g + i) * 32 + mh * 16 + (lane & 15)) * 4);
      SB0();
    }
    if constexpr (n == 52 || n == 64) {
      constexpr int mh = n == 64;
      const float t0 = (sq[0] + sq[1]) + (sq[2] + sq[3]) + sq[4];
      const auto t1 = __builtin_amdgcn_permlane16_swap(__float_as_uint(t0), __float_as_uint(t0), false, false);
      const float t2 = __uint_as_float(t1[0]) + __uint_as_float(t1[1]);
      const auto t3 = __builtin_amdgcn_permlane32_swap(__float_as_uint(t2), __float_as_uint(t2), false, false);
      const float var = (__uint_as_float(t3[0]) + __uint_as_float(t3[1])) * (1.f / D_) + LN_EPS;
      const float rs = __builtin_amdgcn_rsqf(var);
      sigma[mh] = var * rs;
      gsc[mh] = -1.4426950408889634f * rs;
      asm volatile("" : "+v"(sigma[mh]), "+v"(gsc[mh]));
      SB0();
    }
    if constexpr (n == P2 - 1) { ho[0][0] = *(lds_u32x4*)(hh_rd); ho[0][1] = *(lds_u32x4*)(hh_rd + 9216); SB0(); }
    if constexpr (n >= P2 && n < P2 + 72 && (n - P2) % 9 == 4) {
      constexpr int U = (n - P2) / 9 + 1;
      ho[U & 1][0] = *(lds_u32x4*)(hh_rd + U * 1024); ho[U & 1][1] = *(lds_u32x4*)(hh_rd + 9216 + U * 1024);
      SB0();
    }
    if constexpr (FJ == 1) {
      if constexpr (SCH.xst[n] >= 0) {
        st128(xl[0], wsr, voff, xh_off + SCH.xst[n] * 1024);
        st128(xl[1], wsr, voff, xh_off + 9216 + SCH.xst[n] * 1024);
        SB0();
      }
      if constexpr (SCH.flag[n] >= 0) { __builtin_amdgcn_raw_buffer_store_b32((unsigned)SCH.flag[n], wsr, 0, fl_mine, 16); SB0(); }
      if constexpr (SCH.poll[n]) { asm volatile("global_load_dword %0, %1, %2 sc1" : "=&v"(pollv) : "v"(0u), "s"(fl_other) : "memory"); SB0(); }
      if constexpr (SCH.ld[n] >= 0) {
        constexpr int u = SCH.ld[n];
        bdma_sc1<u * 1024>(sx, voff, lds_a + K3_HH + mi * 18432);
        bdma_sc1<9216 + u * 1024>(sx, voff, lds_a + K3_HH + mi * 18432);
        SB0();
      }
      if constexpr (n == SCH.ebar) {
        if (mi == 0 && lane == 0) __hip_atomic_fetch_add((unsigned*)(P.ws + OFF_CNT + (mb * 2 + p) * 128), 1u, __ATOMIC_RELAXED, __HIP_MEMORY_SCOPE_AGENT);
        SB0();
      }
      if constexpr (SCH.pf[n] == 1 || SCH.pf[n] == 2) { pf_gu(mi, SCH.pf[n] == 1 || p ? 256 : 512); SB0(); }
      if constexpr (SCH.pf[n] == 3) {
        const int l41 = lane < 41 ? lane : 0, o = (((mb & 7) * 2 + p) * 4 + mi) * 10368 + l41 * 256;
        const char* pa = P.wd + (size_t)(c >> 1) * 663552 + (o < 663552 - 256 ? o : 663552 - 256);
        touch2(pa, lds_a + K3_DUMP);
        SB0();
      }
      if constexpr (SCH.xrd[n] >= 0) {
        xl[0] = *(lds_u32x4*)(hh_rd + SCH.xrd[n] * 1024); xl[1] = *(lds_u32x4*)(hh_rd + 9216 + SCH.xrd[n] * 1024);
        SB0();
      }
    }
    if constexpr (SCH.rd[n] >= 0) {
      constexpr int u = SCH.rd[n];
      hp[u][0] = *(lds_u32x4*)(hh_rd + u * 1024); hp[u][1] = *(lds_u32x4*)(hh_rd + 9216 + u * 1024);
      SB0();
    }
  });
  ST();
  MFMA_DRAIN();
#pragma unroll
  for (int i = 0; i < 8; ++i) opack(3, i);
  ostore(6);
  ostore(7);
  unsigned vo2 = voff;
  asm volatile("" : "+v"(vo2));
  const int ln = vo2 >> 4, gl = ln >> 4;
  const int j = c >> 1, t16 = mb * 8 + j, f0 = t16 * 18 + 9 * p;
  const int qr = p + wid, qs = p ? 0 : 8;
  const int l16 = 16 * mi + (ln & 15);
  const int row = t16 * 16 + (ln & 15);
  const int hb = ((ln & 15) + 16 * (gl >> 1)) * 16 + 8 * (gl & 1), hbs = ((ln & 15) + 16 * (mi >> 1)) * 16 + 8 * (mi & 1);
  const u32x2 h0 = __builtin_amdgcn_raw_buffer_load_b64(wsr, hb, (int)OFF_HC + (f0 + qr) * 1024, 16);
  const u32x2 h1 = __builtin_amdgcn_raw_buffer_load_b64(wsr, hb + 512, (int)OFF_HC + (f0 + qr) * 1024, 16);
  u32x2 hs0, hs1;
  if constexpr (FJ == 0) {
    hs0 = __builtin_amdgcn_raw_buffer_load_b64(wsr, hbs, (int)OFF_HC + (f0 + qs) * 1024, 16);
    hs1 = __builtin_amdgcn_raw_buffer_load_b64(wsr, hbs + 512, (int)OFF_HC + (f0 + qs) * 1024, 16);
  }
  const float mean = __uint_as_float(__builtin_amdgcn_raw_buffer_load_b32(wsr, row * 4, (int)OFF_MEANH, 16));
  const int cnt_off = (int)OFF_CNT + (mb * 2 + p) * 128;
  if constexpr (FJ == 0) poll_counters(P.ws, cnt_off, 8u);
  else WAITV(0);
  BAR();
  ST();
  if constexpr (FJ == 1) {
    if (mi == 0 && ln == 0) __hip_atomic_fetch_add((unsigned*)(P.ws + cnt_off + 64), 1u, __ATOMIC_RELAXED, __HIP_MEMORY_SCOPE_AGENT);
    poll_counters(P.ws, cnt_off + 64, 8u);
  }
  ST();
  auto area = [&](const int k) { const int ck = 2 * k + (p ^ 1); return (int)OFF_XH + (((mb & 7) * 4 + (ck & 3)) * 8 + (ck >> 2) * 2 + (mb >> 3)) * 73728 + j * 9216; };
  u32x4 pv[8], ps[2];
#pragma unroll
  for (int k = 0; k < 8; ++k) pv[k] = __builtin_amdgcn_raw_buffer_load_b128(wsr, voff, area(k) + qr * 1024, 16);
  if constexpr (FJ == 0) {
#pragma unroll
    for (int k = 0; k < 2; ++k) {
      const u32x2 lo = __builtin_amdgcn_raw_buffer_load_b64(wsr, area(4 * k + gl) + l16 * 8, qs * 1024, 16);
      const u32x2 hi = __builtin_amdgcn_raw_buffer_load_b64(wsr, area(4 * k + gl) + l16 * 8 + 512, qs * 1024, 16);
      ps[k] = (u32x4){lo[0], lo[1], hi[0], hi[1]};
    }
  }
  float o[8];
  o[0] = bflo(h0[0]); o[1] = bfhi(h0[0]); o[2] = bflo(h0[1]); o[3] = bfhi(h0[1]);
  o[4] = bflo(h1[0]); o[5] = bfhi(h1[0]); o[6] = bflo(h1[1]); o[7] = bfhi(h1[1]);
#pragma unroll
  for (int k = 0; k < 8; ++k) o[k] += mean;
#pragma unroll
  for (int r = 0; r < 8; ++r)
#pragma unroll
    for (int k = 0; k < 4; ++k) { o[2 * k] += bflo(pv[r][k]); o[2 * k + 1] += bfhi(pv[r][k]); }
  {
    char* d = P.out + (size_t)row * 1152 + (9 * p + qr) * 64 + gl * 8;
    *(u32x2*)d = (u32x2){pk2(o[0], o[1]), pk2(o[2], o[3])};
    *(u32x2*)(d + 32) = (u32x2){pk2(o[4], o[5]), pk2(o[6], o[7])};
  }
  if constexpr (FJ == 0) {
    float os[8];
#pragma unroll
    for (int k = 0; k < 4; ++k) { os[2 * k] = bflo(ps[0][k]) + bflo(ps[1][k]); os[2 * k + 1] = bfhi(ps[0][k]) + bfhi(ps[1][k]); }
#pragma unroll
    for (int k = 0; k < 8; ++k) { os[k] += __shfl_xor(os[k], 16); os[k] += __shfl_xor(os[k], 32); }
    if (ln < 16) {
      char* d = P.out + (size_t)row * 1152 + (9 * p + qs) * 64 + mi * 8;
      *(u32x2*)d = (u32x2){pk2(os[0] + bflo(hs0[0]) + mean, os[1] + bfhi(hs0[0]) + mean), pk2(os[2] + bflo(hs0[1]) + mean, os[3] + bfhi(hs0[1]) + mean)};
      *(u32x2*)(d + 32) = (u32x2){pk2(os[4] + bflo(hs1[0]) + mean, os[5] + bfhi(hs1[0]) + mean), pk2(os[6] + bflo(hs1[1]) + mean, os[7] + bfhi(hs1[1]) + mean)};
    }
  }
}

__global__ __launch_bounds__(512, 2) void k3_ffn(Ptrs P) {
  extern __shared__ __attribute__((aligned(16))) char smem[];
  const int lane = threadIdx.x & 63, wid = __builtin_amdgcn_readfirstlane(threadIdx.x >> 6);
  const int b = blockIdx.x;
  const int c = ((b & 7) >> 1) * 4 + ((b >> 3) & 3), mb = (b & 1) * 8 + (b >> 5);
  if (wid < 4) k3_body<0>(P, (lds_char*)smem, lane, wid, b, c, mb);
  else k3_body<1>(P, (lds_char*)smem, lane, wid - 4, b, c, mb);
}

constexpr int K0_IMG = 4608;
constexpr int K0_RED = 3 * K0_IMG;
constexpr int K0_DUMP = K0_RED + 9 * 8 * 8;
constexpr int K0_LDS = K0_DUMP + 256;

DI float dpp_sum8(float v) {
  v += __shfl_xor(v, 1); v += __shfl_xor(v, 2); v += __shfl_xor(v, 4);
  return v;
}

__global__ __launch_bounds__(768) void k0_prep(Ptrs P) {
  extern __shared__ __attribute__((aligned(16))) char smem[];
  lds_char* lds = (lds_char*)smem;
  const int lane = threadIdx.x & 63, wid = __builtin_amdgcn_readfirstlane(threadIdx.x >> 6);
  const int b = blockIdx.x;
  ST_DECL; ST();
  if (wid < 9) {
    const int row = b * 8 + (lane >> 3), i = lane & 7;
    const u32x4 v = *(const u32x4*)(P.x + (size_t)row * 1152 + wid * 128 + i * 16);
    float e[8];
#pragma unroll
    for (int k = 0; k < 4; ++k) { e[2 * k] = bflo(v[k]); e[2 * k + 1] = bfhi(v[k]); }
    const float sum = dpp_sum8(((e[0] + e[1]) + (e[2] + e[3])) + ((e[4] + e[5]) + (e[6] + e[7])));
    const float lm = sum * (1.f / 64.f);
    float m2 = 0.f;
#pragma unroll
    for (int k = 0; k < 8; ++k) { const float d = e[k] - lm; m2 = __builtin_fmaf(d, d, m2); }
    m2 = dpp_sum8(m2);
    if (i == 0) *(lds_f32x2*)(lds + K0_RED + (wid * 8 + (lane >> 3)) * 8) = (f32x2){lm, m2};
    WAITL0();
    BAR();
    ST();
    f32x2 pr[9];
#pragma unroll
    for (int w = 0; w < 9; ++w) pr[w] = *(lds_f32x2*)(lds + K0_RED + (w * 8 + (lane >> 3)) * 8);
    float mean = 0.f;
#pragma unroll
    for (int w = 0; w < 9; ++w) mean += pr[w][0];
    mean *= (1.f / 9.f);
    float tot = 0.f;
#pragma unroll
    for (int w = 0; w < 9; ++w) { const float d = pr[w][0] - mean; tot += __builtin_fmaf(64.f * d, d, pr[w][1]); }
    const float rstd = rsqrtf(tot * (1.f / D_) + LN_EPS);
    u32x4 o;
#pragma unroll
    for (int k = 0; k < 4; ++k) o[k] = pk2((e[2 * k] - mean) * rstd, (e[2 * k + 1] - mean) * rstd);
    WS_ST16(P, (int)OFF_XN + ((row >> 5) * 36 * 64 + (row & 31) + 32 * (i & 1) + (4 * wid + (i >> 1)) * 64) * 16, o, ST_AUX);
    if (wid == 0 && i == 0) WS_ST4(P, OFF_MEANX + row * 4, __float_as_uint(mean), ST_AUX);
    return;
  }
  const int id = (wid - 9) * 256 + b;
  lds_char* img = lds + (wid - 9) * K0_IMG;
  const unsigned img_a = (unsigned)(uintptr_t)img;
  const int g = lane >> 4, q = (lane >> 2) & 3, p = lane & 3;
  if (wid == 9) {
    constexpr unsigned NW = (D_ * F_ * 2 + TSTRIDE - 1) / TSTRIDE, NS_ = (unsigned)((WS_END + TSTRIDE - 1) / TSTRIDE), NO = (S_ * D_ * 2 + TSTRIDE - 1) / TSTRIDE;
    constexpr unsigned NT = 3 * NW + NS_ + NO;
    for (unsigned t = (b >> 3) * 64 + lane; t < ((NT + 2047) / 2048) * 2048; t += 2048) {
      const unsigned u = t < NT ? t : 0;
      const char* pp = u < NW ? P.wg + (size_t)u * TSTRIDE : u < 2 * NW ? P.wu + (size_t)(u - NW) * TSTRIDE : u < 3 * NW ? P.wd + (size_t)(u - 2 * NW) * TSTRIDE
                     : u < 3 * NW + NS_ ? P.ws + (size_t)(u - 3 * NW) * TSTRIDE : P.out + (size_t)(u - 3 * NW - NS_) * TSTRIDE;
      touch1(pp, (unsigned)(uintptr_t)lds + K0_DUMP);
    }
  }
  if (wid == 10 && b < 96) {
    unsigned long long pc, k1a;
    asm volatile("s_getpc_b64 %0" : "=s"(pc));
    asm volatile("s_getpc_b64 s[40:41]\n\ts_add_u32 s40, s40, _Z6k3_ffn4Ptrs@rel32@lo+4\n\ts_addc_u32 s41, s41, _Z6k3_ffn4Ptrs@rel32@hi+12\n\t"
                 "s_mov_b64 %0, s[40:41]" : "=s"(k1a) :: "s40", "s41", "scc");
    const char* pp = (const char*)k1a + ((b >> 3) * 64 + lane) * 128;
    pp = pp < (const char*)pc ? pp : (const char*)k1a;
    touch1(pp, (unsigned)(uintptr_t)lds + K0_DUMP);
  }
  if (id < 576) {
    const int s = id >> 4, h = (id >> 3) & 1, hp0 = (id & 7) * 2;
    const char* src = P.wqkv + (size_t)(16 * s + 8 * h) * 3456 + hp0 * 72;
#pragma unroll
    for (int it = 0; it < 4; ++it) {
      int e = it * 64 + lane; e = e < 216 ? e : 215;
      const int sgm = e / 9, ch = e - sgm * 9;
      dma16x1(src, sgm * 1152 + ch * 16, img_a + it * 1024);
    }
    BAR();
    ST();
    WAITV(0);
    ST();
#pragma unroll
    for (int it = 0; it < 4; ++it) {
      int Q = 16 * it + 4 * g + p; Q = Q < 54 ? Q : 53;
      const int hl = Q >= 27 ? 1 : 0, sg = Q - 27 * hl;
      const int cb = sg < 4 ? 8 * sg : (sg < 8 ? 8 * sg + 8 : (sg == 8 ? 32 : (sg < 18 ? 8 * sg + 72 : 8 * sg + 144)));
      lds_char* a = img + q * 432 + cb + 72 * hl;
      u32x4 v = tr_frag(a, a + 4 * 432);
      const int o = it * 64 + lane, ol = o >= 108 ? 1 : 0, slot = o - 108 * ol;
      const int off = slot < 32 ? WQT_Q + s * 1024 + (h * 32 + slot) * 16 : s * 2432 + (h * 76 + slot - 32) * 16;
      if (o < 216) WS_ST16(P, OFF_WQT + (hp0 + ol) * (int)WQT_HP + off, v, ST_AUX);
    }
  } else if (id < 720) {
    const int j = id - 576, ks = j >> 2, cb = j & 3;
#pragma unroll
    for (int it = 0; it < 5; ++it) {
      int e = it * 64 + lane; e = e < 288 ? e : 287;
      const int kk = e / 18, ch = e - kk * 18, h = kk >> 3, jj = kk & 7;
      const int row = ks < 32 ? 18 * ks + (jj & 3) + 8 * (jj >> 2) + 4 * h : 18 * (8 * (ks - 32) + jj) + 16 + h;
      if (it < 4 || lane < 32) dma16x1(P.wo + cb * 288, row * 1152 + ch * 16, img_a + it * 1024);
    }
    BAR();
    WAITV(0);
#pragma unroll
    for (int it = 0; it < 5; ++it) {
      int G16 = 4 * it + g; G16 = G16 < 18 ? G16 : 17;
      const int h = G16 >= 9 ? 1 : 0, sg = G16 - 9 * h;
      lds_char* a = img + (8 * h + q) * 288 + (16 * sg + 4 * p) * 2;
      u32x4 v = tr_frag(a, a + 4 * 288);
      if (it < 4 || lane < 32) WS_ST16(P, OFF_WOT + cb * (int)WOT_CB + ks * 4672 + (h * 146 + 16 * sg + (lane & 15)) * 16, v, ST_AUX);
    }
  } else if (id < 756) {
    const int row = (id - 720) * 16 + (lane >> 2), i = lane & 3;
    const char* src = P.wo + (size_t)row * 1152 + i * 16;
    u32x4 v[18];
#pragma unroll
    for (int j = 0; j < 18; ++j) v[j] = *(const u32x4*)(src + j * 64);
    BAR();
    float s4[4] = {0.f, 0.f, 0.f, 0.f};
    const bf16x2 one2 = __builtin_bit_cast(bf16x2, 0x3f803f80u);
#pragma unroll
    for (int j = 0; j < 18; ++j)
#pragma unroll
      for (int k = 0; k < 4; ++k) { const unsigned t = v[j][k]; s4[k] = __builtin_amdgcn_fdot2_f32_bf16(__builtin_bit_cast(bf16x2, t), one2, s4[k], false); }
    float sum = (s4[0] + s4[1]) + (s4[2] + s4[3]);
    sum += __shfl_xor(sum, 1); sum += __shfl_xor(sum, 2);
    const float mean = sum * (1.f / D_);
    const int hd = row / 18, e = row % 18;
    const int ks = e < 16 ? hd : 32 + (hd >> 3), jj = e < 16 ? (e & 3) + 4 * (e >> 3) : (hd & 7), hh = e < 16 ? (e >> 2) & 1 : e - 16;
    const unsigned hi = pk2(mean, 0.f) & 0xffffu;
    const unsigned lo = pk2(mean - bflo(hi), 0.f) & 0xffffu;
    unsigned short* d = (unsigned short*)(P.ws + OFF_WOT + (size_t)i * WOT_CB + ks * 4672 + (hh * 146 + 144) * 16 + jj * 2);
    d[0] = (unsigned short)hi; d[8] = (unsigned short)lo;
  } else {
    const u32x4 z = {0u, 0u, 0u, 0u};
    for (int k = id - 756; k < 50; k += 12) WS_ST16(P, OFF_KV + k * 1024 + lane * 16, z, ST_AUX);
    for (int k = id - 756; k < 70; k += 12) WS_ST16(P, OFF_FLAG + k * 1024 + lane * 16, z, ST_AUX);
    BAR();
  }
}

extern "C" void kernel_launch(void* const* d_in, const int* in_sizes, int n_in, void* d_out, int out_size, void* d_ws,
                              size_t ws_size, hipStream_t stream) {
  Ptrs P;
  P.x = (const char*)d_in[0]; P.wqkv = (const char*)d_in[1]; P.wo = (const char*)d_in[2];
  P.wg = (const char*)d_in[4]; P.wu = (const char*)d_in[5]; P.wd = (const char*)d_in[6];
  P.ws = (char*)d_ws; P.out = (char*)d_out;
  (void)hipFuncSetAttribute((const void*)k3_ffn, hipFuncAttributeMaxDynamicSharedMemorySize, K3_LDS);
  k0_prep<<<256, 768, K0_LDS, stream>>>(P);
  k3_ffn<<<256, 512, K3_LDS, stream>>>(P);
}
```

```cpp
#include <hip/hip_runtime.h>
#include <hip/hip_bf16.h>
#include <stdint.h>
#include <type_traits>

typedef __attribute__((ext_vector_type(8))) short bf16x8;
typedef __attribute__((ext_vector_type(4))) short s16x4;
typedef __attribute__((ext_vector_type(16))) float f32x16;
typedef __attribute__((ext_vector_type(4))) float f32x4;
typedef __attribute__((ext_vector_type(2))) float f32x2;
typedef __attribute__((ext_vector_type(2))) __bf16 bf16x2;
typedef __attribute__((ext_vector_type(4))) unsigned u32x4;
typedef __attribute__((ext_vector_type(2))) unsigned u32x2;
typedef __attribute__((address_space(3))) char lds_char;
typedef __attribute__((address_space(3))) s16x4 lds_s16x4;
typedef __attribute__((address_space(3))) u32x4 lds_u32x4;
typedef __attribute__((address_space(3))) u32x2 lds_u32x2;
typedef __attribute__((address_space(3))) f32x4 lds_f32x4;
typedef __attribute__((address_space(3))) float lds_float;
typedef __attribute__((address_space(3))) f32x2 lds_f32x2;
typedef __attribute__((address_space(3))) unsigned lds_uint;

#define DI __device__ __forceinline__
#define MFMA32(a, b, c) __builtin_amdgcn_mfma_f32_32x32x16_bf16((a), (b), (c), 0, 0, 0)
#define WAITV(n) asm volatile("s_waitcnt vmcnt(" #n ")" ::: "memory")
#define WAITL0() asm volatile("s_waitcnt lgkmcnt(0)" ::: "memory")
#define BAR() asm volatile("s_barrier" ::: "memory")
#define SB0() __builtin_amdgcn_sched_barrier(0)

constexpr int S_ = 2048, D_ = 576, F_ = 4608;
constexpr float LN_EPS = 1e-5f;

constexpr size_t FLB = (size_t)S_ * D_ * 2;
constexpr size_t WQT_HP = 36 * 3456;
constexpr int WQT_Q = 36 * 2432;
constexpr size_t WOT_CB = 36 * 4672;
constexpr size_t OFF_XN = 0;
constexpr size_t OFF_PQ = OFF_XN + FLB;
constexpr size_t OFF_HC = OFF_PQ + FLB;
constexpr size_t OFF_MEANX = OFF_HC + FLB;
constexpr size_t OFF_MEANH = OFF_MEANX + 8192;
constexpr size_t OFF_SSQ = OFF_MEANH + 8192;
constexpr size_t OFF_KV = OFF_SSQ + 20 * 8192 + 4096;
constexpr size_t OFF_WQT = OFF_KV + 53248;
constexpr size_t OFF_WOT = OFF_WQT + 16 * WQT_HP + 16384;
constexpr size_t OFF_XH = OFF_WOT + 4 * WOT_CB + 4096;
constexpr size_t OFF_FLAG = OFF_XH + 256 * 73728;
constexpr size_t OFF_CNT = OFF_FLAG + 256 * 128;
constexpr size_t OFF_G1 = OFF_CNT + 32 * 128;
constexpr size_t OFF_G3 = OFF_G1 + 256 * 128;
constexpr size_t WS_END = OFF_G3 + 16 * 128;
static_assert(OFF_XH % 1024 == 0 && (WS_END - OFF_FLAG) == 70 * 1024, "workspace");

DI unsigned pk2(float a, float b) {
  f32x2 v = {a, b};
  bf16x2 r = __builtin_convertvector(v, bf16x2);
  return __builtin_bit_cast(unsigned, r);
}
DI float bflo(unsigned u) { return __uint_as_float(u << 16); }
DI float bfhi(unsigned u) { return __uint_as_float(u & 0xffff0000u); }
DI float phi(float t) {
  const float e = __builtin_amdgcn_fmed3f(__builtin_amdgcn_exp2f(t * 1.4426950408889634f), 0.f, 1.f);
  return (__builtin_amdgcn_fmed3f(t, 0.f, __builtin_inff()) + 1e-6f) + e;
}
DI bf16x8 as_frag(u32x4 v) { return __builtin_bit_cast(bf16x8, v); }

DI void dma16x3s(const void* sbase, unsigned voff, unsigned lds_dst) {
  unsigned keep;
  asm volatile("s_mov_b32 %0, m0\n\ts_mov_b32 m0, %3\n\ts_nop 0\n\t"
               "global_load_lds_dwordx4 %1, %2 sc1\n\t"
               "global_load_lds_dwordx4 %1, %2 offset:1024 sc1\n\t"
               "global_load_lds_dwordx4 %1, %2 offset:2048 sc1\n\t"
               "s_mov_b32 m0, %0"
               : "=&s"(keep) : "v"(voff), "s"(sbase), "s"(lds_dst) : "memory");
}
DI void dma16x1(const void* sbase, unsigned voff, unsigned lds_dst) {
  unsigned keep;
  asm volatile("s_mov_b32 %0, m0\n\ts_mov_b32 m0, %3\n\ts_nop 0\n\t"
               "global_load_lds_dwordx4 %1, %2\n\t"
               "s_mov_b32 m0, %0"
               : "=&s"(keep) : "v"(voff), "s"(sbase), "s"(lds_dst) : "memory");
}
DI void touch1(const void* pa, unsigned lds_dump) {
  unsigned keep;
  asm volatile("s_mov_b32 %0, m0\n\ts_mov_b32 m0, %2\n\ts_nop 0\n\tglobal_load_lds_dword %1, off\n\ts_mov_b32 m0, %0"
               : "=&s"(keep) : "v"(pa), "s"(lds_dump) : "memory");
}
DI void touch2(const void* pa, unsigned lds_dump) {
  unsigned keep;
  asm volatile("s_mov_b32 %0, m0\n\ts_mov_b32 m0, %2\n\ts_nop 0\n\tglobal_load_lds_dword %1, off\n\t"
               "global_load_lds_dword %1, off offset:128\n\ts_mov_b32 m0, %0"
               : "=&s"(keep) : "v"(pa), "s"(lds_dump) : "memory");
}
DI u32x4 tr_frag(lds_char* p0, lds_char* p1) {
  s16x4 lo = __builtin_amdgcn_ds_read_tr16_b64_v4i16((lds_s16x4*)p0);
  s16x4 hi = __builtin_amdgcn_ds_read_tr16_b64_v4i16((lds_s16x4*)p1);
  u32x2 a = __builtin_bit_cast(u32x2, lo), b = __builtin_bit_cast(u32x2, hi);
  u32x4 r = {a[0], a[1], b[0], b[1]};
  return r;
}

#define ST_AUX 16
#define WS_RSRC(P) __builtin_amdgcn_make_buffer_rsrc((P).ws, 0, (int)WS_END, 0x00020000)
#define WS_ST16(P, off, v, aux) __builtin_amdgcn_raw_buffer_store_b128((v), WS_RSRC(P), (int)(off), 0, (aux))
#define WS_ST8(P, off, v, aux) __builtin_amdgcn_raw_buffer_store_b64((v), WS_RSRC(P), (int)(off), 0, (aux))
#define WS_ST4(P, off, v, aux) __builtin_amdgcn_raw_buffer_store_b32((v), WS_RSRC(P), (int)(off), 0, (aux))
template <class R> DI void st128(u32x4 v, R rsrc, int voff, int soff) {
  __builtin_amdgcn_raw_buffer_store_b128(v, rsrc, voff, soff, 16);
  asm volatile("s_nop 1" :: "v"(v));
}
struct Ptrs {
  const char* x; const char* wqkv; const char* wo; const char* wg; const char* wu; const char* wd;
  char* ws; char* out;
};

constexpr unsigned TSTRIDE = 2097152;
constexpr int G1R = 16;

constexpr int K1_W = 0;
constexpr int KVP = 192;
constexpr int K2_O = 49152;
constexpr int K2_RED = K2_O + 36864;
constexpr int K2_MEAN = K2_RED + 32768;

DI void poll_counters(const char* base, const int off, const unsigned need) {
#pragma unroll 1
  for (int spin = 0; spin < (1 << 22); ++spin) {
    unsigned v;
    asm volatile("global_load_dword %0, %1, %2 sc1\n\ts_waitcnt vmcnt(0)" : "=&v"(v) : "v"(off), "s"(base) : "memory");
    if (__builtin_amdgcn_ballot_w64(v < need) == 0ull) break;
    __builtin_amdgcn_s_sleep(2);
  }
}

constexpr int K3_RING = 0;
constexpr int K3_HH = 73728;
constexpr int K3_SSQ = K3_HH + 73728;
constexpr int K3_DUMP = K3_SSQ + 4 * 3072;
constexpr int K3_LDS = K3_DUMP + 512;
constexpr int PF0 = 69, PFD1 = 48, PFD2 = 96;
constexpr int EBAR = 156, XOFF = 1;
constexpr int NS = 486, PH = 288, P2 = 324;

struct Sch {
  signed char own[162] = {}, u[162] = {}, db[162] = {}, t01[162] = {};
  short rs[NS + 4] = {}, lo[NS + 4] = {};
  signed char sown[90] = {}, su[90] = {}, sdb[90] = {}, pf[NS] = {};
  bool bar[NS] = {}, l2[NS] = {};
  signed char dslot[NS] = {}, dk[NS] = {};
  signed char e0[NS] = {}, ne0[NS] = {}, ne1[NS] = {}, eg[NS] = {};
  signed char og[NS] = {}, o0[NS] = {};
  signed char xrd[NS] = {}, cbar[NS] = {}, xst[NS] = {}, ost[NS] = {}, xop[NS] = {};
  signed char flag[NS] = {}, fbar[NS] = {}, poll[NS] = {}, chk[NS] = {}, ld[NS] = {}, rd[NS] = {};
  signed char wl[NS] = {}, wf[NS] = {};
  int ebar = 0;
  bool ok = true;
};
constexpr Sch make_sch() {
  Sch s;
  int n2 = 0, nb = 0;
  auto put = [&](int own, int u, int db) {
    s.sown[nb] = own; s.su[nb] = u; s.sdb[nb] = db;
    for (int t = 0; t < (db == 4 ? 1 : 2); ++t) {
      s.own[n2] = own; s.u[n2] = u; s.db[n2] = db; s.t01[n2] = t;
      s.rs[P2 + n2] = 27 + nb / 6; s.lo[P2 + n2] = ((27 + nb / 6) % 3) * 24 + (nb % 6) * 4; ++n2;
    }
    ++nb;
  };
  for (int u = 0; u < 9; ++u) for (int d = 0; d < 5; ++d) put(1, u, d);
  for (int u = 0; u < 6; ++u) { put(0, u, 4); put(0, u, 0); put(0, u, 1); }
  for (int u = 6; u < 9; ++u) { put(0, u, 4); put(0, u, 0); }
  for (int u = 6; u < 9; ++u) put(0, u, 1);
  for (int d = 2; d < 4; ++d) for (int u = 0; u < 9; ++u) put(0, u, d);
  for (int n = 0; n < PH; ++n) { s.rs[n] = n / 12; s.lo[n] = ((n / 12) % 3) * 24 + ((n % 12) >> 1) * 4; }
  for (int n = PH; n < P2; ++n) { s.rs[n] = n / 12; s.lo[n] = ((n / 12) % 3) * 24 + (n % 12) * 2; }
  for (int n = NS; n < NS + 4; ++n) { s.rs[n] = s.rs[NS - 1]; s.lo[n] = s.lo[NS - 1]; }
  for (int n = 0; n < NS; ++n) {
    s.bar[n] = s.rs[n + 3] != s.rs[n + 2];
    s.dslot[n] = -1; s.eg[n] = -1; s.og[n] = -1; s.flag[n] = -1; s.fbar[n] = -1; s.poll[n] = 0; s.chk[n] = -1;
    s.ld[n] = -1; s.rd[n] = -1; s.wl[n] = -1; s.wf[n] = -1; s.xst[n] = -1; s.ost[n] = -1; s.xrd[n] = -1; s.cbar[n] = -1; s.pf[n] = 0;
  }
  for (int n = 0; n < NS; ++n) if (s.bar[n]) {
    int e = n + 1; while (e < NS && !s.bar[e]) ++e;
    const int L = e - n, tgt = s.rs[n + 3] + 2;
    for (int k = 0; k < 6; ++k) {
      const int t = k == 0 ? tgt - 1 : tgt, m = n + (k * L) / 6 + 1;
      if (t < 3 || t > 41) continue;
      s.dslot[m] = t; s.dk[m] = k == 0 ? 5 : k - 1;
      if (m >= e) s.ok = false;
    }
  }
  auto nbar = [&](int n) { while (!s.bar[n]) ++n; return n; };
  for (int g = 0; g < 5; ++g) {
    const int start = (g < 4 ? 72 * (g + 1) : P2) + 2, tot = g == 4 ? 46 : 92;
    int e = 0;
    for (int n = start; e < tot; ++n) {
      const int c0 = g == 4 ? 2 : 1, c1 = g >= 3 ? 2 : ((n - start) & 1);
      if (s.eg[n] >= 0) s.ok = false;
      s.eg[n] = g; s.e0[n] = e; s.ne0[n] = c0; s.ne1[n] = c1;
      for (int i = e; i < e + c0 + c1 && i < tot; ++i) if (i % 46 == 45) { int m = n + 1; while (!s.bar[m]) ++m; m += XOFF; while (s.xrd[m] >= 0) m += 3; s.xrd[m] = 2 * g + i / 46; s.xst[m + 2] = 2 * g + i / 46; }
      e += c0 + c1;
      if (g < 3 && n >= start + 68) s.ok = false;
    }
  }
  s.og[P2 + 119] = 4; s.o0[P2 + 119] = 0; s.og[P2 + 120] = 4; s.o0[P2 + 120] = 4; s.ost[P2 + 120] = 8; s.ost[P2 + 122] = 9;
  const int opk[3] = {121, 128, 145};
  for (int k = 0; k < 3; ++k) {
    for (int i = 0; i < 4; ++i) { const int n = P2 + opk[k] + i; s.og[n] = k; s.o0[n] = 2 * i; }
    s.ost[P2 + opk[k] + 3] = 2 * k; s.ost[P2 + opk[k] + 6] = 2 * k + 1;
  }
  int lds_[9] = {16, 18, 24, 34, 43, 44, 76, 78, 80}, rds[9] = {};
  for (int u = 0; u < 9; ++u) {
    const int cbn = nbar(P2 + lds_[(u / 3) * 3 + 2] + 17);
    rds[u] = cbn - P2 + 1 + (u < 6 ? 3 : 2) * (u % 3);
    s.ld[P2 + lds_[u]] = u; s.rd[P2 + rds[u]] = u; s.cbar[cbn] = u;
  }
  s.poll[P2 + 3] = 1; s.chk[P2 + 15] = 6;
  s.poll[P2 + 63] = 1; s.chk[P2 + 75] = 9;
  s.xop[28] = 3; s.xop[40] = 3;
  s.pf[PF0] = 1; s.pf[PF0 + PFD1] = 2; s.pf[PF0 + PFD2] = 3;
  const int fb[2] = {PH + 21, nbar(P2 + 40)}, fv[2] = {6, 9};
  for (int k = 0; k < 2; ++k) { if (!s.bar[fb[k]]) s.ok = false; s.fbar[fb[k]] = fv[k]; s.flag[fb[k] + 1] = fv[k]; }
  s.ebar = P2 + EBAR;
  for (int n = 1; n < NS; ++n) {
    const int m = n - 1;
    bool extra = m == 9 || m == 21 || m == 45 || m == 57 || m == 48 || m == 60 || m == P2 - 1 || s.rd[m] >= 0 || s.xrd[m] >= 0 || (m >= P2 && m < P2 + 72 && (m - P2) % 9 == 4);
    if (s.eg[m] >= 0) for (int i = s.e0[m]; i < s.e0[m] + s.ne0[m] + s.ne1[m]; ++i) if (i % 23 == 22) extra = true;
    s.l2[n] = !extra;
  }
  int cl[NS + 1] = {}, cf[NS + 1] = {};
  for (int n = 9; n < NS; ++n) {
    const int both = (s.ost[n] >= 0 ? 1 : 0) + s.xop[n];
    cl[n + 1] = cl[n] + both + (s.dslot[n] >= 0 ? 1 : 0);
    cf[n + 1] = cf[n] + both + (s.xst[n] >= 0 ? 2 : 0) + (s.flag[n] >= 0 ? 1 : 0) + s.poll[n] + (s.ld[n] >= 0 ? 2 : 0) + (s.pf[n] ? 2 : 0);
  }
  auto need = [&](int n, int ul_, int uf_) {
    if (ul_ >= 0) { const int w = cl[n] - ul_; if (s.wl[n] < 0 || w < s.wl[n]) s.wl[n] = w; }
    if (uf_ >= 0) { const int w = cf[n] - uf_; if (s.wf[n] < 0 || w < s.wf[n]) s.wf[n] = w; }
  };
  for (int n = 21; n < NS; ++n) {
    if (s.bar[n]) {
      int last = -1;
      for (int i = 9; i < n; ++i) if (s.dslot[i] == s.rs[n + 3]) last = i;
      need(n, last >= 0 ? cl[last] + 1 : 0, n == 21 ? 0 : -1);
      if (n == 45) need(n, cl[29], cf[29]);
      if (n == 57) need(n, cl[41], cf[41]);
      if (s.fbar[n] >= 0) {
        int at = -1;
        for (int i = 9; i < n; ++i) if (s.xst[i] == s.fbar[n] - 1) at = i;
        if (at < 0) s.ok = false;
        for (int i = 9; i < n; ++i) if (s.xst[i] >= 0 && s.xst[i] < s.fbar[n] && i > at) at = i;
        need(n, -1, cf[at + 1]);
      }
    }
    if (n == s.ebar) need(n, cl[P2 + 135], cf[P2 + 135]);
    if (s.chk[n] >= 0) { int i = n; while (!s.poll[i]) --i; need(n, -1, cf[i + 1]); }
    if (s.cbar[n] >= 0) { int i = n; while (s.ld[i] != s.cbar[n]) --i; need(n, -1, cf[i + 1]); }
  }
  for (int u = 0; u < 9; ++u) {
    int own_rd = u == 0 ? P2 - 1 : P2 + 9 * (u - 1) + 4;
    for (int n = 0; n < NS; ++n) if (s.xrd[n] == u && n > own_rd) own_rd = n;
    bool b1 = false;
    for (int n = own_rd + 1; n <= P2 + lds_[u]; ++n) b1 = b1 || s.bar[n];
    int use = 81; while (s.u[use] != u) ++use;
    if (!b1 || rds[u] + 2 > use) s.ok = false;
  }
  return s;
}
constexpr Sch SCH = make_sch();
static_assert(SCH.ok, "schedule");

template <int I, int N, class F> DI void static_for(F&& f) {
  if constexpr (N - I == 1) f(std::integral_constant<int, I>{});
  else if constexpr (N - I > 1) { static_for<I, (I + N) / 2>(f); static_for<(I + N) / 2, N>(f); }
}
template <int N> DI void waitv() { asm volatile("s_waitcnt vmcnt(%0)" :: "n"(N) : "memory"); }
#define MFMA_DRAIN() do { asm volatile("s_nop 7\n\ts_nop 3" ::: "memory"); SB0(); } while (0)
typedef __attribute__((ext_vector_type(4))) int srd_t;
DI srd_t make_srd(const void* p, unsigned bytes) {
  const unsigned long long a = (unsigned long long)p;
  srd_t r = {(int)(unsigned)a, (int)((unsigned)(a >> 32) & 0xffffu), (int)bytes, 0x00020000};
  return r;
}
template <int LOFF, int SOFF> DI void bdma_i(srd_t srd, unsigned voff, unsigned sbase, unsigned lbase) {
  unsigned so;
  asm volatile("s_add_u32 m0, %4, %6\n\ts_add_u32 %0, %3, %5\n\tbuffer_load_dwordx4 %1, %2, %0 offen lds"
               : "=&s"(so) : "v"(voff), "s"(srd), "s"(sbase), "s"(lbase), "n"(SOFF), "n"(LOFF) : "memory", "scc");
}

template <int OFF> DI void bdma_sc1(srd_t srd, unsigned voff, unsigned lbase) {
  unsigned so;
  asm volatile("s_add_u32 m0, %3, %4\n\ts_mov_b32 %0, %4\n\tbuffer_load_dwordx4 %1, %2, %0 offen sc1 lds"
               : "=&s"(so) : "v"(voff), "s"(srd), "s"(lbase), "n"(OFF) : "memory", "scc");
}

template <int FJ>
DI void k3_body(const Ptrs& P, lds_char* lds, const int lane, const int mi, const int b, const int c, const int mb) {
  constexpr int fj = FJ;
  const unsigned lds_a = (unsigned)(uintptr_t)lds;
  const int mt = mb * 4 + mi, p = c & 1;
  const int g = lane >> 4;
  const unsigned voff = lane * 16;
  const srd_t sd = make_srd(P.wd, D_ * F_ * 2);
  const srd_t sg = make_srd(P.wg, D_ * F_ * 2), su = make_srd(P.wu, D_ * F_ * 2);
  const auto wsr = WS_RSRC(P);
  const int wid = fj * 4 + mi;
  const unsigned sgu = 8 * mi * (F_ * 2) + 576 * c + 64 * p;
  const unsigned ring_w = lds_a + K3_RING + mi * 1024;
  auto vgu_f = [&]() { return (unsigned)((lane >> 3) * (F_ * 2) + ((((lane >> 1) & 3) ^ (((lane >> 4) & 1) | ((mi & 1) << 1))) * 32) + (lane & 1) * 16); };
  auto first_slot = [&](auto tc) __attribute__((always_inline)) {
    constexpr int t = decltype(tc)::value;
    static_for<3 * FJ, 3 * FJ + 3>([&](auto kc) __attribute__((always_inline)) {
      constexpr int k = decltype(kc)::value;
      bdma_i<t * 24576 + k * 4096, 32 * (3 * t + (k >> 1)) * (F_ * 2)>((k & 1) ? su : sg, vgu_f(), sgu, ring_w);
    });
  };
  auto pf_gu = [&](const int i, const int col) {
    const int l36 = lane < 36 ? lane : 0, mat = l36 >= 18 ? 1 : 0, row = ((mb & 7) * 4 + i) * 18 + l36 - 18 * mat;
    const char* pa = (mat ? P.wu : P.wg) + (size_t)row * (F_ * 2) + 576 * c + 64 * p + col;
    touch2(pa, lds_a + K3_DUMP);
  };
  {
    const int r = lane & 31, h = lane >> 5;
    const int hp = c;
    const int mt2 = mb * 4 + (c >> 2), cb = c & 3;
    u32x4 af[18], a4[5], xv0, xv1;
    float mean;
    const int k4 = FJ == 0 ? 5 * mi : 20 + 4 * mi;
    constexpr int n4 = FJ == 0 ? 5 : 4;
    const int row = mt2 * 32 + r, s0 = cb * 9 + 2 * (FJ == 0 ? mi : 4);
    auto prefetch2 = [&]() __attribute__((always_inline)) {
      const char* wo = P.ws + OFF_WOT + (size_t)cb * WOT_CB + h * 2336;
#pragma unroll
      for (int i = 0; i < 18; ++i) af[i] = *(const u32x4*)(wo + (18 * FJ + i) * 4672 + (32 * mi + r) * 16);
#pragma unroll
      for (int i = 0; i < n4; ++i) a4[i] = *(const u32x4*)(wo + (k4 + i) * 4672 + (128 + (r < 17 ? r : 17)) * 16);
      xv0 = *(const u32x4*)(P.x + (size_t)row * 1152 + s0 * 32 + h * 16);
      xv1 = *(const u32x4*)(P.x + (size_t)row * 1152 + (FJ == 0 ? s0 + 1 : s0) * 32 + h * 16);
      mean = ((const float*)(P.ws + OFF_MEANX))[row];
      SB0();
    };
    if constexpr (FJ == 1) {
      const char* wsrc = P.ws + OFF_WQT + (size_t)hp * WQT_HP + mi * 1024;
      const unsigned wdst = lds_a + K1_W + mi * 1024;
#pragma unroll
      for (int i = 0; i < 14; ++i) dma16x1(wsrc + i * 4096, voff, wdst + i * 4096);
      WAITV(8); BAR();
#pragma unroll
      for (int i = 14; i < 20; ++i) dma16x1(wsrc + i * 4096, voff, wdst + i * 4096);
      WAITV(9); BAR();
#pragma unroll
      for (int i = 20; i < 26; ++i) dma16x1(wsrc + i * 4096, voff, wdst + i * 4096);
      WAITV(9); BAR();
#pragma unroll
      for (int i = 26; i < 31; ++i) dma16x1(wsrc + i * 4096, voff, wdst + i * 4096);
      WAITV(9); BAR();
      WAITV(0);
      prefetch2();
      if (mi >= 2) { pf_gu((mi & 1) * 2, 0); pf_gu((mi & 1) * 2 + 1, 0); }
      BAR();
      if (mi < 2) {
        __builtin_amdgcn_s_setprio(3);
        const int q = (lane >> 2) & 3, op = 4 * (g & 1) + (lane & 3);
        const int vq = op >= 5 ? 19 : (mi ? 14 : 10) + op, kq = (mi ? 5 : 1) + op;
        lds_char* va = lds + (8 * (g >> 1) + q) * KVP + vq * 8;
        lds_char* ka = lds + (8 * (g >> 1) + q) * KVP + kq * 8;
        f32x16 kv;
#pragma unroll
        for (int i = 0; i < 16; ++i) kv[i] = 0.f;
#pragma unroll
        for (int ks = 0; ks < 8; ++ks) {
          u32x4 a = tr_frag(va + ks * 16 * KVP, va + ks * 16 * KVP + 4 * KVP);
          u32x4 bb = tr_frag(ka + ks * 16 * KVP, ka + ks * 16 * KVP + 4 * KVP);
          kv = MFMA32(as_frag(a), as_frag(bb), kv);
        }
        const int d = r - 2 * mi;
        const int dd = mi ? (d >= 2 ? d - 2 : 16 + d) : d;
        float* dst = (float*)(P.ws + OFF_KV) + (2 * hp + mi) * 400 + dd;
        const bool dok = d >= 0 && d < 18;
#pragma unroll
        for (int i = 0; i < 12; ++i) {
          const int rw = (i & 3) + 8 * (i >> 2) + 4 * h;
          const int e = rw == 20 ? 18 : rw - 2 * mi;
          if (dok && (rw == 20 || (e >= 0 && e < 18 && rw < 20))) atomicAdd(dst + e * 20, kv[i]);
        }
        WAITV(0);
      }
    } else {
      u32x4 xf[36];
      const char* xsrc = P.ws + OFF_XN + (size_t)mt * 36864 + lane * 16;
#pragma unroll
      for (int s = 0; s < 18; ++s) xf[s] = *(const u32x4*)(xsrc + s * 1024);
      f32x16 acc[3], accq;
#pragma unroll
      for (int i = 0; i < 16; ++i) { acc[0][i] = 0.f; acc[1][i] = 0.f; acc[2][i] = 0.f; accq[i] = 0.f; }
      lds_char* wa = lds + K1_W + (h * 76 + r) * 16;
      lds_char* wa2 = lds + K1_W + (h * 76 + 64 + (r < 12 ? r : 11)) * 16;
      u32x4 wf[2][3];
#pragma unroll
      for (int s = 0; s < 36; ++s) {
        if (s % 9 == 0) {
          BAR();
          if (s < 18) {
#pragma unroll
            for (int k = s + 18; k < s + 27; ++k) xf[k] = *(const u32x4*)(xsrc + k * 1024);
          }
          wf[s & 1][0] = *(lds_u32x4*)(wa + s * 2432); wf[s & 1][1] = *(lds_u32x4*)(wa + s * 2432 + 512); wf[s & 1][2] = *(lds_u32x4*)(wa2 + s * 2432);
        }
        if (s % 9 != 8) {
          wf[(s + 1) & 1][0] = *(lds_u32x4*)(wa + (s + 1) * 2432); wf[(s + 1) & 1][1] = *(lds_u32x4*)(wa + (s + 1) * 2432 + 512);
          wf[(s + 1) & 1][2] = *(lds_u32x4*)(wa2 + (s + 1) * 2432);
        }
        SB0();
#pragma unroll
        for (int t = 0; t < 3; ++t) acc[t] = MFMA32(as_frag(wf[s & 1][t]), as_frag(xf[s]), acc[t]);
        SB0();
      }
      lds_char* kvrow = lds + (mi * 32 + r) * KVP;
      u32x2 qrem;
#pragma unroll
      for (int gi = 0; gi < 10; ++gi) {
        const int t = gi >> 2, gg = gi & 3;
        u32x2 o;
        if (gi < 5) { o[0] = pk2(phi(acc[t][4 * gg]), phi(acc[t][4 * gg + 1])); o[1] = pk2(phi(acc[t][4 * gg + 2]), phi(acc[t][4 * gg + 3])); }
        else { o[0] = pk2(acc[t][4 * gg], acc[t][4 * gg + 1]); o[1] = pk2(acc[t][4 * gg + 2], acc[t][4 * gg + 3]); }
        if (gi == 9 && h == 1) { o[0] = 0x3f803f80u; o[1] = 0x3f803f80u; }
        *(lds_u32x2*)(kvrow + (2 * gi + h) * 8) = o;
        if (gi == 0) qrem = o;
      }
      WAITL0();
      BAR();
      lds_char* wq = lds + K1_W + WQT_Q + lane * 16;
      u32x4 aq[2][4];
#pragma unroll
      for (int i = 0; i < 4; ++i) aq[0][i] = *(lds_u32x4*)(wq + i * 1024);
#pragma unroll
      for (int g3 = 0; g3 < 9; ++g3) {
        if (g3 < 8) {
#pragma unroll
          for (int i = 0; i < 4; ++i) aq[(g3 + 1) & 1][i] = *(lds_u32x4*)(wq + (4 * g3 + 4 + i) * 1024);
        }
        SB0();
#pragma unroll
        for (int i = 0; i < 4; ++i) accq = MFMA32(as_frag(aq[g3 & 1][i]), as_frag(xf[4 * g3 + i]), accq);
        SB0();
      }
      const int pq = (int)OFF_PQ + mt * 36864;
#pragma unroll
      for (int e = 0; e < 2; ++e) {
        u32x4 o;
#pragma unroll
        for (int k = 0; k < 4; ++k) o[k] = pk2(phi(accq[8 * e + 2 * k]), phi(accq[8 * e + 2 * k + 1]));
        WS_ST16(P, pq + (2 * hp + e) * 1024 + lane * 16, o, 16);
      }
      if (h == 0) WS_ST8(P, pq + 32768 + hp * 256 + r * 8, qrem, 16);
      WAITV(0);
    }
    BAR();
    if (FJ == 1 && mi == 0) {
      constexpr int NSH = 256 / G1R;
      if (lane < G1R) __hip_atomic_fetch_add((unsigned*)(P.ws + OFF_G1 + (lane * NSH + (b % NSH)) * 128), 1u, __ATOMIC_RELAXED, __HIP_MEMORY_SCOPE_AGENT);
      poll_counters(P.ws, (int)OFF_G1 + ((b / NSH) * NSH + (lane % NSH)) * 128, (unsigned)G1R);
    }
    if constexpr (FJ == 0) { __builtin_amdgcn_s_sleep(4); prefetch2(); }
    BAR();
    const int e = r < 16 ? r : (r == 16 ? 16 : (r == 20 ? 17 : 18));
    const bool rok = r <= 17 || r == 20 || r == 21;
    u32x4 k0[4], k1[4], qf[4]; u32x2 k2[4];
    dma16x3s(P.ws + OFF_KV + wid * 6400, voff, lds_a + wid * 6144);
    dma16x3s(P.ws + OFF_KV + wid * 6400 + 3072, voff, lds_a + wid * 6144 + 3072);
    const u32x4 ktail = __builtin_amdgcn_raw_buffer_load_b128(wsr, (lane & 15) * 16, (int)OFF_KV + wid * 6400 + 6144, 16);
#pragma unroll
    for (int i = 0; i < 4; ++i) qf[i] = __builtin_amdgcn_raw_buffer_load_b128(wsr, voff, (int)OFF_PQ + mt2 * 36864 + (4 * wid + i) * 1024, 16);
    const u32x2 qr0 = __builtin_amdgcn_raw_buffer_load_b64(wsr, r * 8, (int)OFF_PQ + mt2 * 36864 + 32768 + wid * 512, 16);
    const u32x2 qr1 = __builtin_amdgcn_raw_buffer_load_b64(wsr, r * 8 + 256, (int)OFF_PQ + mt2 * 36864 + 32768 + wid * 512, 16);
    const u32x4 qr = {qr0[0], qr0[1], qr1[0], qr1[1]};
    *(lds_u32x4*)(lds + K2_RED + wid * 256 + (lane & 15) * 16) = ktail;
    asm volatile("" ::: "memory");
    SB0();
#pragma unroll
    for (int i = 0; i < 4; ++i) {
      const int o = i * 1600 + e * 80;
      lds_char* kp0 = lds + (o + 16 * h < 6144 ? wid * 6144 + o + 16 * h : K2_RED + wid * 256 + o + 16 * h - 6144);
      lds_char* kp1 = lds + (o + 16 * h + 32 < 6144 ? wid * 6144 + o + 16 * h + 32 : K2_RED + wid * 256 + o + 16 * h + 32 - 6144);
      lds_char* kp2 = lds + (o + 64 < 6144 ? wid * 6144 + o + 64 : K2_RED + wid * 256 + o + 64 - 6144);
      k0[i] = *(lds_u32x4*)kp0; k1[i] = *(lds_u32x4*)kp1; k2[i] = *(lds_u32x2*)kp2;
    }
    float orem[4];
#define F_(x) __uint_as_float(x)
#pragma unroll
    for (int i = 0; i < 4; ++i) {
      u32x4 a1 = {pk2(F_(k0[i][0]), F_(k0[i][1])), pk2(F_(k0[i][2]), F_(k0[i][3])), pk2(F_(k1[i][0]), F_(k1[i][1])), pk2(F_(k1[i][2]), F_(k1[i][3]))};
      u32x4 a2 = {0u, 0u, 0u, 0u};
      a2[i] = (h == (mi & 1)) ? pk2(F_(k2[i][0]), F_(k2[i][1])) : 0u;
      if (!rok) { a1 = (u32x4){0u, 0u, 0u, 0u}; a2[i] = 0u; }
      f32x16 cc;
#pragma unroll
      for (int j = 0; j < 16; ++j) cc[j] = 0.f;
      cc = MFMA32(as_frag(a1), as_frag(qf[i]), cc);
      cc = MFMA32(as_frag(a2), as_frag(qr), cc);
      const float inv = __builtin_amdgcn_rcpf(cc[9] + 1e-6f);
      float o[9];
#pragma unroll
      for (int j = 0; j < 9; ++j) o[j] = cc[j] * inv;
      u32x4 of = {pk2(o[0], o[1]), pk2(o[2], o[3]), pk2(o[4], o[5]), pk2(o[6], o[7])};
      *(lds_u32x4*)(lds + K2_O + (4 * wid + i) * 1024 + lane * 16) = of;
      orem[i] = o[8];
    }
#undef F_
    {
      u32x2 o2 = {pk2(orem[0], orem[1]), pk2(orem[2], orem[3])};
      *(lds_u32x2*)(lds + K2_O + (32 + (wid >> 1)) * 1024 + lane * 16 + 8 * (mi & 1)) = o2;
    }
    WAITL0();
    BAR();
    f32x16 acc, acc4;
#pragma unroll
    for (int j = 0; j < 16; ++j) { acc[j] = 0.f; acc4[j] = 0.f; }
    lds_char* ob = lds + K2_O + lane * 16;
#pragma unroll
    for (int i = 0; i < n4; ++i) acc4 = MFMA32(as_frag(a4[i]), as_frag(*(lds_u32x4*)(ob + (k4 + i) * 1024)), acc4);
#pragma unroll
    for (int i = 0; i < 18; ++i) acc = MFMA32(as_frag(af[i]), as_frag(*(lds_u32x4*)(ob + (18 * FJ + i) * 1024)), acc);
    {
      *(lds_float*)(lds + K2_MEAN + (wid * 64 + lane) * 4) = acc4[8] + acc4[9];
      lds_char* p4 = lds + K2_RED + 16384 + wid * 2048 + lane * 16;
      *(lds_f32x4*)(p4) = (f32x4){acc4[0], acc4[1], acc4[2], acc4[3]};
      *(lds_f32x4*)(p4 + 1024) = (f32x4){acc4[4], acc4[5], acc4[6], acc4[7]};
      if (FJ == 1) {
        lds_char* pr = lds + K2_RED + mi * 4096 + lane * 16;
#pragma unroll
        for (int k = 0; k < 4; ++k) *(lds_f32x4*)(pr + k * 1024) = (f32x4){acc[4 * k], acc[4 * k + 1], acc[4 * k + 2], acc[4 * k + 3]};
      }
    }
    WAITL0();
    BAR();
    if (FJ == 0 || mi == 0) {
#pragma unroll
      for (int w = 0; w < 8; ++w) mean += *(lds_float*)(lds + K2_MEAN + (w * 64 + r) * 4);
      if constexpr (FJ == 0) {
        lds_char* pr = lds + K2_RED + mi * 4096 + lane * 16;
#pragma unroll
        for (int k = 0; k < 4; ++k) {
          f32x4 v = *(lds_f32x4*)(pr + k * 1024);
#pragma unroll
          for (int j = 0; j < 4; ++j) acc[4 * k + j] += v[j];
        }
      } else {
#pragma unroll
        for (int j = 0; j < 8; ++j) acc[j] = 0.f;
#pragma unroll
        for (int w = 0; w < 8; ++w) {
          lds_char* p4 = lds + K2_RED + 16384 + w * 2048 + lane * 16;
          f32x4 v0 = *(lds_f32x4*)(p4), v1 = *(lds_f32x4*)(p4 + 1024);
#pragma unroll
          for (int j = 0; j < 4; ++j) { acc[j] += v0[j]; acc[4 + j] += v1[j]; }
        }
      }
      float ssq = 0.f;
#pragma unroll
      for (int i = 0; i < (FJ == 0 ? 2 : 1); ++i) {
        float v[8];
#pragma unroll
        for (int j = 0; j < 4; ++j) {
          auto sw2 = __builtin_amdgcn_permlane32_swap(__float_as_uint(acc[8 * i + j]), __float_as_uint(acc[8 * i + 4 + j]), false, false);
          v[j] = __uint_as_float(sw2[0]); v[4 + j] = __uint_as_float(sw2[1]);
        }
        const u32x4 xv = i ? xv1 : xv0;
        u32x4 o;
#pragma unroll
        for (int k = 0; k < 4; ++k) {
          const float a0 = v[2 * k] + bflo(xv[k]) - mean, a1 = v[2 * k + 1] + bfhi(xv[k]) - mean;
          ssq += a0 * a0 + a1 * a1;
          o[k] = pk2(a0, a1);
        }
        const int d8 = 2 * (s0 + i) + h;
        WS_ST16(P, OFF_HC + (((row >> 4) * 18 + (d8 >> 2)) * 64 + (row & 15) + 16 * (d8 & 3)) * 16, o, 16);
      }
      ssq += __shfl_xor(ssq, 32);
      if (h == 0) {
        WS_ST4(P, OFF_SSQ + ((mt2 * 20 + cb * 5 + wid) * 32 + r) * 4, __float_as_uint(ssq), 16);
        if (cb == 0 && wid == 0) WS_ST4(P, OFF_MEANH + row * 4, __float_as_uint(mean), 16);
      }
    }
    WAITV(0); WAITL0();
    BAR();
    if (FJ == 1 && mi == 1 && lane == 0) __hip_atomic_fetch_add((unsigned*)(P.ws + OFF_G3 + mb * 128), 1u, __ATOMIC_RELAXED, __HIP_MEMORY_SCOPE_AGENT);
    first_slot(std::integral_constant<int, 0>{});
    first_slot(std::integral_constant<int, 1>{});
    if (FJ == 1 && mi == 1) poll_counters(P.ws, (int)OFF_G3 + mb * 128, 16u);
    BAR();
  }
  const char* hsrc = P.ws + OFF_HC + (size_t)mt * 36864;
  const char* hmine = hsrc + fj * 18432;
  constexpr int ST0 = K3_RING + 2 * 24576, ST1 = K3_HH, ST2 = K3_HH + 24576;
  dma16x3s(hmine, voff, lds_a + ST0 + (mi * 2 + fj) * 3072);
  dma16x3s(hmine + 3072, voff, lds_a + ST1 + (mi * 2 + fj) * 3072);
  dma16x3s(hmine + 6144, voff, lds_a + ST2 + (mi * 2 + fj) * 6144);
  dma16x3s(hmine + 9216, voff, lds_a + ST2 + (mi * 2 + fj) * 6144 + 3072);
  u32x4 hf[2][18];
  WAITV(9);
  BAR();
  const int q = (lane >> 2) & 3, pp = lane & 3, xg = ((q >> 1) & 1) | ((g & 1) << 1);
  lds_char* rd_g0 = lds + K3_RING + (8 * g + q) * 128 + ((fj ^ xg) * 32) + 8 * pp;
  lds_char* rd_g1 = lds + K3_RING + (8 * g + q) * 128 + (((fj + 2) ^ xg) * 32) + 8 * pp;
  lds_char* hh_rd = lds + K3_HH + mi * 18432 + lane * 16;
  const unsigned vgu = vgu_f();
  u32x4 a[6];
  auto read_frag = [&](auto nc) __attribute__((always_inline)) {
    constexpr int n = decltype(nc)::value;
    if constexpr (n < NS) {
      constexpr int o = SCH.lo[n] * 1024;
      if constexpr (n < PH) { lds_char* r = ((n & 1) ? rd_g1 : rd_g0) + o; a[n % 6] = tr_frag(r, r + 512); }
      else if constexpr (n < P2) { lds_char* r = lds + K3_RING + (8 * g + q) * 64 + ((fj ^ (g & 1)) * 32) + 8 * pp + o; a[n % 6] = tr_frag(r, r + 256); }
      else {
        const int gr = SCH.db[n - P2] == 4 ? 2 * p + fj : 2 * fj + SCH.t01[n - P2];
        lds_char* r = lds + K3_RING + (4 * g + q) * 128 + ((gr ^ xg) * 32) + 8 * pp + o;
        a[n % 6] = tr_frag(r, r + 2048);
      }
    }
  };
#pragma unroll
  for (int i = 0; i < 6; ++i) hf[i & 1][i >> 1] = *(lds_u32x4*)(lds + ST0 + (mi * 2 + (i & 1)) * 3072 + (i >> 1) * 1024 + lane * 16);
  static_for<0, 3>(read_frag);
  WAITL0();
  BAR();
  first_slot(std::integral_constant<int, 2>{});
  __builtin_amdgcn_s_setprio(FJ == 0 ? 1 : 0);
  const int fl_mine = (int)OFF_FLAG + b * 128;
  const char* fl_other = P.ws + OFF_FLAG + (b ^ 8) * 128;
  const int xh_off = (int)OFF_XH + (b * 4 + mi) * 18432;
  const srd_t sx = make_srd(P.ws + OFF_XH + (size_t)((b ^ 8) * 4 + mi) * 18432, 18432);
  const int pbase = (int)OFF_XH + (b ^ 8) * 73728 + mi * 18432;
  lds_char* hh_wr = hh_rd + 8 * fj;
  float sigma[2], gsc[2];
  float sq[5];
  f32x4 acc[2][2][2][2];
  f32x4 oc[9][2];
  u32x4 ov[2], ho[2][2], hp[9][2], xl[2];
  u32x2 hpk;
  u32x2 ovs[2];
  unsigned pollv;

  auto epi = [&](auto gc, auto ic) __attribute__((always_inline)) {
    constexpr int G = decltype(gc)::value, i = decltype(ic)::value;
    if constexpr (i < (G == 4 ? 46 : 92)) {
      constexpr int uu = i / 46, mh = (i % 46) / 23, r = i % 23, set = G & 1, U = 2 * G + uu;
      if constexpr (r < 20) {
        constexpr int e = (r / 10) * 2 + (r & 1), ph = (r % 10) >> 1;
        float tv = acc[set][uu][0][mh][e];
        if (ph == 0) tv = tv * gsc[mh];
        if (ph == 1) tv = __builtin_amdgcn_exp2f(tv);
        if (ph == 2) tv = __builtin_fmaf(tv, sigma[mh], sigma[mh]);
        if (ph == 3) tv = __builtin_amdgcn_rcpf(tv);
        if (ph == 4) tv = tv * acc[set][uu][1][mh][e];
        asm volatile("" : "+v"(tv));
        acc[set][uu][0][mh][e] = tv;
      } else if constexpr (r < 22) {
        unsigned t = pk2(acc[set][uu][0][mh][2 * (r - 20)], acc[set][uu][0][mh][2 * (r - 20) + 1]);
        asm volatile("" : "+v"(t));
        hpk[r - 20] = t;
      } else {
        *(lds_u32x2*)(hh_wr + mh * 9216 + U * 1024) = hpk;
      }
      SB0();
    }
  };
  auto opack = [&](const int pr, const int i) __attribute__((always_inline)) {
    const int mh = i >> 2, r = i & 3;
    if (pr == 4 && r >= 2) return;
    const int ti = pr == 4 ? 8 : 2 * pr + (r >> 1), e = pr == 4 ? 2 * r : 2 * (r & 1);
    unsigned t = pk2(oc[ti][mh][e], oc[ti][mh][e + 1]);
    asm volatile("" : "+v"(t));
    if (pr == 4) ovs[mh][r & 1] = t; else ov[mh][r] = t;
    SB0();
  };
  auto ostore = [&](const int x) __attribute__((always_inline)) {
    const int pr = x >> 1, mh = x & 1;
    if (pr < 4) st128(ov[mh], wsr, voff, pbase + (p + 2 * pr + fj + 9 * mh) * 1024);
    else __builtin_amdgcn_raw_buffer_store_b64(ovs[mh], wsr, voff >> 1, pbase + ((p ? 0 : 8) + 9 * mh) * 1024 + 512 * fj, 16);
    SB0();
  };

  static_for<0, NS>([&](auto nc) __attribute__((always_inline)) {
    constexpr int n = decltype(nc)::value;
    constexpr bool bar = SCH.bar[n];
    if constexpr (n == 9) WAITV(12);
    else if constexpr (FJ == 0 && SCH.wl[n] >= 0) waitv<SCH.wl[n]>();
    else if constexpr (FJ == 1 && SCH.wf[n] >= 0) waitv<SCH.wf[n]>();
    if constexpr (FJ == 1 && SCH.chk[n] >= 0) {
      asm volatile("" : "+v"(pollv));
      if (__builtin_amdgcn_readfirstlane(pollv) < (unsigned)SCH.chk[n]) {
#pragma unroll 1
        for (int spin = 0; spin < (1 << 22); ++spin) {
          asm volatile("global_load_dword %0, %1, %2 sc1\n\ts_waitcnt vmcnt(0)" : "=&v"(pollv) : "v"(0u), "s"(fl_other) : "memory");
          if (__builtin_amdgcn_readfirstlane(pollv) >= (unsigned)SCH.chk[n]) break;
          __builtin_amdgcn_s_sleep(2);
        }
      }
    }
    if constexpr (bar) {
      if constexpr (SCH.l2[n]) asm volatile("s_waitcnt lgkmcnt(2)" ::: "memory"); else WAITL0();
      BAR();
    }
    else if constexpr (n == SCH.ebar) BAR();
    const u32x4 af = a[n % 6];
    SB0();
    static_for<0, 2>([&](auto mc) __attribute__((always_inline)) {
      constexpr int mh = decltype(mc)::value;
      if constexpr (n < P2) {
        constexpr int y = n % 72;
        constexpr int set = (n / 72) & 1, S = n >= PH ? y >> 1 : y >> 2, mat = n >= PH ? y & 1 : (y >> 1) & 1, uu = n >= PH ? 0 : y & 1;
        if constexpr (S == 0) asm volatile("v_mfma_f32_16x16x32_bf16 %0, %1, %2, 0" : "=&v"(acc[set][uu][mat][mh]) : "v"(af), "v"(hf[mh][S]));
        else asm volatile("v_mfma_f32_16x16x32_bf16 %0, %1, %2, %0" : "+v"(acc[set][uu][mat][mh]) : "v"(af), "v"(hf[mh][S]));
      } else {
        constexpr int s2 = n - P2, U = SCH.u[s2], ti = SCH.db[s2] == 4 ? 8 : 2 * SCH.db[s2] + SCH.t01[s2];
        if constexpr (s2 < 9) asm volatile("v_mfma_f32_16x16x32_bf16 %0, %1, %2, 0" : "=&v"(oc[ti][mh]) : "v"(af), "v"(ho[0][mh]));
        else if constexpr (SCH.own[s2]) asm volatile("v_mfma_f32_16x16x32_bf16 %0, %1, %2, %0" : "+v"(oc[ti][mh]) : "v"(af), "v"(ho[U & 1][mh]));
        else asm volatile("v_mfma_f32_16x16x32_bf16 %0, %1, %2, %0" : "+v"(oc[ti][mh]) : "v"(af), "v"(hp[U][mh]));
      }
      SB0();
      if constexpr (mh == 0) { read_frag(std::integral_constant<int, n + 3>{}); SB0(); }
      if constexpr (SCH.eg[n] >= 0) {
        constexpr int f = SCH.e0[n] + (mh ? SCH.ne0[n] : 0), cnt = mh ? SCH.ne1[n] : SCH.ne0[n];
        static_for<f, f + cnt>([&](auto ic) __attribute__((always_inline)) { epi(std::integral_constant<int, SCH.eg[n]>{}, ic); });
      }
      if constexpr (SCH.og[n] >= 0) opack(SCH.og[n], SCH.o0[n] + mh);
    });
    if constexpr (FJ == 0 && SCH.dslot[n] >= 0) {
      constexpr int t = SCH.dslot[n], k = SCH.dk[n];
      if constexpr (t < 24) {
        bdma_i<(t % 3) * 24576 + k * 4096, 32 * (3 * (t % 6) + (k >> 1)) * (F_ * 2) + (t / 6) * 128>((k & 1) ? su : sg, vgu, sgu, ring_w);
      } else if constexpr (t < 27) {
        const unsigned vhalf = (lane >> 2) * (F_ * 2) + ((((lane >> 1) & 1) ^ ((lane >> 5) & 1)) * 32) + (lane & 1) * 16;
        bdma_i<(t % 3) * 24576 + k * 4096, 32 * (6 * (t - 24) + k) * (F_ * 2)>((mi & 1) ? su : sg, vhalf, 16 * (mi >> 1) * (F_ * 2) + 576 * c + (p ? 0 : 512),
                                                                          lds_a + K3_RING + (mi & 1) * 2048 + (mi >> 1) * 1024);
      } else {
        constexpr int sb = (t - 27) * 6 + k, ow = SCH.sown[sb], U = SCH.su[sb], d = SCH.sdb[sb];
        const unsigned vd = (lane >> 3) * (D_ * 2) + ((((lane >> 1) & 3) ^ ((lane >> 4) & 3)) * 32) + (lane & 1) * 16;
        const int cc = ow ? c : c ^ 1;
        bdma_i<(t % 3) * 24576 + k * 4096, (U == 8 ? 0 : 32 * U * (D_ * 2)) + (d == 4 ? 512 : 128 * d)>(
            sd, vd, (288 * cc + 8 * mi + (U == 8 ? ((cc & 1) ? 0 : 256) : ((cc & 1) ? 32 : 0))) * (D_ * 2) + (d == 4 ? 0 : 640 * p), ring_w);
      }
      SB0();
    }
    if constexpr (SCH.ost[n] >= 0) ostore(SCH.ost[n]);
    if constexpr (n == 5) { dma16x3s(P.ws + OFF_SSQ + mt * 2560, voff, lds_a + K3_SSQ + mi * 3072); SB0(); }
    if constexpr (n == 28) { dma16x3s(hmine + 12288, voff, lds_a + ST1 + (mi * 2 + fj) * 3072); SB0(); }
    if constexpr (n == 40) { dma16x3s(hmine + 15360, voff, lds_a + ST2 + (mi * 2 + fj) * 3072); SB0(); }
    if constexpr (n == 9 || n == 45 || n == 57) {
      constexpr int s0 = n == 9 ? 3 : (n == 45 ? 12 : 15), stg = n == 57 ? ST2 : ST1;
#pragma unroll
      for (int i = 0; i < 6; ++i) hf[i & 1][s0 + (i >> 1)] = *(lds_u32x4*)(lds + stg + (mi * 2 + (i & 1)) * 3072 + (i >> 1) * 1024 + lane * 16);
      SB0();
    }
    if constexpr (n == 21) {
#pragma unroll
      for (int i = 0; i < 12; ++i) hf[i & 1][6 + (i >> 1)] = *(lds_u32x4*)(lds + ST2 + (mi * 2 + (i & 1)) * 6144 + (i >> 1) * 1024 + lane * 16);
      SB0();
    }
    if constexpr (n == 48 || n == 60) {
      constexpr int mh = n == 60;
#pragma unroll
      for (int i = 0; i < 5; ++i) sq[i] = *(lds_float*)(lds + K3_SSQ + mi * 3072 + ((5 * g + i) * 32 + mh * 16 + (lane & 15)) * 4);
      SB0();
    }
    if constexpr (n == 52 || n == 64) {
      constexpr int mh = n == 64;
      const float t0 = (sq[0] + sq[1]) + (sq[2] + sq[3]) + sq[4];
      const auto t1 = __builtin_amdgcn_permlane16_swap(__float_as_uint(t0), __float_as_uint(t0), false, false);
      const float t2 = __uint_as_float(t1[0]) + __uint_as_float(t1[1]);
      const auto t3 = __builtin_amdgcn_permlane32_swap(__float_as_uint(t2), __float_as_uint(t2), false, false);
      const float var = (__uint_as_float(t3[0]) + __uint_as_float(t3[1])) * (1.f / D_) + LN_EPS;
      const float rs = __builtin_amdgcn_rsqf(var);
      sigma[mh] = var * rs;
      gsc[mh] = -1.4426950408889634f * rs;
      asm volatile("" : "+v"(sigma[mh]), "+v"(gsc[mh]));
      SB0();
    }
    if constexpr (n == P2 - 1) { ho[0][0] = *(lds_u32x4*)(hh_rd); ho[0][1] = *(lds_u32x4*)(hh_rd + 9216); SB0(); }
    if constexpr (n >= P2 && n < P2 + 72 && (n - P2) % 9 == 4) {
      constexpr int U = (n - P2) / 9 + 1;
      ho[U & 1][0] = *(lds_u32x4*)(hh_rd + U * 1024); ho[U & 1][1] = *(lds_u32x4*)(hh_rd + 9216 + U * 1024);
      SB0();
    }
    if constexpr (FJ == 1) {
      if constexpr (SCH.xst[n] >= 0) {
        st128(xl[0], wsr, voff, xh_off + SCH.xst[n] * 1024);
        st128(xl[1], wsr, voff, xh_off + 9216 + SCH.xst[n] * 1024);
        SB0();
      }
      if constexpr (SCH.flag[n] >= 0) { __builtin_amdgcn_raw_buffer_store_b32((unsigned)SCH.flag[n], wsr, 0, fl_mine, 16); SB0(); }
      if constexpr (SCH.poll[n]) { asm volatile("global_load_dword %0, %1, %2 sc1" : "=&v"(pollv) : "v"(0u), "s"(fl_other) : "memory"); SB0(); }
      if constexpr (SCH.ld[n] >= 0) {
        constexpr int u = SCH.ld[n];
        bdma_sc1<u * 1024>(sx, voff, lds_a + K3_HH + mi * 18432);
        bdma_sc1<9216 + u * 1024>(sx, voff, lds_a + K3_HH + mi * 18432);
        SB0();
      }
      if constexpr (n == SCH.ebar) {
        if (mi == 0 && lane == 0) __hip_atomic_fetch_add((unsigned*)(P.ws + OFF_CNT + (mb * 2 + p) * 128), 1u, __ATOMIC_RELAXED, __HIP_MEMORY_SCOPE_AGENT);
        SB0();
      }
      if constexpr (SCH.pf[n] == 1 || SCH.pf[n] == 2) { pf_gu(mi, SCH.pf[n] == 1 || p ? 256 : 512); SB0(); }
      if constexpr (SCH.pf[n] == 3) {
        const int l41 = lane < 41 ? lane : 0, o = (((mb & 7) * 2 + p) * 4 + mi) * 10368 + l41 * 256;
        const char* pa = P.wd + (size_t)(c >> 1) * 663552 + (o < 663552 - 256 ? o : 663552 - 256);
        touch2(pa, lds_a + K3_DUMP);
        SB0();
      }
      if constexpr (SCH.xrd[n] >= 0) {
        xl[0] = *(lds_u32x4*)(hh_rd + SCH.xrd[n] * 1024); xl[1] = *(lds_u32x4*)(hh_rd + 9216 + SCH.xrd[n] * 1024);
        SB0();
      }
    }
    if constexpr (SCH.rd[n] >= 0) {
      constexpr int u = SCH.rd[n];
      hp[u][0] = *(lds_u32x4*)(hh_rd + u * 1024); hp[u][1] = *(lds_u32x4*)(hh_rd + 9216 + u * 1024);
      SB0();
    }
  });
  MFMA_DRAIN();
#pragma unroll
  for (int i = 0; i < 8; ++i) opack(3, i);
  ostore(6);
  ostore(7);
  unsigned vo2 = voff;
  asm volatile("" : "+v"(vo2));
  const int ln = vo2 >> 4, gl = ln >> 4;
  const int j = c >> 1, t16 = mb * 8 + j, f0 = t16 * 18 + 9 * p;
  const int qr = p + wid, qs = p ? 0 : 8;
  const int l16 = 16 * mi + (ln & 15);
  const int row = t16 * 16 + (ln & 15);
  const int hb = ((ln & 15) + 16 * (gl >> 1)) * 16 + 8 * (gl & 1), hbs = ((ln & 15) + 16 * (mi >> 1)) * 16 + 8 * (mi & 1);
  const u32x2 h0 = __builtin_amdgcn_raw_buffer_load_b64(wsr, hb, (int)OFF_HC + (f0 + qr) * 1024, 16);
  const u32x2 h1 = __builtin_amdgcn_raw_buffer_load_b64(wsr, hb + 512, (int)OFF_HC + (f0 + qr) * 1024, 16);
  u32x2 hs0, hs1;
  if constexpr (FJ == 0) {
    hs0 = __builtin_amdgcn_raw_buffer_load_b64(wsr, hbs, (int)OFF_HC + (f0 + qs) * 1024, 16);
    hs1 = __builtin_amdgcn_raw_buffer_load_b64(wsr, hbs + 512, (int)OFF_HC + (f0 + qs) * 1024, 16);
  }
  const float mean = __uint_as_float(__builtin_amdgcn_raw_buffer_load_b32(wsr, row * 4, (int)OFF_MEANH, 16));
  const int cnt_off = (int)OFF_CNT + (mb * 2 + p) * 128;
  if constexpr (FJ == 0) poll_counters(P.ws, cnt_off, 8u);
  else WAITV(0);
  BAR();
  if constexpr (FJ == 1) {
    if (mi == 0 && ln == 0) __hip_atomic_fetch_add((unsigned*)(P.ws + cnt_off + 64), 1u, __ATOMIC_RELAXED, __HIP_MEMORY_SCOPE_AGENT);
    poll_counters(P.ws, cnt_off + 64, 8u);
  }
  auto area = [&](const int k) { const int ck = 2 * k + (p ^ 1); return (int)OFF_XH + (((mb & 7) * 4 + (ck & 3)) * 8 + (ck >> 2) * 2 + (mb >> 3)) * 73728 + j * 9216; };
  u32x4 pv[8], ps[2];
#pragma unroll
  for (int k = 0; k < 8; ++k) pv[k] = __builtin_amdgcn_raw_buffer_load_b128(wsr, voff, area(k) + qr * 1024, 16);
  if constexpr (FJ == 0) {
#pragma unroll
    for (int k = 0; k < 2; ++k) {
      const u32x2 lo = __builtin_amdgcn_raw_buffer_load_b64(wsr, area(4 * k + gl) + l16 * 8, qs * 1024, 16);
      const u32x2 hi = __builtin_amdgcn_raw_buffer_load_b64(wsr, area(4 * k + gl) + l16 * 8 + 512, qs * 1024, 16);
      ps[k] = (u32x4){lo[0], lo[1], hi[0], hi[1]};
    }
  }
  float o[8];
  o[0] = bflo(h0[0]); o[1] = bfhi(h0[0]); o[2] = bflo(h0[1]); o[3] = bfhi(h0[1]);
  o[4] = bflo(h1[0]); o[5] = bfhi(h1[0]); o[6] = bflo(h1[1]); o[7] = bfhi(h1[1]);
#pragma unroll
  for (int k = 0; k < 8; ++k) o[k] += mean;
#pragma unroll
  for (int r = 0; r < 8; ++r)
#pragma unroll
    for (int k = 0; k < 4; ++k) { o[2 * k] += bflo(pv[r][k]); o[2 * k + 1] += bfhi(pv[r][k]); }
  {
    char* d = P.out + (size_t)row * 1152 + (9 * p + qr) * 64 + gl * 8;
    *(u32x2*)d = (u32x2){pk2(o[0], o[1]), pk2(o[2], o[3])};
    *(u32x2*)(d + 32) = (u32x2){pk2(o[4], o[5]), pk2(o[6], o[7])};
  }
  if constexpr (FJ == 0) {
    float os[8];
#pragma unroll
    for (int k = 0; k < 4; ++k) { os[2 * k] = bflo(ps[0][k]) + bflo(ps[1][k]); os[2 * k + 1] = bfhi(ps[0][k]) + bfhi(ps[1][k]); }
#pragma unroll
    for (int k = 0; k < 8; ++k) { os[k] += __shfl_xor(os[k], 16); os[k] += __shfl_xor(os[k], 32); }
    if (ln < 16) {
      char* d = P.out + (size_t)row * 1152 + (9 * p + qs) * 64 + mi * 8;
      *(u32x2*)d = (u32x2){pk2(os[0] + bflo(hs0[0]) + mean, os[1] + bfhi(hs0[0]) + mean), pk2(os[2] + bflo(hs0[1]) + mean, os[3] + bfhi(hs0[1]) + mean)};
      *(u32x2*)(d + 32) = (u32x2){pk2(os[4] + bflo(hs1[0]) + mean, os[5] + bfhi(hs1[0]) + mean), pk2(os[6] + bflo(hs1[1]) + mean, os[7] + bfhi(hs1[1]) + mean)};
    }
  }
}

__global__ __launch_bounds__(512, 2) void k3_ffn(Ptrs P) {
  extern __shared__ __attribute__((aligned(16))) char smem[];
  const int lane = threadIdx.x & 63, wid = __builtin_amdgcn_readfirstlane(threadIdx.x >> 6);
  const int b = blockIdx.x;
  const int c = ((b & 7) >> 1) * 4 + ((b >> 3) & 3), mb = (b & 1) * 8 + (b >> 5);
  if (wid < 4) k3_body<0>(P, (lds_char*)smem, lane, wid, b, c, mb);
  else k3_body<1>(P, (lds_char*)smem, lane, wid - 4, b, c, mb);
}

constexpr int K0_IMG = 4608;
constexpr int K0_RED = 3 * K0_IMG;
constexpr int K0_DUMP = K0_RED + 9 * 8 * 8;
constexpr int K0_LDS = K0_DUMP + 256;

DI float dpp_sum8(float v) {
  v += __shfl_xor(v, 1); v += __shfl_xor(v, 2); v += __shfl_xor(v, 4);
  return v;
}

__global__ __launch_bounds__(768) void k0_prep(Ptrs P) {
  extern __shared__ __attribute__((aligned(16))) char smem[];
  lds_char* lds = (lds_char*)smem;
  const int lane = threadIdx.x & 63, wid = __builtin_amdgcn_readfirstlane(threadIdx.x >> 6);
  const int b = blockIdx.x;
  if (wid < 9) {
    const int row = b * 8 + (lane >> 3), i = lane & 7;
    const u32x4 v = *(const u32x4*)(P.x + (size_t)row * 1152 + wid * 128 + i * 16);
    float e[8];
#pragma unroll
    for (int k = 0; k < 4; ++k) { e[2 * k] = bflo(v[k]); e[2 * k + 1] = bfhi(v[k]); }
    const float sum = dpp_sum8(((e[0] + e[1]) + (e[2] + e[3])) + ((e[4] + e[5]) + (e[6] + e[7])));
    const float lm = sum * (1.f / 64.f);
    float m2 = 0.f;
#pragma unroll
    for (int k = 0; k < 8; ++k) { const float d = e[k] - lm; m2 = __builtin_fmaf(d, d, m2); }
    m2 = dpp_sum8(m2);
    if (i == 0) *(lds_f32x2*)(lds + K0_RED + (wid * 8 + (lane >> 3)) * 8) = (f32x2){lm, m2};
    WAITL0();
    BAR();
    f32x2 pr[9];
#pragma unroll
    for (int w = 0; w < 9; ++w) pr[w] = *(lds_f32x2*)(lds + K0_RED + (w * 8 + (lane >> 3)) * 8);
    float mean = 0.f;
#pragma unroll
    for (int w = 0; w < 9; ++w) mean += pr[w][0];
    mean *= (1.f / 9.f);
    float tot = 0.f;
#pragma unroll
    for (int w = 0; w < 9; ++w) { const float d = pr[w][0] - mean; tot += __builtin_fmaf(64.f * d, d, pr[w][1]); }
    const float rstd = rsqrtf(tot * (1.f / D_) + LN_EPS);
    u32x4 o;
#pragma unroll
    for (int k = 0; k < 4; ++k) o[k] = pk2((e[2 * k] - mean) * rstd, (e[2 * k + 1] - mean) * rstd);
    WS_ST16(P, (int)OFF_XN + ((row >> 5) * 36 * 64 + (row & 31) + 32 * (i & 1) + (4 * wid + (i >> 1)) * 64) * 16, o, ST_AUX);
    if (wid == 0 && i == 0) WS_ST4(P, OFF_MEANX + row * 4, __float_as_uint(mean), ST_AUX);
    return;
  }
  const int id = (wid - 9) * 256 + b;
  lds_char* img = lds + (wid - 9) * K0_IMG;
  const unsigned img_a = (unsigned)(uintptr_t)img;
  const int g = lane >> 4, q = (lane >> 2) & 3, p = lane & 3;
  if (wid == 9) {
    constexpr unsigned NW = (D_ * F_ * 2 + TSTRIDE - 1) / TSTRIDE, NS_ = (unsigned)((WS_END + TSTRIDE - 1) / TSTRIDE), NO = (S_ * D_ * 2 + TSTRIDE - 1) / TSTRIDE;
    constexpr unsigned NT = 3 * NW + NS_ + NO;
    for (unsigned t = (b >> 3) * 64 + lane; t < ((NT + 2047) / 2048) * 2048; t += 2048) {
      const unsigned u = t < NT ? t : 0;
      const char* pp = u < NW ? P.wg + (size_t)u * TSTRIDE : u < 2 * NW ? P.wu + (size_t)(u - NW) * TSTRIDE : u < 3 * NW ? P.wd + (size_t)(u - 2 * NW) * TSTRIDE
                     : u < 3 * NW + NS_ ? P.ws + (size_t)(u - 3 * NW) * TSTRIDE : P.out + (size_t)(u - 3 * NW - NS_) * TSTRIDE;
      touch1(pp, (unsigned)(uintptr_t)lds + K0_DUMP);
    }
  }
  if (wid == 10 && b < 96) {
    unsigned long long pc, k1a;
    asm volatile("s_getpc_b64 %0" : "=s"(pc));
    asm volatile("s_getpc_b64 s[40:41]\n\ts_add_u32 s40, s40, _Z6k3_ffn4Ptrs@rel32@lo+4\n\ts_addc_u32 s41, s41, _Z6k3_ffn4Ptrs@rel32@hi+12\n\t"
                 "s_mov_b64 %0, s[40:41]" : "=s"(k1a) :: "s40", "s41", "scc");
    const char* pp = (const char*)k1a + ((b >> 3) * 64 + lane) * 128;
    pp = pp < (const char*)pc ? pp : (const char*)k1a;
    touch1(pp, (unsigned)(uintptr_t)lds + K0_DUMP);
  }
  if (id < 576) {
    const int s = id >> 4, h = (id >> 3) & 1, hp0 = (id & 7) * 2;
    const char* src = P.wqkv + (size_t)(16 * s + 8 * h) * 3456 + hp0 * 72;
#pragma unroll
    for (int it = 0; it < 4; ++it) {
      int e = it * 64 + lane; e = e < 216 ? e : 215;
      const int sgm = e / 9, ch = e - sgm * 9;
      dma16x1(src, sgm * 1152 + ch * 16, img_a + it * 1024);
    }
    BAR();
    WAITV(0);
#pragma unroll
    for (int it = 0; it < 4; ++it) {
      int Q = 16 * it + 4 * g + p; Q = Q < 54 ? Q : 53;
      const int hl = Q >= 27 ? 1 : 0, sg = Q - 27 * hl;
      const int cb = sg < 4 ? 8 * sg : (sg < 8 ? 8 * sg + 8 : (sg == 8 ? 32 : (sg < 18 ? 8 * sg + 72 : 8 * sg + 144)));
      lds_char* a = img + q * 432 + cb + 72 * hl;
      u32x4 v = tr_frag(a, a + 4 * 432);
      const int o = it * 64 + lane, ol = o >= 108 ? 1 : 0, slot = o - 108 * ol;
      const int off = slot < 32 ? WQT_Q + s * 1024 + (h * 32 + slot) * 16 : s * 2432 + (h * 76 + slot - 32) * 16;
      if (o < 216) WS_ST16(P, OFF_WQT + (hp0 + ol) * (int)WQT_HP + off, v, ST_AUX);
    }
  } else if (id < 720) {
    const int j = id - 576, ks = j >> 2, cb = j & 3;
#pragma unroll
    for (int it = 0; it < 5; ++it) {
      int e = it * 64 + lane; e = e < 288 ? e : 287;
      const int kk = e / 18, ch = e - kk * 18, h = kk >> 3, jj = kk & 7;
      const int row = ks < 32 ? 18 * ks + (jj & 3) + 8 * (jj >> 2) + 4 * h : 18 * (8 * (ks - 32) + jj) + 16 + h;
      if (it < 4 || lane < 32) dma16x1(P.wo + cb * 288, row * 1152 + ch * 16, img_a + it * 1024);
    }
    BAR();
    WAITV(0);
#pragma unroll
    for (int it = 0; it < 5; ++it) {
      int G16 = 4 * it + g; G16 = G16 < 18 ? G16 : 17;
      const int h = G16 >= 9 ? 1 : 0, sg = G16 - 9 * h;
      lds_char* a = img + (8 * h + q) * 288 + (16 * sg + 4 * p) * 2;
      u32x4 v = tr_frag(a, a + 4 * 288);
      if (it < 4 || lane < 32) WS_ST16(P, OFF_WOT + cb * (int)WOT_CB + ks * 4672 + (h * 146 + 16 * sg + (lane & 15)) * 16, v, ST_AUX);
    }
  } else if (id < 756) {
    const int row = (id - 720) * 16 + (lane >> 2), i = lane & 3;
    const char* src = P.wo + (size_t)row * 1152 + i * 16;
    u32x4 v[18];
#pragma unroll
    for (int j = 0; j < 18; ++j) v[j] = *(const u32x4*)(src + j * 64);
    BAR();
    float s4[4] = {0.f, 0.f, 0.f, 0.f};
    const bf16x2 one2 = __builtin_bit_cast(bf16x2, 0x3f803f80u);
#pragma unroll
    for (int j = 0; j < 18; ++j)
#pragma unroll
      for (int k = 0; k < 4; ++k) { const unsigned t = v[j][k]; s4[k] = __builtin_amdgcn_fdot2_f32_bf16(__builtin_bit_cast(bf16x2, t), one2, s4[k], false); }
    float sum = (s4[0] + s4[1]) + (s4[2] + s4[3]);
    sum += __shfl_xor(sum, 1); sum += __shfl_xor(sum, 2);
    const float mean = sum * (1.f / D_);
    const int hd = row / 18, e = row % 18;
    const int ks = e < 16 ? hd : 32 + (hd >> 3), jj = e < 16 ? (e & 3) + 4 * (e >> 3) : (hd & 7), hh = e < 16 ? (e >> 2) & 1 : e - 16;
    const unsigned hi = pk2(mean, 0.f) & 0xffffu;
    const unsigned lo = pk2(mean - bflo(hi), 0.f) & 0xffffu;
    unsigned short* d = (unsigned short*)(P.ws + OFF_WOT + (size_t)i * WOT_CB + ks * 4672 + (hh * 146 + 144) * 16 + jj * 2);
    d[0] = (unsigned short)hi; d[8] = (unsigned short)lo;
  } else {
    const u32x4 z = {0u, 0u, 0u, 0u};
    for (int k = id - 756; k < 50; k += 12) WS_ST16(P, OFF_KV + k * 1024 + lane * 16, z, ST_AUX);
    for (int k = id - 756; k < 70; k += 12) WS_ST16(P, OFF_FLAG + k * 1024 + lane * 16, z, ST_AUX);
    BAR();
  }
}

extern "C" void kernel_launch(void* const* d_in, const int* in_sizes, int n_in, void* d_out, int out_size, void* d_ws,
                              size_t ws_size, hipStream_t stream) {
  Ptrs P;
  P.x = (const char*)d_in[0]; P.wqkv = (const char*)d_in[1]; P.wo = (const char*)d_in[2];
  P.wg = (const char*)d_in[4]; P.wu = (const char*)d_in[5]; P.wd = (const char*)d_in[6];
  P.ws = (char*)d_ws; P.out = (char*)d_out;
  (void)hipFuncSetAttribute((const void*)k3_ffn, hipFuncAttributeMaxDynamicSharedMemorySize, K3_LDS);
  k0_prep<<<256, 768, K0_LDS, stream>>>(P);
  k3_ffn<<<256, 512, K3_LDS, stream>>>(P);
}
```
